```python
import torch

def _softmax(x, axis=-1):
    return torch.softmax(x, dim=axis)

def _gelu(x):
    return torch.nn.functional.gelu(x, approximate='tanh')

def _silu(x):
    return torch.nn.functional.silu(x)

def _sigmoid(x):
    return torch.sigmoid(x)

def _layernorm(x, axis=-1, eps=1e-05):
    m = x.mean(dim=axis, keepdim=True)
    v = x.var(dim=axis, keepdim=True, unbiased=False)
    return (x - m) / torch.sqrt(v + eps)

def _scatter_add(tgt, idx, src, axis=0):
    return tgt.clone().index_add_(axis, idx, src)

def _segment_sum(src, seg, n):
    out = torch.zeros((n,) + src.shape[1:], dtype=src.dtype)
    return out.index_add_(0, seg, src)

def _pad(x, pw):
    flat = tuple((v for pair in reversed(pw) for v in pair))
    return torch.nn.functional.pad(x, flat)
kernel_name = 'interleave_layers__sliding_local_dilated__mixer_gmlp__prompt'

def setup_inputs():

    _w = lambda a: (lambda t: t.to(torch.bfloat16) if t.is_floating_point() else t)(torch.as_tensor(a))

    def _A_setup():
        B, S, D = (1, 2048, 640)
        return {'x': _w((lambda *s: torch.randn(*s, dtype=torch.bfloat16) * 0.05)(B, S, D)), 'wqkv': _w((lambda *s: torch.randn(*s, dtype=torch.bfloat16) * 0.1)(D, 3 * D)), 'wo': _w((lambda *s: torch.randn(*s, dtype=torch.bfloat16) * 0.1)(D, D))}
    _A_in = _A_setup()

    def _B_setup():
        B, S, D, F = (1, 2048, 640, 1728)
        Fh = F // 2
        return {'x': _w((lambda *s: torch.randn(*s, dtype=torch.bfloat16) * 0.05)(B, S, D)), 'w_in': _w((lambda *s: torch.randn(*s, dtype=torch.bfloat16) * 0.8)(D, 2 * Fh)), 'w_sp': _w(0.01 * (lambda *s: torch.randn(*s, dtype=torch.bfloat16) * 0.8)(S, S)), 'b_sp': _w(1.0 + 0.01 * (lambda *s: torch.randn(*s, dtype=torch.bfloat16) * 0.05)(S)), 'w_out': _w((lambda *s: torch.randn(*s, dtype=torch.bfloat16) * 0.05)(Fh, D))}
    _B_in = _B_setup()
    return {'A_x': _A_in['x'], 'A_wqkv': _A_in['wqkv'], 'A_wo': _A_in['wo'], 'B_x': _B_in['x'], 'B_w_in': _B_in['w_in'], 'B_w_sp': _B_in['w_sp'], 'B_b_sp': _B_in['b_sp'], 'B_w_out': _B_in['w_out']}

def reference(A_x, A_wqkv, A_wo, B_x, B_w_in, B_w_sp, B_b_sp, B_w_out):

    def _A_ref(x, wqkv, wo):
        B, S, D = x.shape
        H = 32
        Dh = D // H
        W, dil = (8, 2)
        qkv = (lambda x, *d: x.permute(*d))((x @ wqkv).reshape(B, S, 3, H, Dh), 2, 0, 3, 1, 4)
        q, k, v = (qkv[0], qkv[1], qkv[2])
        scale = Dh ** (-0.5)
        pos = torch.arange(S)
        off = torch.arange(W) * dil
        idx = pos[:, None] - off[None, :]
        mask = idx >= 0
        idx_c = torch.where(mask, idx, 0).reshape(-1)
        kg = k[:, :, idx_c, :].reshape(B, H, S, W, Dh)
        vg = v[:, :, idx_c, :].reshape(B, H, S, W, Dh)
        logits = torch.einsum('bhsd,bhswd->bhsw', q, kg) * scale
        logits = torch.where(mask[None, None, :, :], logits, -1000000000.0)
        attn = _softmax(logits, axis=-1)
        o = torch.einsum('bhsw,bhswd->bhsd', attn, vg)
        return (lambda x, *d: x.permute(*d))(o, 0, 2, 1, 3).reshape(B, S, D) @ wo

    def _B_ref(x, w_in, w_sp, b_sp, w_out):
        B, S, D = x.shape
        F = w_in.shape[-1]
        Fh = F // 2
        z = _gelu(torch.einsum('bsd,df->bsf', x, w_in))
        z1 = z[..., :Fh].clone()
        z2 = _layernorm(z[..., Fh:], axis=-1)
        g = torch.einsum('ts,bsf->btf', w_sp, z2) + b_sp[None, :, None]
        return torch.einsum('bsf,fd->bsd', z1 * g, w_out)
    x = A_x
    x = x + _A_ref(_layernorm(x, axis=-1), A_wqkv, A_wo)
    x = x + _B_ref(_layernorm(x, axis=-1), B_w_in, B_w_sp, B_b_sp, B_w_out)
    x = x + _A_ref(_layernorm(x, axis=-1), A_wqkv, A_wo)
    x = x + _B_ref(_layernorm(x, axis=-1), B_w_in, B_w_sp, B_b_sp, B_w_out)
    return x
```

```cpp
#include <hip/hip_runtime.h>
#include <hip/hip_bf16.h>
#include <stdint.h>
typedef unsigned short u16;
__device__ __forceinline__ float bf2f(u16 b){ return __uint_as_float(((unsigned)b)<<16); }
__device__ __forceinline__ u16 f2bf(float f){ __hip_bfloat16 h = __float2bfloat16(f); return *(u16*)&h; }
constexpr int S=2048, D=640, F=1728, FH=864, H=32, DH=20;

__global__ void k_ln(const u16* in, int ld_in, int c0, int n, u16* out){
  int r = blockIdx.x; int t = threadIdx.x;
  __shared__ float red[256];
  float s=0; for(int i=t;i<n;i+=256) s += bf2f(in[(size_t)r*ld_in+c0+i]);
  red[t]=s; __syncthreads();
  for(int o=128;o>0;o>>=1){ if(t<o) red[t]+=red[t+o]; __syncthreads(); }
  float m = red[0]/n; __syncthreads();
  float q=0; for(int i=t;i<n;i+=256){ float d=bf2f(in[(size_t)r*ld_in+c0+i])-m; q+=d*d; }
  red[t]=q; __syncthreads();
  for(int o=128;o>0;o>>=1){ if(t<o) red[t]+=red[t+o]; __syncthreads(); }
  float rs = rsqrtf(red[0]/n + 1e-5f);
  for(int i=t;i<n;i+=256) out[(size_t)r*n+i] = f2bf((bf2f(in[(size_t)r*ld_in+c0+i])-m)*rs);
}
template<int MODE>
__global__ void k_gemm(const u16* A, const u16* B, u16* C, int M, int N, int K, const u16* aux, const u16* aux2){
  __shared__ float sa[16][17], sb[16][17];
  int tx=threadIdx.x, ty=threadIdx.y; int row=blockIdx.y*16+ty, col=blockIdx.x*16+tx;
  float acc=0;
  for(int k0=0;k0<K;k0+=16){
    sa[ty][tx]=bf2f(A[(size_t)row*K+k0+tx]);
    sb[ty][tx]=bf2f(B[(size_t)(k0+ty)*N+col]);
    __syncthreads();
    for(int k=0;k<16;k++) acc += sa[ty][k]*sb[k][tx];
    __syncthreads();
  }
  if(MODE==1){ acc = bf2f(f2bf(acc)) + bf2f(aux[(size_t)row*N+col]); }
  if(MODE==2){ float v=bf2f(f2bf(acc)); float u=0.7978845608028654f*(v+0.044715f*v*v*v); acc=0.5f*v*(1.f+tanhf(u)); }
  if(MODE==3){ float g=bf2f(f2bf(bf2f(f2bf(acc))+bf2f(aux[row]))); acc = g*bf2f(aux2[(size_t)row*F+col]); }
  C[(size_t)row*N+col]=f2bf(acc);
}
__global__ void k_attn(const u16* qkv, u16* o){
  int idx = blockIdx.x*blockDim.x+threadIdx.x; if(idx>=S*H) return;
  int s=idx/H, h=idx%H;
  float q[DH];
  #pragma unroll
  for(int d=0;d<DH;d++) q[d]=bf2f(qkv[(size_t)s*3*D+h*DH+d]);
  float lg[8]; float mx=-1e30f;
  #pragma unroll
  for(int w=0;w<8;w++){ int p=s-2*w; float a=0; if(p>=0){
    #pragma unroll
    for(int d=0;d<DH;d++) a+=q[d]*bf2f(qkv[(size_t)p*3*D+D+h*DH+d]);
    a*=0.22360679774997896f; } else a=-1e9f; lg[w]=a; mx=fmaxf(mx,a);}
  float sum=0;
  #pragma unroll
  for(int w=0;w<8;w++){ lg[w]=__expf(lg[w]-mx); sum+=lg[w]; }
  float inv=1.f/sum;
  #pragma unroll
  for(int d=0;d<DH;d++){ float a=0;
    #pragma unroll
    for(int w=0;w<8;w++){ int p=s-2*w; if(p<0) p=0; a+=lg[w]*inv*bf2f(qkv[(size_t)p*3*D+2*D+h*DH+d]); }
    o[(size_t)s*D+h*DH+d]=f2bf(a);}
}
extern "C" void kernel_launch(void* const* d_in, const int* in_sizes, int n_in, void* d_out, int out_size, void* d_ws, size_t ws_size, hipStream_t stream){
  const u16* x0=(const u16*)d_in[0]; const u16* wqkv=(const u16*)d_in[1]; const u16* wo=(const u16*)d_in[2];
  const u16* win=(const u16*)d_in[4]; const u16* wsp=(const u16*)d_in[5]; const u16* bsp=(const u16*)d_in[6]; const u16* wout=(const u16*)d_in[7];
  u16* ws=(u16*)d_ws;
  u16* xn=ws; u16* qkv=xn+S*D; u16* o=qkv+S*3*D; u16* x1=o+S*D; u16* z=x1+S*D; u16* z2n=z+S*F; u16* gz=z2n+S*FH; u16* x2=gz+S*FH;
  const u16* xin=x0;
  for(int l=0;l<2;l++){
    u16* xo = (l==1)?(u16*)d_out:x2;
    k_ln<<<S,256,0,stream>>>(xin,D,0,D,xn);
    k_gemm<0><<<dim3(3*D/16,S/16),dim3(16,16),0,stream>>>(xn,wqkv,qkv,S,3*D,D,nullptr,nullptr);
    k_attn<<<S*H/256,256,0,stream>>>(qkv,o);
    k_gemm<1><<<dim3(D/16,S/16),dim3(16,16),0,stream>>>(o,wo,x1,S,D,D,xin,nullptr);
    k_ln<<<S,256,0,stream>>>(x1,D,0,D,xn);
    k_gemm<2><<<dim3(F/16,S/16),dim3(16,16),0,stream>>>(xn,win,z,S,F,D,nullptr,nullptr);
    k_ln<<<S,256,0,stream>>>(z,F,FH,FH,z2n);
    k_gemm<3><<<dim3(FH/16,S/16),dim3(16,16),0,stream>>>(wsp,z2n,gz,S,FH,S,bsp,z);
    k_gemm<1><<<dim3(D/16,S/16),dim3(16,16),0,stream>>>(gz,wout,xo,S,D,FH,x1,nullptr);
    xin=x2;
  }
}
```

```cpp
#include <hip/hip_runtime.h>
#include <hip/hip_bf16.h>
#include <stdint.h>
typedef unsigned short u16;
__device__ __forceinline__ float bf2f(u16 b){ return __uint_as_float(((unsigned)b)<<16); }
__device__ __forceinline__ u16 f2bf(float f){ __hip_bfloat16 h = __float2bfloat16(f); return *(u16*)&h; }
constexpr int S=2048, D=640, F=1728, FH=864, H=32, DH=20;

__global__ void k_ln(const u16* in, int ld_in, int c0, int n, u16* out, float* outf){
  int r = blockIdx.x; int t = threadIdx.x;
  __shared__ float red[256];
  float s=0; for(int i=t;i<n;i+=256) s += bf2f(in[(size_t)r*ld_in+c0+i]);
  red[t]=s; __syncthreads();
  for(int o=128;o>0;o>>=1){ if(t<o) red[t]+=red[t+o]; __syncthreads(); }
  float m = red[0]/n; __syncthreads();
  float q=0; for(int i=t;i<n;i+=256){ float d=bf2f(in[(size_t)r*ld_in+c0+i])-m; q+=d*d; }
  red[t]=q; __syncthreads();
  for(int o=128;o>0;o>>=1){ if(t<o) red[t]+=red[t+o]; __syncthreads(); }
  float rs = rsqrtf(red[0]/n + 1e-5f);
  for(int i=t;i<n;i+=256){ float y=(bf2f(in[(size_t)r*ld_in+c0+i])-m)*rs; if(outf) outf[(size_t)r*n+i]=y; else out[(size_t)r*n+i] = f2bf(y);}
}
template<int MODE>
__global__ void k_gemm(const u16* A, const u16* B, u16* C, int M, int N, int K, const u16* aux, const u16* aux2){
  __shared__ float sa[16][17], sb[16][17];
  int tx=threadIdx.x, ty=threadIdx.y; int row=blockIdx.y*16+ty, col=blockIdx.x*16+tx;
  float acc=0;
  for(int k0=0;k0<K;k0+=16){
    sa[ty][tx]=(MODE==0||MODE==2)?((const float*)A)[(size_t)row*K+k0+tx]:bf2f(A[(size_t)row*K+k0+tx]);
    sb[ty][tx]=bf2f(B[(size_t)(k0+ty)*N+col]);
    __syncthreads();
    for(int k=0;k<16;k++) acc += sa[ty][k]*sb[k][tx];
    __syncthreads();
  }
  if(MODE==1){ acc = acc + bf2f(aux[(size_t)row*N+col]); }
  if(MODE==2){ float v=acc; float u=0.7978845608028654f*(v+0.044715f*v*v*v); acc=0.5f*v*(1.f+tanhf(u)); }
  if(MODE==3){ float g=acc+bf2f(aux[row]); acc = g*bf2f(aux2[(size_t)row*F+col]); }
  C[(size_t)row*N+col]=f2bf(acc);
}
__global__ void k_attn(const u16* qkv, u16* o){
  int idx = blockIdx.x*blockDim.x+threadIdx.x; if(idx>=S*H) return;
  int s=idx/H, h=idx%H;
  float q[DH];
  #pragma unroll
  for(int d=0;d<DH;d++) q[d]=bf2f(qkv[(size_t)s*3*D+h*DH+d]);
  float lg[8]; float mx=-1e30f;
  #pragma unroll
  for(int w=0;w<8;w++){ int p=s-2*w; float a=0; if(p>=0){
    #pragma unroll
    for(int d=0;d<DH;d++) a+=q[d]*bf2f(qkv[(size_t)p*3*D+D+h*DH+d]);
    a*=0.22360679774997896f; } else a=-1e9f; lg[w]=a; mx=fmaxf(mx,a);}
  float sum=0;
  #pragma unroll
  for(int w=0;w<8;w++){ lg[w]=__expf(lg[w]-mx); sum+=lg[w]; }
  float inv=1.f/sum;
  #pragma unroll
  for(int d=0;d<DH;d++){ float a=0;
    #pragma unroll
    for(int w=0;w<8;w++){ int p=s-2*w; if(p<0) p=0; a+=lg[w]*inv*bf2f(qkv[(size_t)p*3*D+2*D+h*DH+d]); }
    o[(size_t)s*D+h*DH+d]=f2bf(a);}
}
extern "C" void kernel_launch(void* const* d_in, const int* in_sizes, int n_in, void* d_out, int out_size, void* d_ws, size_t ws_size, hipStream_t stream){
  const u16* x0=(const u16*)d_in[0]; const u16* wqkv=(const u16*)d_in[1]; const u16* wo=(const u16*)d_in[2];
  const u16* win=(const u16*)d_in[4]; const u16* wsp=(const u16*)d_in[5]; const u16* bsp=(const u16*)d_in[6]; const u16* wout=(const u16*)d_in[7];
  u16* ws=(u16*)d_ws;
  u16* xn=ws; u16* qkv=xn+2*S*D; u16* o=qkv+S*3*D; u16* x1=o+S*D; u16* z=x1+S*D; u16* z2n=z+S*F; u16* gz=z2n+S*FH; u16* x2=gz+S*FH;
  const u16* xin=x0;
  for(int l=0;l<2;l++){
    u16* xo = (l==1)?(u16*)d_out:x2;
    k_ln<<<S,256,0,stream>>>(xin,D,0,D,xn,(float*)xn);
    k_gemm<0><<<dim3(3*D/16,S/16),dim3(16,16),0,stream>>>(xn,wqkv,qkv,S,3*D,D,nullptr,nullptr);
    k_attn<<<S*H/256,256,0,stream>>>(qkv,o);
    k_gemm<1><<<dim3(D/16,S/16),dim3(16,16),0,stream>>>(o,wo,x1,S,D,D,xin,nullptr);
    k_ln<<<S,256,0,stream>>>(x1,D,0,D,xn,(float*)xn);
    k_gemm<2><<<dim3(F/16,S/16),dim3(16,16),0,stream>>>(xn,win,z,S,F,D,nullptr,nullptr);
    k_ln<<<S,256,0,stream>>>(z,F,FH,FH,z2n,nullptr);
    k_gemm<3><<<dim3(FH/16,S/16),dim3(16,16),0,stream>>>(wsp,z2n,gz,S,FH,S,bsp,z);
    k_gemm<1><<<dim3(D/16,S/16),dim3(16,16),0,stream>>>(gz,wout,xo,S,D,FH,x1,nullptr);
    xin=x2;
  }
}
```

```cpp
#include <hip/hip_runtime.h>
#include <hip/hip_bf16.h>
#include <stdint.h>

#define AI __attribute__((always_inline))
#define LAS __attribute__((address_space(3)))
#define GAS __attribute__((address_space(1)))
typedef unsigned v6u __attribute__((ext_vector_type(6)));
typedef unsigned v4u __attribute__((ext_vector_type(4)));
typedef unsigned v2u __attribute__((ext_vector_type(2)));
typedef int v4i __attribute__((ext_vector_type(4)));
typedef float f32x4 __attribute__((ext_vector_type(4)));
typedef float f32x2 __attribute__((ext_vector_type(2)));
typedef __bf16 bf2 __attribute__((ext_vector_type(2)));
typedef short s4 __attribute__((ext_vector_type(4)));
typedef unsigned long long u64;
typedef __amdgpu_buffer_rsrc_t rsrc_t;

#define PROF 0

namespace {
constexpr int SC1 = 16;
constexpr int F0C = 4096;
constexpr int F0B = F0C + 16 * 256;
constexpr int F0S = F0B + 32 * 256;
constexpr int FLAY = F0S + 32 * 256;
constexpr int F1 = 0, F2 = F1 + 16 * 256, F4 = F2 + 32 * 256, F5 = F4 + 8 * 256, F6 = F5 + 32 * 256, FLAY_SZ = F6 + 32 * 256;
constexpr int ST_OFF = 1 << 17;
constexpr int ST_LAY = 32 * 8192;
constexpr int CSQ_OFF = ST_OFF + 2 * ST_LAY;
constexpr int CSI_OFF = CSQ_OFF + 16 * 16 * 512;
constexpr int WQ_OFF = 1 << 20, WQ_T = 153600;
constexpr int WO_OFF = WQ_OFF + 16 * WQ_T, WO_T = 102400;
constexpr int WI_OFF = WO_OFF + 8 * WO_T, WI_T = 143360;
constexpr int WU_OFF = WI_OFF + 16 * WI_T, WU_T = 138240;
constexpr int WS_OFF = WU_OFF + 8 * WU_T;
constexpr int ACT_OFF = WS_OFF + 128 * 65536;
constexpr int XT = 21504, OT = 20480, GT = 27648;
constexpr int XSZ = 128 * XT, OSZ = 128 * OT, ZSZ = 8 * 64 * 7168, GSZ = 128 * GT;
constexpr int A_XIN = 0, A_O = A_XIN + XSZ, A_X1 = A_O + OSZ, A_Z = A_X1 + XSZ, A_G = A_Z + ZSZ, ACT_LAY = A_G + GSZ;
constexpr int PROF_OFF = ACT_OFF + 2 * ACT_LAY;
constexpr int WS_USED = PROF_OFF + 256 * 256 * 4;
static_assert(FLAY + 2 * FLAY_SZ <= ST_OFF, "");
static_assert(CSI_OFF + 16 * 16 * 512 <= WQ_OFF, "");
constexpr int OOB = 0x40000000;
constexpr int LDS_BYTES = 163840;
constexpr int L_TOP = 159744;
constexpr int L_CSQ = L_TOP, L_CS2 = L_CSQ + 512, L_CS1 = L_CS2 + 512, L_XST3 = L_CS1 + 512, L_JUNK = L_XST3 + 512, L_PROF = L_JUNK + 256;
constexpr int L_XST1 = 153600, L_HST = L_XST1 + 2048;
constexpr int L_HALO = 0, L_NB = 24576;
constexpr int L_WO = 57344, L_X2 = L_WO;
constexpr int L_Z2B = 73728 - 4 * 14336;
constexpr int L_Z1B = 59392 - 3 * 14336;
constexpr int L_X3 = 43008, L_X3B = 102400, L_X5 = 139264;
constexpr int L_RED0 = 98304, L_RED1 = L_RED0 + 24576, L_RED2 = L_RED1 + 24576;
static_assert(L_RED2 >= 138240 && L_RED2 + 12288 <= L_TOP, "");
#define MAPQ 1
#define MAPO 1
#define MAPZ 1
constexpr int SHQ = MAPQ ? 4 : 2, SHO = MAPO ? 8 : 4, SHZ = MAPZ ? 8 : 4;
constexpr unsigned GTAG = 0x5A5A5A5Bu;
constexpr unsigned ONE2 = 0x3f803f80u;

template <int I> struct IC { static constexpr int v = I; };
template <int I, int N, class F> __device__ __forceinline__ void sfor(F&& f) {
  if constexpr (I < N) { f(IC<I>{}); sfor<I + 1, N>(f); }
}
__device__ __forceinline__ int opq_s(int x) { asm volatile("" : "+s"(x)); return x; }
__device__ __forceinline__ int opq_u(int x) { return opq_s(__builtin_amdgcn_readfirstlane(x)); }
__device__ __forceinline__ int opq_v(int x) { asm volatile("" : "+v"(x)); return x; }
template <class T> __device__ __forceinline__ void pin(T& c) { asm volatile("" : "+v"(c)); }

template <bool FIRST> __device__ __forceinline__ void mma_aa(f32x4& c, v4u a, v4u b) {
  if constexpr (FIRST) asm volatile("v_mfma_f32_16x16x32_bf16 %0, %1, %2, 0" : "=&v"(c) : "a"(a), "v"(b));
  else asm volatile("v_mfma_f32_16x16x32_bf16 %0, %1, %2, %0" : "+v"(c) : "a"(a), "v"(b));
}
template <bool FIRST, bool BA> __device__ __forceinline__ void mma(f32x4& c, v4u a, v4u b) {
  if constexpr (FIRST) {
    if constexpr (BA) asm volatile("v_mfma_f32_16x16x32_bf16 %0, %1, %2, 0" : "=&v"(c) : "v"(a), "a"(b));
    else asm volatile("v_mfma_f32_16x16x32_bf16 %0, %1, %2, 0" : "=&v"(c) : "v"(a), "v"(b));
  } else {
    if constexpr (BA) asm volatile("v_mfma_f32_16x16x32_bf16 %0, %1, %2, %0" : "+v"(c) : "v"(a), "a"(b));
    else asm volatile("v_mfma_f32_16x16x32_bf16 %0, %1, %2, %0" : "+v"(c) : "v"(a), "v"(b));
  }
}
__device__ __forceinline__ void mma_c(f32x4& d, v4u a, v4u b, f32x4 c) { asm volatile("v_mfma_f32_16x16x32_bf16 %0, %1, %2, %3" : "=&v"(d) : "v"(a), "a"(b), "v"(c)); }
#define NOP2() asm volatile("s_nop 1")
__device__ __forceinline__ void mma_settle() { asm volatile("s_nop 7" ::: "memory"); }
__device__ __forceinline__ float dot2(unsigned a, unsigned b, float c) {
  return __builtin_amdgcn_fdot2_f32_bf16(__builtin_bit_cast(bf2, a), __builtin_bit_cast(bf2, b), c, false);
}
__device__ __forceinline__ unsigned pk(float lo, float hi) {
  f32x2 v = {lo, hi};
  return __builtin_bit_cast(unsigned, __builtin_convertvector(v, bf2));
}
__device__ __forceinline__ v2u pk4(f32x4 v) { return v2u{pk(v[0], v[1]), pk(v[2], v[3])}; }
__device__ __forceinline__ float blo(unsigned u) { return __builtin_bit_cast(float, u << 16); }
__device__ __forceinline__ float bhi(unsigned u) { return __builtin_bit_cast(float, u & 0xffff0000u); }
__device__ __forceinline__ f32x4 unpk4(v2u r) { return f32x4{blo(r.x), bhi(r.x), blo(r.y), bhi(r.y)}; }
__device__ __forceinline__ float asf(unsigned u) { return __builtin_bit_cast(float, u); }
__device__ __forceinline__ unsigned asu(float f) { return __builtin_bit_cast(unsigned, f); }
__device__ __forceinline__ unsigned sel(unsigned a, unsigned b, u64 m) { unsigned r; asm("v_cndmask_b32_e64 %0, %1, %2, %3" : "=v"(r) : "v"(a), "v"(b), "s"(m)); return r; }
__device__ __forceinline__ float self(float a, float b, u64 m) { return asf(sel(asu(a), asu(b), m)); }
__device__ __forceinline__ unsigned sel0(unsigned b, u64 m) { unsigned r; asm("v_cndmask_b32_e64 %0, 0, %1, %2" : "=v"(r) : "v"(b), "s"(m)); return r; }
__device__ __forceinline__ int seloob(int b, u64 m) { int r; asm("v_cndmask_b32_e64 %0, 2.0, %1, %2" : "=v"(r) : "v"(b), "s"(m)); return r; }
__device__ __forceinline__ float vmax(float a, float b) { return __builtin_elementwise_maximum(a, b); }
__device__ __forceinline__ float gadd(float a, float b) { float r; asm volatile("v_add_f32 %0, %1, %2" : "=v"(r) : "v"(a), "v"(b)); return r; }
__device__ __forceinline__ float gmul(float a, float b) { float r; asm volatile("v_mul_f32 %0, %1, %2" : "=v"(r) : "v"(a), "v"(b)); return r; }
__device__ __forceinline__ unsigned gcvt(float a, float b) { unsigned r; asm volatile("v_cvt_pk_bf16_f32 %0, %1, %2" : "=v"(r) : "v"(a), "v"(b)); return r; }
template <int C> __device__ __forceinline__ int gaddi(int a) { int r; asm volatile("v_add_u32 %0, %1, %2" : "=v"(r) : "n"(C), "v"(a)); return r; }
constexpr u64 M_ODD = 0xAAAAAAAAAAAAAAAAull, M_G0 = 0xFFFFull, M_G1 = 0xFFFF0000ull, M_GEVEN = 0x0000FFFF0000FFFFull, M_LO = 0xFFFFFFFFull;
constexpr u64 le_mask(int add) { u64 m = 0; for (int l = 0; l < 64; l++) if (4 * (l >> 4) + (l & 1) + add <= (l & 15)) m |= 1ull << l; return m; }
__device__ __forceinline__ rsrc_t mk(const void* p, int bytes) { return __builtin_amdgcn_make_buffer_rsrc((void*)p, 0, bytes, 0x00020000); }
__device__ __forceinline__ v4i mkd(const void* p, int bytes) {
  u64 a = (u64)p; v4i r; r.x = (int)(unsigned)a; r.y = (int)((unsigned)(a >> 32) & 0xffffu); r.z = bytes; r.w = 0x00020000; return r;
}
template <int AUX> __device__ __forceinline__ v4u ld16(rsrc_t r, int v, int s) { return __builtin_amdgcn_raw_buffer_load_b128(r, v, s, AUX); }
template <int AUX> __device__ __forceinline__ v2u ld8(rsrc_t r, int v, int s) { return __builtin_amdgcn_raw_buffer_load_b64(r, v, s, AUX); }
__device__ __forceinline__ void st16(v4u d, rsrc_t r, int v, int s) { __builtin_amdgcn_raw_buffer_store_b128(d, r, v, s, SC1); }
#define BAR() asm volatile("s_waitcnt lgkmcnt(0)\n\ts_barrier" ::: "memory")
#define BAR0() asm volatile("s_barrier" ::: "memory")
#define DRAIN() asm volatile("s_waitcnt vmcnt(0)" ::: "memory")
#define WAITV(n) asm volatile("s_waitcnt vmcnt(%0)" ::"n"(n) : "memory")
#define SB() __builtin_amdgcn_sched_barrier(0)
#define FENCE() do { asm volatile("" ::: "memory"); __builtin_amdgcn_sched_barrier(0); } while (0)
#define GL(o) "\n\tbuffer_load_dwordx4 %1, %2, %3 offen offset:" #o " sc1 lds"
#define GLARGS ::"s"(lds), "v"(voff), "s"(rs), "s"(soff)
__device__ __forceinline__ void glds_in(v4i rs, int voff, int soff, int lds) {
  asm volatile("s_mov_b32 m0, %0\n\ts_nop 0\n\tbuffer_load_dwordx4 %1, %2, %3 offen lds" GLARGS : "memory");
}
template <int N> __device__ __forceinline__ void gldsn(v4i rs, int voff, int soff, int lds) {
  static_assert(N >= 1 && N <= 4, "");
  if constexpr (N == 1) asm volatile("s_mov_b32 m0, %0\n\ts_nop 0" GL(0) GLARGS : "memory");
  if constexpr (N == 2) asm volatile("s_mov_b32 m0, %0\n\ts_nop 0" GL(0) GL(1024) GLARGS : "memory");
  if constexpr (N == 3) asm volatile("s_mov_b32 m0, %0\n\ts_nop 0" GL(0) GL(1024) GL(2048) GLARGS : "memory");
  if constexpr (N == 4) asm volatile("s_mov_b32 m0, %0\n\ts_nop 0" GL(0) GL(1024) GL(2048) GL(3072) GLARGS : "memory");
}
template <int N0, int N1> __device__ __forceinline__ void cp_run(v4i rs, int voff, int src, int dst) {
  if constexpr (N0 < N1) {
    constexpr int n = N1 - N0 < 4 ? N1 - N0 : 4;
    gldsn<n>(rs, voff, src + N0 * 1024, dst + N0 * 1024);
    cp_run<N0 + n, N1>(rs, voff, src, dst);
  }
}
__device__ __forceinline__ void cp_arm(int lds) { asm volatile("s_mov_b32 m0, %0" ::"s"(lds) : "memory"); }
template <int U> __device__ __forceinline__ void cp_one(v4i rs, int voff, int soff) {
  asm volatile("buffer_load_dwordx4 %0, %1, %2 offen offset:%3 sc1 lds" ::"v"(voff), "s"(rs), "s"(soff), "n"((U & 3) * 1024) : "memory");
}
__device__ __forceinline__ void touch(v4i rs, int voff, int soff) {
  asm volatile("s_mov_b32 m0, %0\n\ts_nop 0\n\tbuffer_load_dword %1, %2, %3 offen sc1 lds" ::"s"(L_JUNK), "v"(voff), "s"(rs), "s"(soff) : "memory");
}
template <int OFF, bool WS> __device__ __forceinline__ void lda16(v4u& d, v4i rs, int voff, int soff) {
  if constexpr (WS) asm volatile("buffer_load_dwordx4 %0, %1, %2, %3 offen offset:%4 sc1" : "+a"(d) : "v"(voff), "s"(rs), "s"(soff), "n"(OFF) : "memory");
  else asm volatile("buffer_load_dwordx4 %0, %1, %2, %3 offen offset:%4" : "+a"(d) : "v"(voff), "s"(rs), "s"(soff), "n"(OFF) : "memory");
}
template <int OFF> __device__ __forceinline__ void sta16(const v4u& d, v4i rs, int voff, int soff) {
  asm volatile("buffer_store_dwordx4 %0, %1, %2, %3 offen offset:%4 sc1" ::"a"(d), "v"(voff), "s"(rs), "s"(soff), "n"(OFF) : "memory");
}
template <int OFF> __device__ __forceinline__ void ldsa16(v4u& d, int addr) { asm volatile("ds_read_b128 %0, %1 offset:%2" : "+a"(d) : "v"(addr), "n"(OFF) : "memory"); }
__device__ __forceinline__ void park(v4u& d, v4u v) { asm volatile("" : "=a"(d) : "0"(v)); }
__device__ __forceinline__ void tch(v4i rs, int voff, int soff) {
  asm volatile("buffer_load_dword %0, %1, %2 offen sc1 lds" ::"v"(voff), "s"(rs), "s"(soff) : "memory");
}
__device__ __forceinline__ int qslot_off(int w) { return (w * 7680) & ~1023; }
template <class T> __device__ __forceinline__ LAS T* lp(int off) { return (LAS T*)(size_t)(unsigned)off; }
__device__ __forceinline__ v4u lds16(int off) { return *lp<v4u>(off); }
__device__ __forceinline__ v2u lds8(int off) { return *lp<v2u>(off); }
__device__ __forceinline__ f32x4 ldsf4(int off) { return *lp<f32x4>(off); }
__device__ __forceinline__ float ldsf(int off) { return *lp<float>(off); }
__device__ __forceinline__ void poll(rsrc_t rws, int voff, bool off, unsigned lo, unsigned hi) {
  for (unsigned spins = 0; spins < (1u << 18); ++spins) {
    asm volatile("" ::: "memory");
    const v2u v = ld8<SC1>(rws, voff, 0);
    if (__all(off || (v.x == lo && v.y == hi))) break;
  }
  asm volatile("" ::: "memory");
}
__device__ __forceinline__ v2u sw16(unsigned a, unsigned b) { auto r = __builtin_amdgcn_permlane16_swap(a, b, false, false); return v2u{r[0], r[1]}; }
__device__ __forceinline__ v2u sw32(unsigned a, unsigned b) { auto r = __builtin_amdgcn_permlane32_swap(a, b, false, false); return v2u{r[0], r[1]}; }
__device__ __forceinline__ v4u join16(v2u x, v2u y) { const v2u a = sw16(x.x, y.x), b = sw16(x.y, y.y); return v4u{a.x, b.x, a.y, b.y}; }
template <int CTRL> __device__ __forceinline__ float dpp(float v) {
  return __builtin_bit_cast(float, __builtin_amdgcn_update_dpp(0, __builtin_bit_cast(int, v), CTRL, 0xf, 0xf, true));
}
__device__ __forceinline__ float xsum2(float s, float q) {
  const v2u a = sw32(asu(s), asu(q)); const float t = asf(a.x) + asf(a.y);
  const v2u b = sw16(asu(t), asu(t)); return asf(b.x) + asf(b.y);
}
__device__ __forceinline__ f32x2 finish_stat(float s, float q, float inv_n) {
  const float m = s * inv_n;
  return f32x2{m, __builtin_amdgcn_rsqf(fmaxf(q * inv_n - m * m, 0.f) + 1e-5f)};
}
__device__ __forceinline__ f32x4 gelu4(f32x4 v) {
  const float k1 = -2.0f * 0.7978845608028654f * 1.4426950408889634f, k2 = k1 * 0.044715f;
  f32x4 e = v * ((v * v) * k2 + k1);
  asm("" : "+v"(e));
#pragma unroll
  for (int i = 0; i < 4; i++) e[i] = __builtin_amdgcn_exp2f(e[i]);
  e += 1.0f;
#pragma unroll
  for (int i = 0; i < 4; i++) e[i] = __builtin_amdgcn_rcpf(e[i]);
  return v * e;
}
__device__ __forceinline__ f32x2 rowsum8(f32x4 s, f32x4 q) {
  float a, b, c, d;
  asm volatile("s_nop 1\n\t"
      "v_add_f32_dpp %0, %4, %4 row_ror:8 row_mask:0xf bank_mask:0x3\n\t"
      "v_add_f32_dpp %1, %5, %5 row_ror:8 row_mask:0xf bank_mask:0x3\n\t"
      "v_add_f32_dpp %2, %8, %8 row_ror:8 row_mask:0xf bank_mask:0x3\n\t"
      "v_add_f32_dpp %3, %9, %9 row_ror:8 row_mask:0xf bank_mask:0x3\n\t"
      "v_add_f32_dpp %0, %6, %6 row_ror:8 row_mask:0xf bank_mask:0xc\n\t"
      "v_add_f32_dpp %1, %7, %7 row_ror:8 row_mask:0xf bank_mask:0xc\n\t"
      "v_add_f32_dpp %2, %10, %10 row_ror:8 row_mask:0xf bank_mask:0xc\n\t"
      "v_add_f32_dpp %3, %11, %11 row_ror:8 row_mask:0xf bank_mask:0xc\n\t"
      "v_add_f32_dpp %0, %0, %0 row_half_mirror row_mask:0xf bank_mask:0x5\n\t"
      "v_add_f32_dpp %0, %1, %1 row_half_mirror row_mask:0xf bank_mask:0xa\n\t"
      "v_add_f32_dpp %2, %2, %2 row_half_mirror row_mask:0xf bank_mask:0x5\n\t"
      "v_add_f32_dpp %2, %3, %3 row_half_mirror row_mask:0xf bank_mask:0xa\n\t"
      "v_add_f32_dpp %0, %0, %0 quad_perm:[1,0,3,2] row_mask:0xf bank_mask:0xf\n\t"
      "s_nop 0\n\t"
      "v_add_f32_dpp %2, %2, %2 quad_perm:[1,0,3,2] row_mask:0xf bank_mask:0xf\n\t"
      "v_add_f32_dpp %0, %0, %0 quad_perm:[2,3,0,1] row_mask:0xf bank_mask:0xf\n\t"
      "s_nop 0\n\t"
      "v_add_f32_dpp %2, %2, %2 quad_perm:[2,3,0,1] row_mask:0xf bank_mask:0xf"
      : "=&v"(a), "=&v"(b), "=&v"(c), "=&v"(d)
      : "v"(s[0]), "v"(s[1]), "v"(s[2]), "v"(s[3]), "v"(q[0]), "v"(q[1]), "v"(q[2]), "v"(q[3]));
  return f32x2{a, c};
}
__device__ __forceinline__ v4u bperm4(int idx, v4u v) {
  v4u r;
  r.x = __builtin_amdgcn_ds_bpermute(idx, v.x); r.y = __builtin_amdgcn_ds_bpermute(idx, v.y);
  r.z = __builtin_amdgcn_ds_bpermute(idx, v.z); r.w = __builtin_amdgcn_ds_bpermute(idx, v.w);
  return r;
}
__device__ __forceinline__ v2u trd(int off) { return __builtin_bit_cast(v2u, __builtin_amdgcn_ds_read_tr16_b64_v4i16(lp<s4>(off))); }
#define STAMP(i) do {} while (0)
#define CSTAMP(i) do {} while (0)
#define LANE() const int l = opq_v((int)threadIdx.x) & 63, l16 = l * 16, g = l >> 4, c16 = l & 15; (void)g; (void)c16; (void)l16; \
  const int w = opq_s(w_), po = opq_s(po_), co = opq_s(co_), pz = opq_s(pz_), cz = opq_s(cz_), P = opq_s(P_), h = opq_s(h_); \
  (void)w; (void)po; (void)co; (void)pz; (void)cz; (void)P; (void)h; \
  const int kh = w & 1, w2 = w >> 1; (void)kh; (void)w2; \
  auto flag = [&](int off) AI { __builtin_amdgcn_raw_buffer_store_b64(v2u{tag_lo, tag_hi}, rws, seloob(0, 1ull), w == 0 ? off : OOB, SC1); }; (void)flag;
#define WSP() int wsb[4]; \
  _Pragma("unroll") for (int r = 0; r < 4; r++) wsb[r] = opq_s(WS_OFF + (pz * 256 + ((w + r) & 3) * 64 + 16 * w) * 1024); \
  const int zr = opq_s(MAPZ ? pz + 2 * cz + (cz >> 2) : 2 * pz + cz); \
  auto wsp_ld = [&](auto ni) AI { constexpr int n = ni.v, e = n >> 3, r = (n >> 1) & 3, s_ = n & 1; \
    lda16<s_ * 1024, true>(W[r][2 * e + s_], dws, l16, wsb[r] + ((e + zr) & 7) * 2048); }; (void)wsp_ld;

template <int NS, int NCT, bool TR, int WIN, bool INIT, class BA, class GO, class LD, class HA, class HB>
__device__ __forceinline__ void gemm_m2k2(BA&& badr, int xw, v4u (&A)[2][NS], f32x4 (&acc)[2][NCT], GO&& go, LD&& lda, HA&& ha, HB&& hb) {
  constexpr int NF = NS * NCT, NL = NF - NCT;
  static_assert(NF % 2 == 0, "");
  v4u B[NF];
  auto ldB = [&](auto fi) AI { constexpr int f = fi.v; B[f] = lds16(badr(IC<f / NCT>{}) + (f % NCT) * 1024); };
  auto mm = [&](auto ii, auto fi) AI {
    constexpr int i = ii.v, f = fi.v, t = f / NCT, ct = f % NCT;
    if (TR) mma<t == 0 && !(INIT && i == 0), false>(acc[i][ct], B[f], A[i][t]); else mma<t == 0 && !(INIT && i == 0), false>(acc[i][ct], A[i][t], B[f]);
    SB();
  };
  sfor<0, 6>([&](auto fi) AI { ldB(IC<(fi.v ^ 1)>{}); SB(); });
  go();
  if constexpr (WIN > 0) sfor<0, WIN>([&](auto ti) AI { A[1][ti.v] = lda(IC<1>{}, ti); A[0][ti.v] = lda(IC<0>{}, ti); });
  SB();
  sfor<0, NL>([&](auto fi) AI {
    constexpr int f = fi.v;
    mm(IC<0>{}, fi);
    if constexpr (((f + 6) ^ 1) < NF) ldB(IC<((f + 6) ^ 1)>{});
    ha(fi);
    SB();
    mm(IC<1>{}, fi);
    if constexpr (WIN > 0) {
      constexpr int n = 2 * WIN + f / 2;
      if constexpr (f % 2 == 0 && n < 2 * NS) A[(n & 1) ^ 1][n >> 1] = lda(IC<((n & 1) ^ 1)>{}, IC<(n >> 1)>{});
    }
    if constexpr (f == NL - 1) sfor<NL, NF>([&](auto gi) AI { if constexpr (((gi.v + 6) ^ 1) < NF) ldB(IC<((gi.v + 6) ^ 1)>{}); });
    hb(fi);
    SB();
  });
  sfor<NL, NF>([&](auto fi) AI { mm(IC<1>{}, fi); ha(fi); SB(); });
  sfor<NL, NF>([&](auto fi) AI {
    constexpr int j = fi.v - NL, D = NCT == 5 ? 3 : 1;
    mm(IC<0>{}, fi);
    if constexpr (j >= D) { pin(acc[1][j - D]); *lp<f32x4>(xw + (j - D) * 1024) = acc[1][j - D]; }
    if constexpr (j == NCT - 1) sfor<NCT - D, NCT>([&](auto ki) AI { pin(acc[1][ki.v]); *lp<f32x4>(xw + ki.v * 1024) = acc[1][ki.v]; });
    SB();
  });
}
template <int NCT> __device__ __forceinline__ void xchg_read(int src, f32x4 (&acc)[2][NCT]) {
#pragma unroll
  for (int j = 0; j < NCT; j++) acc[1][j] = ldsf4(src + j * 1024);
  SB();
#pragma unroll
  for (int j = 0; j < NCT; j++) { pin(acc[0][j]); acc[0][j] += acc[1][j]; }
}
__device__ __forceinline__ void store_x(const f32x4 (&x)[5], rsrc_t rws, int so, int ss, int vo, int vo4, int vs) {
  v2u d[5];
#pragma unroll
  for (int j = 0; j < 5; j++) d[j] = pk4(x[j]);
  float s0 = 0.f, q0 = 0.f;
#pragma unroll
  for (int j = 0; j < 5; j++) { s0 = dot2(d[j].x, ONE2, s0); q0 = dot2(d[j].x, d[j].x, q0); s0 = dot2(d[j].y, ONE2, s0); q0 = dot2(d[j].y, d[j].y, q0); }
  st16(join16(d[0], d[1]), rws, vo, so);
  st16(join16(d[2], d[3]), rws, vo + 1024, so);
  st16(join16(d[4], d[4]), rws, vo4 + 2048, so);
  __builtin_amdgcn_raw_buffer_store_b32(asu(xsum2(s0, q0)), rws, vs, ss, SC1);
}
constexpr int QPRE = 9;
constexpr int q_step(int n) { return n % 9 == 0 ? n / 9 * 4 : n / 9 * 4 + (n % 9 - 1) / 2; }
constexpr int q_cnt(int f) { int n = QPRE; for (int i = 0; i <= f; i++) if (i % 2 == 0 && n < 45 && q_step(n) <= i / 8 + 6) n++; return n; }
constexpr int q_free(int f) { int n = 0; for (int i = 0; i < f; i += 2) if (q_cnt(i) == q_cnt(i - 1)) n++; return (f % 2 == 0 && q_cnt(f) == q_cnt(f - 1)) ? n : -100; }
constexpr int l0_vm(int f) { return (f < 72 ? f % 3 == 0 : f < 104 ? f % 4 == 0 : 0) + (f >= 73 && f < 105 && f % 4 == 2) + (f == 1) + (f == 86); }
constexpr int l0_cnt(int f0, int f1) { int n = 0; for (int i = f0; i < f1; i++) n += l0_vm(i); return n; }
constexpr int sp_s(int f) { return f < 14 ? f / 7 : f < 98 ? 2 + ((f - 14) % 28) / 2 : 2 + (f - 98); }
constexpr int sp_j(int f) { return f < 14 ? f % 7 : f < 98 ? (f - 14) / 28 * 2 + (f & 1) : 6; }
#define XA(n) W[(n) / 14][2 + (n) % 14]

__global__ __launch_bounds__(256, 1) void fused_kernel(const char* __restrict__ x0, const char* __restrict__ wqkv, const char* __restrict__ wo,
                                                       const char* __restrict__ win, const char* __restrict__ wsp, const char* __restrict__ bsp,
                                                       const char* __restrict__ wout, char* out, char* ws) {
  const int w_ = __builtin_amdgcn_readfirstlane(threadIdx.x >> 6);
  const int b = blockIdx.x, xcd = b & 7, jb = b >> 3;
  const int pa = xcd * 4 + (jb & 3), ca = jb >> 2, pb = (xcd >> 1) * 8 + (jb & 7), cb = (xcd & 1) * 4 + (jb >> 3);
  const int po_ = MAPO ? pb : pa, co_ = MAPO ? cb : ca, pz_ = MAPZ ? pb : pa, cz_ = MAPZ ? cb : ca;
  const int P_ = MAPQ ? (xcd >> 1) * 4 + (jb & 3) : xcd * 2 + (jb & 1), h_ = MAPQ ? (xcd & 1) * 8 + (jb >> 2) : jb >> 1;
  const rsrc_t rws = mk(ws, WS_USED);
  const v4i dws = mkd(ws, WS_USED);
  STAMP(0); CSTAMP(62);
  unsigned tag_lo, tag_hi;
  v4u W[4][16];
#pragma unroll
  for (int r = 0; r < 4; r++)
#pragma unroll
    for (int s = 0; s < 16; s++) asm volatile("" : "=a"(W[r][s]));
  {
    LANE();
    {
      const v4i dq = mkd(wqkv, 640 * 3840);
      const int vq = g * 3840 + c16 * 16 + (c16 < 5 ? 1280 : c16 < 10 ? -80 : 2400), s0 = (P * 40 + w * 12) * 3840 + 80 * h, kill = w == 3 ? OOB : 0;
      glds_in(dq, vq, s0, w * 3072);
      glds_in(dq, vq, (s0 + 4 * 3840) | kill, w * 3072 + 1024);
      glds_in(dq, vq, (s0 + 8 * 3840) | kill, w * 3072 + 2048);
    }
    const int tau = jb, sl = xcd, stg = 32768 + w * 8192;
    const int ty = tau < 8 ? 0 : tau < 24 ? 1 : 2, ot = tau - ty * 8 - (ty >> 1) * 8, cc = ot & 7;
    const int o_kg = opq_u((ty < 2 ? 10 * sl : sl < 4 ? 14 * sl : 4 + 13 * sl) + w2), o_n = opq_u((ty < 2 ? 10 : sl < 4 ? 14 : 13) - w2);
    {
      const char* src = ty == 0 ? wo : ty == 1 ? win : wout;
      const int ldb = ty == 1 ? 3456 : 1280, lim = ty != 1 ? 9 : cc < 6 ? 13 : 11, chi = kh * 8 + (l & 7);
      const int col = ty != 1 ? ot * 160 : (ot < 8 ? 1728 : 0) + (cc < 6 ? 7 * cc : 6 + 6 * cc) * 32;
      const v4i dsrc = mkd(src, (ty == 2 ? 864 : 640) * ldb);
      const int ov = (l >> 3) * ldb + (chi < lim ? chi : lim) * 16;
      const int o0 = opq_u(o_kg * 8 * ldb + col), od = opq_u(16 * ldb), k5 = opq_u(o_n > 10 ? 0 : OOB), k6 = opq_u(o_n > 12 ? 0 : OOB);
      asm volatile("s_nop 4");
#pragma unroll
      for (int i = 0; i < 7; i++) glds_in(dsrc, ov, (o0 + i * od) | (i == 5 ? k5 : i == 6 ? k6 : 0), stg + i * 1024);
    }
    const v2u ep = ld8<SC1>(rws, 0, 0);
    FENCE();
    const rsrc_t rx = mk(x0, 2048 * 1280);
    const int xo = (128 * P + 32 * w) * 1280, ho = P > 0 ? (128 * P - 16) * 1280 : OOB;
    int vx[4];
#pragma unroll
    for (int i = 0; i < 4; i++) vx[i] = (l >> 2) * 1280 + (((l ^ (0 - g)) & 3) + ((i + w) & 3) * 4) * 16;
    v4u R[45];
    auto ldX = [&](auto ni) AI {
      constexpr int n = ni.v, t = q_step(n), i = (n % 9) & 1;
      R[n] = ld16<0>(rx, vx[t & 3] + (t >> 2) * 256, n % 9 == 0 ? ho : xo + i * 16 * 1280);
    };
    const int ct = 2 * w + (g & 1), pq = l & 3;
    const int cb_ = ct == 2 ? 32 + (pq & 1) * 8 + (pq >> 1) * 80 : ct == 7 ? 192 + (pq & 1) * 8 : (ct < 2 ? ct * 48 : ((ct - 1) >> 1) * 80 + ((ct - 1) & 1) * 48) + pq * 8;
    const int trq = opq_v(((g >> 1) * 8 + (c16 >> 2)) * 256 + cb_);
    int qv[3];
#pragma unroll
    for (int i = 0; i < 3; i++) {
      const int k = 2 * i + (g >> 1), kap = 5 * P + k;
      qv[i] = opq_v((k < 5 && (ct < 7 || c16 < 8)) ? (kap >> 2) * 7680 + ct * 1024 + ((kap & 3) * (ct == 7 ? 8 : 16) + c16) * 16 : OOB);
    }
    FENCE();
    sfor<0, 4>(ldX);
    FENCE();
    WAITV(12);
    STAMP(1);
    BAR0();
    {
      v2u lo[3], hi[3];
#pragma unroll
      for (int i = 0; i < 3; i++) { lo[i] = trd(trq + i * 4096); hi[i] = trd(trq + i * 4096 + 1024); }
      SB();
#pragma unroll
      for (int i = 0; i < 3; i++) st16(v4u{lo[i].x, lo[i].y, hi[i].x, hi[i].y}, rws, qv[i], WQ_OFF + h * WQ_T);
      float cs = 0.f;
#pragma unroll
      for (int i = 0; i < 3; i++) { cs = dot2(lo[i].x, ONE2, cs); cs = dot2(lo[i].y, ONE2, cs); cs = dot2(hi[i].x, ONE2, cs); cs = dot2(hi[i].y, ONE2, cs); }
      const v2u t = sw32(asu(cs), asu(cs));
      __builtin_amdgcn_raw_buffer_store_b32(asu(asf(t.x) + asf(t.y)), rws, seloob(l * 4, M_LO), CSQ_OFF + (h * 16 + P) * 512 + w * 128, SC1);
    }
    FENCE();
    sfor<4, 18>(ldX);
    FENCE();
    {
      const u64 epoch = ((u64)(unsigned)__builtin_amdgcn_readfirstlane(ep.y) << 32) | (unsigned)__builtin_amdgcn_readfirstlane(ep.x);
      const u64 tag = epoch * 0x9E3779B97F4A7C15ull + 0xD1B54A32D192ED03ull;
      tag_lo = opq_s((unsigned)tag); tag_hi = opq_s((unsigned)(tag >> 32));
    }
    WAITV(14);
    BAR0();
    flag(F0C + h * 256 + P * 8);
    STAMP(2);
    float xs[3] = {0.f, 0.f, 0.f}, xq[3] = {0.f, 0.f, 0.f};
    const int xwr = opq_v(16384 + w * 4096 + l16), xrd = opq_v(16384 + w * 4096 + c16 * 64 + ((g ^ (0 - (c16 >> 2))) & 3) * 16);
    auto doX = [&](auto ni) AI {
      constexpr int n = ni.v, i = n % 9 == 0 ? 2 : (n % 9) & 1;
      *lp<v4u>(xwr + (n & 3) * 1024) = R[n];
      ldsa16<(n & 3) * 1024>(XA(n), xrd);
#pragma unroll
      for (int e = 0; e < 4; e++) { xs[i] = dot2(R[n][e], ONE2, xs[i]); xq[i] = dot2(R[n][e], R[n][e], xq[i]); }
      pin(xs[i]); pin(xq[i]);
      SB();
    };
    sfor<18, 45>([&](auto ni) AI { ldX(ni); doX(IC<ni.v - 18>{}); });
    {
      const int tro = stg + (c16 >> 2) * 128 + g * 32 + (l & 3) * 8;
      const int o_v = kh * 4 + g < (ty == 1 ? 7 : 5) ? (kh * 4 + g) * 1024 + c16 * 16 : OOB;
      const int o_dst = ty == 0 ? WO_OFF + ot * WO_T : ty == 1 ? WI_OFF + ot * WI_T : WU_OFF + ot * WU_T, kb = ty < 2 ? 10 : 14, kstr = ty == 1 ? 7168 : 5120;
      float cs = 0.f;
#pragma unroll
      for (int i = 0; i < 7; i++) {
        const v2u lo = trd(tro + i * 1024), hi = trd(tro + i * 1024 + 512);
        if (i < 5) { cs = dot2(lo.x, ONE2, cs); cs = dot2(lo.y, ONE2, cs); cs = dot2(hi.x, ONE2, cs); cs = dot2(hi.y, ONE2, cs); }
        const int kap = o_kg + 2 * i, ks = kap >> 2, pos = ks < kb ? 2 * ks : 2 * (ks - kb) + 1;
        st16(v4u{lo.x, lo.y, hi.x, hi.y}, rws, o_v, opq_u(2 * i < o_n ? o_dst + pos * kstr + (kap & 3) * 256 : OOB));
      }
      __builtin_amdgcn_raw_buffer_store_b32(asu(cs), rws, l * 4, opq_u(ty == 1 ? CSI_OFF + (ot * 16 + sl * 2 + w2) * 512 + kh * 256 : OOB), SC1);
      st16(v4u{0, 0, 0, 0}, rws, seloob(g * ST_LAY + c16 * 16, M_LO), ST_OFF + po * 8192 + w * 2048 + co * 256);
    }
    FENCE();
    sfor<27, 45>(doX);
#pragma unroll
    for (int i = 0; i < 3; i++) { xs[i] += dpp<0xB1>(xs[i]); xq[i] += dpp<0xB1>(xq[i]); xs[i] += dpp<0x4E>(xs[i]); xq[i] += dpp<0x4E>(xq[i]); }
#pragma unroll
    for (int i = 0; i < 2; i++) {
      const f32x2 v = finish_stat(xs[i], xq[i], 1.f / 640.f);
      *lp<float>(L_XST1 + w * 512 + i * 64 + (l & 60)) = v.x; *lp<float>(L_XST1 + w * 512 + 256 + i * 64 + (l & 60)) = v.y;
    }
    *lp<f32x2>(L_HST + w * 128 + (l >> 2) * 8) = f32x2{xs[2], xq[2]};
    STAMP(3);
  }

#pragma unroll 1
  for (int Lr = 0;; Lr = 1) {
    const int L = opq_s(Lr), kill1 = L << 30;
    const int act = ACT_OFF + L * ACT_LAY, fl = FLAY + L * FLAY_SZ;
    const int sb = L * 24 + 5;
    f32x4 z1[7];
    unsigned braw;
    constexpr int RZ = 14;
    v4u Z[RZ];
    { LANE();
      const int qo = qslot_off(w), wq = WQ_OFF + h * WQ_T + qo;
      int vb[4], vh[4];
#pragma unroll
      for (int i = 0; i < 4; i++) { const int k = (i + w) & 3; vb[i] = opq_v(l16 + k * 7680); vh[i] = opq_v((g * 8 + (l & 7)) * 16 + k * 7680); }
      const int h2 = w < 2 ? w + 4 : w;
      const int c4 = opq_v(c16 * 4), g16 = opq_v(g * 16);
      const int wos = opq_s(WO_OFF + co * WO_T), wzs = opq_s(WI_OFF + cz * WI_T);
      f32x4 acc[2][8];
      float mt[3], rt_[3], csn[3], hcn; f32x4 mn[3], rn[3], cst[5], ha[2][4], hcs;
      auto qkv = [&](auto l0) AI {
        constexpr bool L0 = l0.v;
        v4u A[2][20], Hh[5], B[8];
        int va[4], spo, xoff, hoff, fbo, cqo, wv, wr, wk; v4i dw;
        auto ldB = [&](auto fi) AI { constexpr int f = fi.v, t = f >> 3, ct = f & 7; B[f & 7] = lds16((ct < 7 ? vb[t & 3] : vh[t & 3]) + ((t >> 2) & 1) * 30720 + ct * 1024); };
        auto ldA = [&](auto ni) AI {
          constexpr int n = ni.v, t = q_step(n), i = (n % 9) & 1;
          if constexpr (n % 9 == 0) Hh[t >> 2] = ld16<SC1>(rws, va[0], hoff + (t >> 2) * 4096);
          else A[i][t] = ld16<SC1>(rws, va[t & 3], xoff + i * XT + (t >> 2) * 4096);
        };
        if constexpr (L0) {
          dw = mkd(wsp, 2048 * 4096);
          wv = opq_v((l >> 2) * 4096 + ((l ^ (0 - g)) & 3) * 16);
          wr = opq_v(24576 + w * 8192 + c16 * 64 + ((g ^ (0 - (c16 >> 2))) & 3) * 16);
          wk = opq_s(16 * w + ((MAPZ ? pz + 2 * cz + (cz >> 2) : 2 * pz + cz) & 7) * 2);
          fbo = opq_v(seloob(F0B + ((l >> 3) * 8 + (((l >> 3) + 1) & 2 ? cz : co)) * 256 + (l & 7) * 8, M_LO));
          cqo = opq_v((l >> 5) * 4096 + (l & 31) * 4);
          poll(rws, seloob(F0C + h * 256 + c16 * 8, M_G0), l >= 16, tag_lo, tag_hi);
          BAR();
          STAMP(sb - 1);
          cp_run<0, 8>(dws, l16, wq, qo); cp_run<0, 8>(dws, l16, wq + 30720, qo + 30720);
          const f32x2 a = (*lp<f32x2>(L_HST + c16 * 8) + *lp<f32x2>(L_HST + 128 + c16 * 8)) + (*lp<f32x2>(L_HST + 256 + c16 * 8) + *lp<f32x2>(L_HST + 384 + c16 * 8));
          const f32x2 v = finish_stat(a.x, a.y, 1.f / 640.f);
          *lp<float>(L_XST1 + w * 512 + 128 + c4) = v.x; *lp<float>(L_XST1 + w * 512 + 384 + c4) = v.y;
          WAITV(8);
          BAR0();
          sfor<0, 6>([&](auto fi) AI { ldB(IC<(fi.v ^ 1)>{}); SB(); });
        } else {
#pragma unroll
          for (int i = 0; i < 4; i++) va[i] = opq_v(l16 + ((i + w) & 3) * 1024);
          spo = opq_v(g < 2 ? (2 * w + 1 + g) * XT + c16 * 8 : (g == 2 && P > 0) ? c16 * 8 : OOB);
          xoff = opq_s(act + A_XIN + (8 * P + 2 * w) * XT); hoff = opq_s(P > 0 ? act + A_XIN + (8 * P - 1) * XT : OOB);
          sfor<0, 6>([&](auto fi) AI { ldB(IC<(fi.v ^ 1)>{}); SB(); });
          const bool on = l < 8 || (l < 16 && P > 0);
          poll(rws, on ? FLAY + F6 + (l < 8 ? 2 * P + w2 : 2 * P - 1) * 256 + (l & 7) * 8 : OOB, !on, tag_lo, tag_hi);
          STAMP(sb - 1);
          sfor<0, QPRE>(ldA);
          SB();
        }
        f32x4 hac[6];
        v2u sp[8], fb; float ss[8], sq[8]; int xa, xb;
        sfor<0, 160>([&](auto fi) AI {
          constexpr int f = fi.v, t = f >> 3, ct = f & 7, hc = ct < 3 ? ct : ct - 2, xn = (t >> 2) * 9 + 1 + (t & 3) * 2;
          constexpr bool halo = (t & 3) == 0 && (ct < 3 || ct >= 5);
          constexpr int fr = L0 ? -100 : q_free(f);
          constexpr int si = f == 33 ? 0 : f == 37 ? 1 : f == 39 ? 2 : f == 62 ? 3 : f == 65 ? 4 : f == 68 ? 5 : f == 69 ? 6 : f == 71 ? 7 : -1; (void)si;
          const v4u bb = B[f & 7];
          auto mm = [&](f32x4& d, const v4u& x) AI {
            if constexpr (ct < 5) mma<t == 0, L0>(d, bb, x); else if constexpr (L0) mma_aa<t == 0>(d, x, bb); else mma<t == 0, false>(d, x, bb);
            SB();
          };
          if constexpr (L0) mm(acc[0][ct], XA(xn + 1)); else mm(acc[0][ct], A[0][t]);
          if constexpr (L0 && f == 26) WAITV(l0_cnt(0, 26));
          if constexpr (L0 && f == 58) WAITV(l0_cnt(22, 58));
          if constexpr (L0 && f == 90) WAITV(l0_cnt(46, 90));
          if constexpr (f == 26 || (L0 && (f == 58 || f == 67 || f == 90)) || (!L0 && f == 122)) BAR0();
          if constexpr (L0 && f == 122) BAR();
          if constexpr (((f + 6) ^ 1) < 160) ldB(IC<((f + 6) ^ 1)>{});
          if constexpr ((t & 7) >= 4 && t < 16 && ct == 3) vb[t & 3] = gaddi<61440>(vb[t & 3]);
          if constexpr ((t & 7) >= 4 && t < 16 && ct == 5) vh[t & 3] = gaddi<61440>(vh[t & 3]);
          if constexpr (L0 && f % 12 == 0 && f < 72) cp_arm(qo + (2 + f / 24) * 30720 + (f / 12 & 1) * 4096);
          if constexpr (L0 && f >= 72 && f < 104 && f % 4 == 0) cp_arm(24576 + w * 8192 + (f - 72) * 256);
          if constexpr (fr == 0 || fr == 4) cp_arm(qo + 4 * 30720 + fr * 1024);
          if constexpr (f == 124 || f == 140) cp_arm(L_WO + w * 8192 + (f - 124) * 256);
          if constexpr (!L0) {
            if constexpr (f >= 96 && f < 104 && !(f & 1)) ss[(f - 96) / 2] = gadd(asf(sp[(f - 96) / 2].x), asf(sp[(f - 96) / 2 + 4].x));
            if constexpr (f == 104 || f == 106) ss[(f - 104) / 2] = gadd(ss[(f - 104) / 2], ss[(f - 104) / 2 + 2]);
            if constexpr (f == 108) ss[0] = gadd(ss[0], ss[1]);
            if constexpr (f == 110) { ss[0] = ss[0] * (1.f / 640.f); pin(ss[0]); }
            if constexpr (f == 111) ss[1] = gmul(ss[0], ss[0]);
            if constexpr (f == 113) { sq[0] = sq[0] * (1.f / 640.f) - ss[1]; pin(sq[0]); }
            if constexpr (f == 114) { sq[0] = fmaxf(sq[0], 0.f); pin(sq[0]); }
            if constexpr (f == 116) { sq[0] += 1e-5f; pin(sq[0]); }
            if constexpr (f == 118) { sq[0] = __builtin_amdgcn_rsqf(sq[0]); pin(sq[0]); }
            if constexpr (f == 119) xa = opq_v(L_XST1 + w * 512 + l * 4);
            if constexpr (f == 120) *lp<float>(xa) = ss[0];
            if constexpr (f == 121) *lp<float>(xa + 256) = sq[0];
          } else {
            if constexpr (f == 118) {
              const float cs = ((ss[0] + ss[1]) + (ss[2] + ss[3])) + ((ss[4] + ss[5]) + (ss[6] + ss[7]));
              const v2u t2 = sw32(asu(cs), asu(cs));
              *lp<float>(L_CSQ + w * 128 + (l & 31) * 4) = asf(t2.x) + asf(t2.y);
            }
          }
          if constexpr (f == 126) xb = opq_v(L_XST1 + w * 512 + g16);
          if constexpr (f >= 127 && f < 130) mn[f - 127] = ldsf4(xb + (f - 127) * 64);
          if constexpr (f == 130 || f == 131 || f == 133) rn[f == 133 ? 2 : f - 130] = ldsf4(xb + 256 + (f == 133 ? 2 : f - 130) * 64);
          if constexpr (f == 134) xa = opq_v(L_HALO + w * 6144 + l16);
          if constexpr (f == 142) xb = opq_v(L_CSQ + g16);
          if constexpr (f >= 143 && f < 148) cst[f - 143] = ldsf4(xb + (f - 143) * 64);
          if constexpr (f == 148) xb = opq_v(xb + w * 64);
          if constexpr (f == 149) hcs = ldsf4(xb);
          if constexpr (f == 150) xa = opq_v(L_HALO + w * 1024 + l16);
          if constexpr (f == 151) xb = opq_v(L_HALO + h2 * 1024 + l16);
          if constexpr (f >= 152 && f < 156) ha[0][f - 152] = ldsf4(xa + (f - 152) * 6144);
          SB();
          if constexpr (L0) mm(acc[1][ct], XA(xn)); else mm(acc[1][ct], A[1][t]);
          if constexpr (L0) {
            if constexpr (f % 3 == 0 && f < 72) cp_one<f / 3>(dws, l16, wq + (2 + f / 24) * 30720 + (f / 12 & 1) * 4096);
            if constexpr (f == 1) flag(F0B + jb * 256 + xcd * 8);
            if constexpr (f >= 72 && f < 104 && f % 4 == 0) {
              constexpr int n = (f - 72) / 4;
              asm volatile("buffer_load_dwordx4 %0, %1, %2 offen lds" ::"v"(wv), "s"(dw), "s"((64 * pz + 16 * ((w + (n >> 1)) & 3)) * 4096 + (wk + (n & 1)) * 64) : "memory");
            }
            if constexpr (f >= 73 && f < 105 && f % 4 == 2) ss[(f - 74) / 4] = asf(__builtin_amdgcn_raw_buffer_load_b32(rws, cqo + (f - 74) * 128, CSQ_OFF + h * 8192 + w * 128, SC1));
            if constexpr (f == 86) fb = ld8<SC1>(rws, fbo, 0);
            if constexpr (f == 119) { if (!__all(l >= 32 || (fb.x == tag_lo && fb.y == tag_hi))) poll(rws, fbo, l >= 32, tag_lo, tag_hi); }
            if constexpr (f == 120 || f == 121) sfor<0, 4>([&](auto ki) AI { constexpr int n = (f - 120) * 4 + ki.v; ldsa16<n * 1024>(W[n >> 1][n & 1], wr); });
            if constexpr (f >= 130 && f < 160 && f % 4 == 2) {
              constexpr int n = (f - 130) / 4;
              sta16<(n & 1) * 1024>(W[n >> 1][n & 1], dws, l16, WS_OFF + ((4 * pz + ((w + (n >> 1)) & 3)) * 64 + wk) * 1024);
            }
          } else {
            if constexpr (q_cnt(f) > q_cnt(f - 1)) ldA(IC<q_cnt(f - 1)>{});
            if constexpr (fr >= 0 && fr < 8) cp_one<fr & 7>(dws, l16, wq + 4 * 30720 + (fr & 4) * 1024);
            if constexpr (si >= 0) sp[si] = ld8<SC1>(rws, spo + si * 128, act + A_XIN + (8 * P - 1) * XT + 20480);
            if constexpr (f >= 96 && f < 104 && (f & 1)) sq[(f - 96) / 2] = gadd(asf(sp[(f - 96) / 2].y), asf(sp[(f - 96) / 2 + 4].y));
            if constexpr (f == 105 || f == 107) sq[(f - 105) / 2] = gadd(sq[(f - 105) / 2], sq[(f - 105) / 2 + 2]);
            if constexpr (f == 109) sq[0] = gadd(sq[0], sq[1]);
          }
          if constexpr (f == 123) xa = opq_v(L_XST1 + w * 512 + c4);
          if constexpr (!L0 && f == 123) asm volatile("" ::"v"(A[0][19]));
          if constexpr (f >= 124 && f < 156 && f % 4 == 0) cp_one<(f - 124) / 4>(dws, l16, wos + w * 8192 + (f - 124) / 16 * 4096);
          if constexpr (f == 125 || f == 127 || f == 131) { constexpr int i = f == 131 ? 2 : (f - 125) / 2; mt[i] = ldsf(xa + i * 64); rt_[i] = ldsf(xa + 256 + i * 64); }
          if constexpr (f >= 136 && f < 142) *lp<f32x4>(xa + (f - 136) * 1024) = hac[f - 136];
          if constexpr (f == 143) xa = opq_v(L_CSQ + c4);
          if constexpr (f == 144) { csn[0] = ldsf(xa + 320); csn[1] = ldsf(xa + 384); }
          if constexpr (f == 146) { csn[2] = ldsf(xa + 448); xa = opq_v(xa + (h2 + 2) * 64); }
          if constexpr (f == 148) hcn = ldsf(xa);
          if constexpr (f == 150) BAR0();
          if constexpr (f >= 152 && f < 156) ha[1][f - 152] = ldsf4(xb + (f - 152) * 6144);
          if constexpr (halo) { SB(); if constexpr (L0) mm(hac[hc], XA(xn - 1)); else mm(hac[hc], Hh[t >> 2]); }
          SB();
        });
        mma_settle();
#pragma unroll
        for (int j = 0; j < 8; j++) { pin(acc[0][j]); pin(acc[1][j]); }
      };
      if (L == 0) qkv(IC<1>{}); else qkv(IC<0>{});
      STAMP(sb + 0);
      const int nbw = opq_v(L_NB + l * 8), w2s = wos + 32768 + w * 8192, w2d = L_WO + 32768 + w * 8192, w3s = wos + 65536 + w * 9216, w3d = L_WO + 65536 + w * 9216;
      cp_run<0, 3>(dws, l16, w2s, w2d);
      {
        const f32x4 a0 = (ha[0][0] + ha[0][1]) + (ha[0][2] + ha[0][3]), a1 = (ha[1][0] + ha[1][1]) + (ha[1][2] + ha[1][3]);
        *lp<v2u>(nbw + (w < 3 ? w * 512 : 15360)) = pk4((a0 - mt[2] * hcs) * rt_[2]);
        *lp<v2u>(nbw + (w != 2 ? h2 * 512 : 15360)) = pk4((a1 - mn[2] * hcn) * rn[2]);
      }
      v2u tk[2][8];
      auto fix = [&](int i, int ct) AI { tk[i][ct] = ct < 5 ? pk4((acc[i][ct] - mt[i] * cst[ct]) * rt_[i]) : pk4((acc[i][ct] - mn[i] * csn[ct - 5]) * rn[i]); };
      fix(1, 0); fix(1, 1); fix(1, 2); fix(1, 5); fix(1, 6); fix(1, 7);
      const int nbr = opq_v(nbw + w * 3072);
#pragma unroll
      for (int i = 0; i < 6; i++) *lp<v2u>(nbr + 3072 + i * 512) = tk[1][i < 3 ? i : i + 2];
      cp_run<3, 6>(dws, l16, w2s, w2d);
      SB();
      fix(0, 0); fix(0, 1); fix(0, 2); fix(1, 3); fix(1, 4);
      BAR();
      v2u nb[6];
#pragma unroll
      for (int i = 0; i < 6; i++) { nb[i] = lds8(nbr + i * 512); asm volatile("" ::: "memory"); }
      cp_run<6, 8>(dws, l16, w2s, w2d);
      STAMP(sb + 1);
      SB();
      fix(0, 3); fix(0, 4);
      v6u K6[3]; v4u Q[2][2];
      {
        const v2u qx = sw32(tk[0][2].x, tk[1][2].x), qy = sw32(tk[0][2].y, tk[1][2].y);
        const unsigned q1x = sw32(qx.y, qx.y).y, q1y = sw32(qy.y, qy.y).y;
        Q[0][0] = v4u{tk[0][3].x, tk[0][3].y, sel0(qx.y, M_G0), sel0(qy.y, M_G0)};
        Q[0][1] = v4u{sel0(qx.y, M_G1), sel0(qy.y, M_G1), tk[0][4].x, tk[0][4].y};
        Q[1][0] = v4u{tk[1][3].x, tk[1][3].y, sel0(q1x, M_G0), sel0(q1y, M_G0)};
        Q[1][1] = v4u{sel0(q1x, M_G1), sel0(q1y, M_G1), tk[1][4].x, tk[1][4].y};
      }
#pragma unroll
      for (int i = 0; i < 2; i++) {
        K6[i + 1] = v6u{tk[i][0].x, tk[i][0].y, tk[i][2].x, tk[i][2].y, tk[i][1].x, tk[i][1].y};
        pin(Q[i][0]); pin(Q[i][1]); pin(K6[i + 1]);
      }
      K6[0] = v6u{nb[0].x, nb[0].y, nb[2].x, nb[2].y, nb[1].x, nb[1].y};
      pin(K6[0]);
      NOP2();
      f32x4 S[4][2];
#pragma unroll
      for (int u = 3; u >= 0; u--)
#pragma unroll
        for (int k = 1; k >= 0; k--) {
          const v6u kk = K6[(u >> 1) + k];
          mma<true, false>(S[u][k], (u & 1) ? __builtin_shufflevector(kk, kk, 2, 3, 4, 5) : __builtin_shufflevector(kk, kk, 0, 1, 2, 3), Q[u >> 1][u & 1]);
        }
      SB();
      cp_run<0, 3>(dws, l16, w3s, w3d);
      unsigned sel[4];
      {
        const unsigned zz = 0x0c0c0c0cu, a = ::sel(0x0c0c0504u, 0x05040c0cu, M_ODD), d = ::sel(0x0c0c0706u, 0x07060c0cu, M_ODD);
        sel[2] = ::sel(zz, a, le_mask(0)); sel[3] = ::sel(zz, d, le_mask(2));
        sel[0] = ::sel(a, zz, le_mask(0)); sel[1] = ::sel(d, zz, le_mask(2));
      }
      const int ovo = (g & 1) * 768 + (g >> 1) * 256 + c16 * 16, ovt = seloob(g * OT + 512 + c16 * 16, M_LO);
      fix(0, 5); fix(0, 6); fix(0, 7);
      v6u V6[3];
#pragma unroll
      for (int j = 0; j < 3; j++) { V6[j] = v6u{nb[3 + j].x, nb[3 + j].y, tk[0][5 + j].x, tk[0][5 + j].y, tk[1][5 + j].x, tk[1][5 + j].y}; pin(V6[j]); }
      SB();
#pragma unroll
      for (int u = 0; u < 8; u++) pin(S[u >> 1][u & 1]);
      if (P + w == 0) { S[0][0] = f32x4{-1e30f, -1e30f, -1e30f, -1e30f}; pin(S[0][0]); S[1][0] = S[0][0]; }
      float s0[4], s1[4], mx[4];
#pragma unroll
      for (int u = 0; u < 4; u++) {
        const f32x4 a = S[u][0], bq = S[u][1];
        s0[u] = self(self(a[0], a[1], M_ODD), self(bq[0], bq[1], M_ODD), le_mask(0));
        s1[u] = self(self(a[2], a[3], M_ODD), self(bq[2], bq[3], M_ODD), le_mask(2));
        mx[u] = vmax(s0[u], s1[u]);
      }
      auto spread = [&](float t, float (&o)[4]) AI {
        const v2u a = sw16(asu(t), asu(t)), x = sw32(a.x, a.x), y = sw32(a.y, a.y);
        o[0] = asf(x.x); o[1] = asf(x.y); o[2] = asf(y.x); o[3] = asf(y.y);
      };
      const float kk = 0.22360679774997896f * 1.4426950408889634f;
      {
        const v2u a = sw32(asu(mx[0]), asu(mx[1])), bq = sw32(asu(mx[2]), asu(mx[3]));
        const v2u d = sw16(asu(vmax(asf(a.x), asf(a.y))), asu(vmax(asf(bq.x), asf(bq.y))));
        spread(vmax(asf(d.x), asf(d.y)), mx);
      }
      SB();
      cp_run<3, 6>(dws, l16, w3s, w3d);
      float sm4[4];
#pragma unroll
      for (int u = 0; u < 4; u++) {
        const f32x2 e = (f32x2{s0[u], s1[u]} - mx[u]) * kk;
        s0[u] = __builtin_amdgcn_exp2f(e.x); s1[u] = __builtin_amdgcn_exp2f(e.y);
        sm4[u] = s0[u] + s1[u];
      }
      {
        const v2u a = sw32(asu(sm4[0]), asu(sm4[1])), bq = sw32(asu(sm4[2]), asu(sm4[3]));
        const v2u d = sw16(asu(asf(a.x) + asf(a.y)), asu(asf(bq.x) + asf(bq.y)));
        spread(__builtin_amdgcn_rcpf(asf(d.x) + asf(d.y)), sm4);
      }
      v4u pf[4];
#pragma unroll
      for (int u = 0; u < 4; u++) {
        const f32x2 pn = f32x2{s0[u], s1[u]} * sm4[u];
        const unsigned pp = pk(pn.x, pn.y);
        pf[u] = v4u{__builtin_amdgcn_perm(pp, 0u, sel[0]), __builtin_amdgcn_perm(pp, 0u, sel[1]), __builtin_amdgcn_perm(pp, 0u, sel[2]), __builtin_amdgcn_perm(pp, 0u, sel[3])};
        pin(pf[u]);
      }
      NOP2();
      f32x4 Om[4], Ot[4];
#pragma unroll
      for (int u = 0; u < 4; u++) { const v6u v = V6[u & 1]; mma<true, false>(Om[u], (u >> 1) ? __builtin_shufflevector(v, v, 2, 3, 4, 5) : __builtin_shufflevector(v, v, 0, 1, 2, 3), pf[u]); }
#pragma unroll
      for (int u = 0; u < 4; u++) { const v6u v = V6[2]; mma<true, false>(Ot[u], (u >> 1) ? __builtin_shufflevector(v, v, 2, 3, 4, 5) : __builtin_shufflevector(v, v, 0, 1, 2, 3), pf[u]); }
      asm volatile("s_nop 3");
#pragma unroll
      for (int u = 0; u < 4; u++) pin(Om[u]);
      const int so = act + A_O + (8 * P + 2 * w) * OT + 5 * h * 256;
      st16(join16(pk4(Om[0]), pk4(Om[1])), rws, ovo, so);
      st16(join16(pk4(Om[2]), pk4(Om[3])), rws, ovo, so + OT);
      SB();
#pragma unroll
      for (int u = 0; u < 4; u++) pin(Ot[u]);
      {
        const v2u x0_ = pk4(Ot[0]), y0_ = pk4(Ot[1]), x1_ = pk4(Ot[2]), y1_ = pk4(Ot[3]);
        st16(v4u{sw16(x0_.x, x1_.x).x, sw16(x0_.y, x1_.y).x, sw16(y0_.x, y1_.x).y, sw16(y0_.y, y1_.y).y}, rws, ovt, so);
      }
      STAMP(sb + 2);
      cp_run<6, 9>(dws, l16, w3s, w3d);
      cp_run<0, 6>(dws, l16, wzs + w * 6144, w * 6144);
      WAITV(9);
      BAR();
      flag(fl + F1 + P * 256 + h * 8);
      flag((F0S + pz * 256 + cz * 8) | kill1);
      STAMP(sb + 3);
      touch(dws, (g < 2 && c16 < 10) ? g * OT + c16 * 128 : OOB, so);
      cp_run<0, 8>(dws, l16, wzs + 24576 + w * 8192, 24576 + w * 8192);
    }
    { LANE();
      f32x4 acc[2][5];
      v2u rr[5];
      const int tile = opq_s(act + A_X1 + (4 * po + w) * XT), wzs = opq_s(WI_OFF + cz * WI_T);
      if (L == 0) {
        const rsrc_t rx = mk(x0, 2048 * 1280);
#pragma unroll
        for (int j = 0; j < 5; j++) rr[j] = ld8<0>(rx, c16 * 1280 + g * 8 + j * 32, (64 * po + 16 * w) * 1280 + co * 160);
        float cs = 0.f;
#pragma unroll
        for (int i = 0; i < 16; i++) cs += asf(__builtin_amdgcn_raw_buffer_load_b32(rws, l * 4 + (i & 7) * 512, CSI_OFF + (w2 * 8 + cz) * 8192 + kh * 256 + (i >> 3) * 4096, SC1));
        *lp<float>(L_CS2 + (w * 64 + l) * 4) = cs;
      } else {
#pragma unroll
        for (int j = 0; j < 5; j++) rr[j] = ld8<SC1>(rws, (g >> 1) * 256 + c16 * 16 + (g & 1) * 8 + j * 512, tile + (A_XIN - A_X1) + co * 2560);
      }
      const int vo = opq_v((g & 1) * 512 + (g >> 1) * 256 + c16 * 16), vo4 = opq_v(seloob(vo, M_GEVEN)), vs = opq_v(seloob(c16 * 8 + (g >> 1) * 4, M_GEVEN));
      const int xw = opq_v(L_X2 + (w ^ 1) * 5120 + l16), xr = opq_v(L_X2 + w * 5120 + l16);
      const int a0 = opq_s(act + A_O + (4 * po + w) * OT + kh * 10240), a1 = opq_s(act + A_O + (4 * po + (w ^ 1)) * OT + kh * 10240);
      const int b0 = opq_v(L_WO + l16 + kh * 5120), b1 = opq_v(b0 + 51200);
      const bool pon = l < 8 || (l < 16 && L == 0);
      const int pv = opq_v(pon ? (l < 8 ? fl + F1 + (po >> 1) * 256 + kh * 64 : F0S + pz * 256 - 64) + l * 8 : OOB);
      v4u A[2][10];
      gemm_m2k2<10, 5, true, 5, true>([&](auto ti) AI { return ti.v < 5 ? b0 + ti.v * 10240 : b1 + (ti.v - 5) * 10240; }, xw, A, acc,
          [&]() AI {
#pragma unroll
        for (int j = 0; j < 5; j++) { acc[0][j] = unpk4(rr[j]); pin(acc[0][j]); }
        poll(rws, pv, !pon, tag_lo, tag_hi);
        STAMP(sb + 4);
      },
          [&](auto ii, auto ti) AI { return ld16<SC1>(rws, l16 + (ti.v & 3) * 1024, (ii.v ? a1 : a0) + (ti.v >> 2) * 4096); },
          [&](auto fi) AI { if constexpr (fi.v == 13) BAR0(); },
          [&](auto) AI {});
      STAMP(sb + 5);
      BAR();
      xchg_read<5>(xr, acc);
      store_x(acc[0], rws, tile + co * 2560, tile + 20480 + co * 128, vo, vo4, vs);
      cp_run<0, 8>(dws, l16, wzs + 71680 + w * 10240, L_Z2B + 71680 + w * 10240);
      WAITV(8);
      BAR();
      flag(fl + F2 + po * 256 + co * 8);
      STAMP(sb + 6);
      touch(dws, l < 20 ? co * 2560 + l * 128 : l == 20 ? 20480 + co * 128 : OOB, tile);
      cp_run<0, 4>(dws, l16, wzs + 57344 + ((w * 3584) & ~1023), L_Z2B + 57344 + ((w * 3584) & ~1023));
      cp_run<8, 10>(dws, l16, wzs + 71680 + w * 10240, L_Z2B + 71680 + w * 10240);
      cp_run<0, 8>(dws, l16, wzs + 112640 + qslot_off(w), L_Z2B + 112640 + qslot_off(w));
    }
    { LANE(); WSP();
      const int wz1 = opq_s(WI_OFF + (8 + cz) * WI_T);
      const int a0 = opq_s(act + A_X1 + (4 * pz + w) * XT + kh * 10240), a1 = opq_s(act + A_X1 + (4 * pz + (w ^ 1)) * XT + kh * 10240);
      int xw = opq_v(L_X3 + (w ^ 1) * 7168 + l16), xr = opq_v(L_X3 + w * 7168 + l16);
      const int gso = opq_v(seloob(l * 4, 0x1111111111111111ull)), gro = opq_v(((l >> 2) & 3) * 256 + (4 * g + (l & 3)) * 16);
      const int zvo = opq_v((g & 1) * 1024 + (g >> 1) * 256 + c16 * 16), spo = opq_v(g * 256 + c16 * 8);
      const int sta = opq_v(L_XST3 + w * 128 + (g & 1) * 64 + c16 * 4), stb = opq_v(L_XST3 + w * 128 + g * 16), tv = opq_v(l * 128);
      const int b0 = opq_v(l16 + kh * 7168), b1 = opq_v(b0 + L_Z1B + 3 * 14336), b2 = opq_v(b1 + 4 * 14336);
      static_assert(L_Z1B == L_Z2B, "");
      const int sps = opq_s(act + A_X1 + (4 * pz + w) * XT + 20480), za0 = opq_s((w * 10752) & ~1023);
      float cs2[7];
#pragma unroll
      for (int j = 0; j < 7; j++) { cs2[j] = ldsf(L_CS2 + c16 * 4 + j * 64); pin(cs2[j]); }
      braw = __builtin_amdgcn_raw_buffer_load_b16(mk(bsp, 4096), c16 * 2, (64 * pz + 16 * w) * 2, 0);
      v4u A[2][10]; f32x4 acc[2][7];
      v2u sp0, sp1, u_;
      float m1, r1, st_; f32x4 m4, r4;
      gemm_m2k2<10, 7, false, 5, false>([&](auto ti) AI { return ti.v < 4 ? b0 + ti.v * 14336 : ti.v < 7 ? b1 + (ti.v - 3) * 14336 : b2 + (ti.v - 7) * 14336; }, xw, A, acc,
          [&]() AI { poll(rws, seloob(fl + F2 + pz * 256 + l * 8, 0xFFull), l >= 8, tag_lo, tag_hi); pin(braw); STAMP(sb + 7); },
          [&](auto ii, auto ti) AI { return ld16<SC1>(rws, l16 + (ti.v & 3) * 1024, (ii.v ? a1 : a0) + (ti.v >> 2) * 4096); },
          [&](auto fi) AI {
        constexpr int f = fi.v;
        if constexpr (f == 15 || f == 31) BAR0();
        if constexpr (f == 46 || f == 54 || f == 62) cp_arm(za0 + (f - 46) * 512);
        if constexpr (f == 47) { u_ = sw32(asu(m1), asu(r1)); pin(u_); }
        if constexpr (f == 48) { st_ = gadd(asf(u_.x), asf(u_.y)); }
        if constexpr (f == 49) { u_ = sw16(asu(st_), asu(st_)); pin(u_); }
        if constexpr (f == 50) { st_ = gadd(asf(u_.x), asf(u_.y)); }
        if constexpr (f == 51) { u_ = sw32(asu(st_), asu(st_)); pin(u_); }
        if constexpr (f == 52) { m1 = asf(u_.x) * (1.f / 640.f); pin(m1); }
        if constexpr (f == 53) { st_ = gmul(m1, m1); }
        if constexpr (f == 55) { r1 = asf(u_.y) * (1.f / 640.f) - st_; pin(r1); }
        if constexpr (f == 56) { r1 = fmaxf(r1, 0.f); pin(r1); }
        if constexpr (f == 57) { r1 += 1e-5f; pin(r1); }
        if constexpr (f == 58) { r1 = __builtin_amdgcn_rsqf(r1); pin(r1); }
        if constexpr (f == 60) *lp<float>(sta) = self(r1, m1, M_GEVEN);
        if constexpr (f == 63) cp_one<1>(dws, l16, wz1 + za0 + 8192);
        if constexpr (f == 65) { m4 = ldsf4(stb); r4 = ldsf4(stb + 64); }
        if constexpr (f == 67) cp_one<2>(dws, l16, wz1 + za0 + 8192);
      },
          [&](auto fi) AI {
        constexpr int f = fi.v;
        if constexpr (f == 20) sp0 = ld8<SC1>(rws, spo, sps);
        if constexpr (f == 22) sp1 = ld8<SC1>(rws, spo + 128, sps);
        if constexpr (f == 44) { m1 = gadd(asf(sp0.x), asf(sp1.x)); }
        if constexpr (f == 45) { r1 = gadd(asf(sp0.y), asf(sp1.y)); }
        if constexpr (f >= 46 && !(f & 1)) cp_one<(f - 46) / 2>(dws, l16, wz1 + za0 + (f - 46) / 8 * 4096);
      });
      STAMP(sb + 8);
      BAR();
      xchg_read<7>(xr, acc);
      BAR();
      f32x4 z2[7], zs, zq;
      const int gs = wz1 + 43008 + w * 14336, gd = L_Z1B + 43008 + w * 14336;
      auto gel = [&](auto l0) AI {
        sfor<0, 7>([&](auto ji) AI {
          constexpr int j = ji.v;
          cp_run<2 * j, 2 * j + 2>(dws, l16, gs, gd);
          if constexpr (l0.v && j < 6) sfor<8 + 2 * j, 10 + 2 * j>(wsp_ld);
          SB();
          f32x4 v = gelu4((acc[0][j] - m4 * cs2[j]) * r4);
          if constexpr (j == 6) v *= cz < 6 ? 1.f : 0.f;
          z2[j] = v;
          if (j == 0) { zs = v; zq = v * v; } else { zs += v; zq += v * v; }
          pin(zs); pin(zq);
          SB();
        });
      };
      if (L == 0) gel(IC<1>{}); else gel(IC<0>{});
      const int sto = ST_OFF + L * ST_LAY + pz * 8192 + w * 2048;
      {
        const f32x2 t = rowsum8(zs, zq);
        st16(v4u{asu(t.x), GTAG, asu(t.y), GTAG}, rws, gso, sto + cz * 256);
      }
      STAMP(sb + 9);
      if (L == 0) sfor<20, 30>(wsp_ld);
      float ts, tq;
      {
        v4u ga, gb;
        for (unsigned spins = 0; spins < (1u << 18); ++spins) {
          asm volatile("" ::: "memory");
          ga = ld16<SC1>(rws, gro, sto); gb = ld16<SC1>(rws, gro + 1024, sto);
          if (__all(ga.y == GTAG && ga.w == GTAG && gb.y == GTAG && gb.w == GTAG)) break;
        }
        ts = asf(ga.x) + asf(gb.x); tq = asf(ga.z) + asf(gb.z);
        ts += dpp<0x128>(ts); tq += dpp<0x128>(tq); ts += dpp<0x124>(ts); tq += dpp<0x124>(tq);
      }
      STAMP(sb + 10);
      const int zo = act + A_Z + (cz * 64 + 2 * pz) * 7168;
      {
        f32x2 zz = finish_stat(ts, tq, 1.f / 864.f);
        zz.x = -zz.x * zz.y;
        const f32x4 mm = {dpp<0x00>(zz.x), dpp<0x55>(zz.x), dpp<0xAA>(zz.x), dpp<0xFF>(zz.x)};
        const f32x4 rr = {dpp<0x00>(zz.y), dpp<0x55>(zz.y), dpp<0xAA>(zz.y), dpp<0xFF>(zz.y)};
        const int so = zo + w2 * 7168 + kh * 512;
        v2u d[7];
#pragma unroll
        for (int j = 0; j < 7; j++) d[j] = pk4(z2[j] * rr + mm);
        st16(join16(d[0], d[1]), rws, zvo, so);
        st16(join16(d[2], d[3]), rws, zvo + 2048, so);
        st16(join16(d[4], d[5]), rws, zvo, so + 4096);
        st16(join16(d[6], d[6]), rws, seloob(zvo, M_GEVEN) + 2048, so + 4096);
      }
      cp_run<0, 8>(dws, l16, wz1 + 100352 + za0, L_Z1B + 100352 + za0);
      WAITV(8);
      BAR();
      flag(fl + F4 + cz * 256 + pz * 8);
      STAMP(sb + 11);
      touch(dws, seloob(tv, (1ull << 28) - 1), zo + w * 3584);
      cp_run<8, 11>(dws, l16, wz1 + 100352 + za0, L_Z1B + 100352 + za0);
      const int us = opq_s(WU_OFF + co * WU_T), ua = opq_s((w * 12800) & ~1023), ub = opq_s(51200 + w * 10240);
      f32x4 cs1[7];
      int tmp;
      xw += L_X3B - L_X3; xr += L_X3B - L_X3;
      v2u fz;
      const int fzo = opq_v(seloob(fl + F4 + cz * 256 + w * 64 + l * 8, 0xFFull));
      auto z1gemm = [&](auto l0) AI {
        constexpr bool L0 = l0.v;
        gemm_m2k2<10, 7, true, 0, false>([&](auto ti) AI { return ti.v < 3 ? b0 + ti.v * 14336 : ti.v < 7 ? b1 + (ti.v - 3) * 14336 : b2 + (ti.v - 7) * 14336; }, xw, A, acc,
            [&]() AI {}, [&](auto, auto) AI { return v4u{}; },
            [&](auto fi) AI {
          constexpr int f = fi.v;
          if constexpr (f == 22) { WAITV(L0 ? 22 : 0); BAR0(); }
          if constexpr (f == 43 || f == 57) BAR0();
          if constexpr (f >= 23 && f < 49 && (f - 23) % 8 == 0) cp_arm(ua + (f - 23) * 512);
          if constexpr (f >= 49 && f < 59 && (f - 49) % 4 == 0) cp_arm(ub + (f - 49) * 1024);
          if constexpr (f == 3) tmp = opq_v(L_CS1 + g * 16);
          if constexpr (f >= 5 && f < 19 && (f & 1)) cs1[(f - 5) / 2] = ldsf4(tmp + (f - 5) * 32);
          if constexpr (f == 63) fz = ld8<SC1>(rws, fzo, 0);
        },
            [&](auto fi) AI {
          constexpr int f = fi.v;
          if constexpr (L0 && f < 22) wsp_ld(IC<30 + f>{});
          if constexpr (f >= 24 && f < 49 && !(f & 1)) cp_one<(f - 24) / 2>(dws, l16, us + ua + (f - 24) / 8 * 4096);
          if constexpr (L0 && f >= 25 && f < 49 && (f & 1)) wsp_ld(IC<52 + (f - 25) / 2>{});
          if constexpr (f >= 49 && f < 59) cp_one<f - 49>(dws, l16, us + ub + (f - 49) / 4 * 4096);
        });
      };
      if (L == 0) z1gemm(IC<1>{}); else z1gemm(IC<0>{});
      STAMP(sb + 12);
      BAR();
      xchg_read<7>(xr, acc);
      if (!__all(l >= 8 || (fz.x == tag_lo && fz.y == tag_hi))) poll(rws, fzo, l >= 8, tag_lo, tag_hi);
      FENCE();
      STAMP(sb + 13);
      const int z0 = act + A_Z + (cz * 64 + 16 * w + (zr & 7) * 2) * 7168;
      auto ldZ0 = [&](auto fi) AI { Z[fi.v] = ld16<SC1>(rws, l16 + (fi.v & 3) * 1024, z0 + (fi.v >> 2) * 4096); };
      sfor<0, 7>([&](auto ji) AI {
        constexpr int j = ji.v;
        if constexpr (j < 4) sfor<3 * j, 3 * j + 3>(ldZ0);
        if constexpr (j == 4) sfor<12, 14>(ldZ0);
        SB();
        z1[j] = gelu4((acc[0][j] - m1 * cs1[j]) * r1);
        pin(z1[j]);
        SB();
      });
    }
    { LANE();
      const int ct0 = cz < 6 ? 7 * cz : 6 + 6 * cz, zr = MAPZ ? pz + 2 * cz + (cz >> 2) : 2 * pz + cz;
      STAMP(sb + 14);
      f32x4 acc[4][7];
      const int so = act + A_G + (4 * pz + w) * GT + ct0 * 512;
      f32x4 bias = blo(braw);
      pin(bias);
      int zp[8], za, rw[3], rd, vo; unsigned d[4];
      constexpr int RN = RZ + 1;
      v4u Y[RN];
      sfor<0, RZ>([&](auto fi) AI { Y[fi.v] = Z[fi.v]; });
      const int zb = opq_s(act + A_Z + (cz * 64 + 16 * w) * 7168);
      zp[1] = opq_s(zb + ((1 + zr) & 7) * 14336);
      NOP2();
      SB();
      sfor<0, 112>([&](auto fi) AI {
        constexpr int f = fi.v, s = sp_s(f), j = sp_j(f), fn = f + RZ;
        constexpr int q = f < 42 ? -1 : (f - 42) / 28, k = f < 42 ? 0 : (f - 42) % 28, buf = (q & 1) ? L_RED1 - L_RED0 : 0;
        auto fin = [&](auto ui) AI {
          constexpr int u = ui.v;
          if constexpr (q >= 0 && u >= 0 && u < 36) {
            constexpr int jj = 2 * q + (u < 32 ? (u >> 2) & 1 : (u - 32) >> 1), e = u & 3, st = u >> 3;
            if constexpr (st == 0) acc[0][jj][e] = gadd(acc[0][jj][e], acc[1][jj][e]);
            if constexpr (st == 1) acc[2][jj][e] = gadd(acc[2][jj][e], acc[3][jj][e]);
            if constexpr (st == 2) acc[0][jj][e] = gadd(acc[0][jj][e], acc[2][jj][e]);
            if constexpr (st == 3) acc[0][jj][e] = gmul(acc[0][jj][e], z1[jj][e]);
            if constexpr (st == 4) d[u - 32] = gcvt(acc[0][jj][(u & 1) * 2], acc[0][jj][(u & 1) * 2 + 1]);
          }
        };
        constexpr int per = q == 2 ? 7 : 4, u0 = (k - (q == 2 ? 9 : 10)) * per;
        const v4u z = Y[f % RN];
        mma<s == 0, true>(acc[1][j], z, W[1][s]);
        SB();
        if constexpr (fn < 112 && (fn >= 98 || !(fn & 1))) za = opq_s(zp[sp_s(fn) / 2] + ((sp_s(fn) & 1) * 7 + (sp_j(fn) & 6)) * 1024);
        if constexpr (q >= 0 && k >= 1 && k < 4) *lp<f32x4>(rw[k - 1] + buf) = acc[k][2 * q];
        if constexpr (q >= 0 && k >= 6 && k < 9) acc[k - 5][2 * q] = ldsf4(rd + buf + (k - 6) * 2048);
        fin(IC<u0>{});
        if constexpr (q == 2) fin(IC<u0 + 1>{});
        SB();
        mma<s == 0, true>(acc[2][j], z, W[2][s]);
        SB();
        if constexpr (fn < 112) Y[fn % RN] = ld16<SC1>(rws, l16 + (sp_j(fn) & 1) * 1024, za);
        if constexpr (q == 2) { fin(IC<u0 + 2>{}); fin(IC<u0 + 3>{}); }
        SB();
        mma<s == 0, true>(acc[3][j], z, W[3][s]);
        SB();
        if constexpr (q >= 0 && k >= 2 && k < 5) *lp<f32x4>(rw[k - 2] + buf + 1024) = acc[k - 1][2 * q + 1];
        if constexpr (q >= 0 && k >= 6 && k < 9) acc[k - 5][2 * q + 1] = ldsf4(rd + buf + (k - 6) * 2048 + 1024);
        fin(IC<u0 + per - 3>{}); fin(IC<u0 + per - 2>{});
        if constexpr (f % 4 == 1 && f / 4 + 2 < 8) zp[f / 4 + 2] = opq_s(zb + ((f / 4 + 2 + zr) & 7) * 14336);
        if constexpr (f >= 28 && f < 31) rw[f - 28] = opq_v(L_RED0 + ((((w + f - 27) & 3) * 3 + f - 28) * 2) * 1024 + l16);
        if constexpr (f == 31) rd = opq_v(L_RED0 + w * 6144 + l16);
        if constexpr (f == 32) vo = opq_v((g & 1) * 512 + c16 * 16);
        if constexpr (f == 34) vo = opq_v(vo + (g >> 1) * 256);
        if constexpr (q >= 0 && q < 2 && k == 20) { const v2u a = sw16(d[0], d[2]), e = sw16(d[1], d[3]); d[0] = a.x; d[2] = a.y; d[1] = e.x; d[3] = e.y; pin(d[0]); pin(d[1]); }
        if constexpr (q >= 0 && q < 2 && k == 21) st16(v4u{d[0], d[1], d[2], d[3]}, rws, vo + q * 1024, so);
        if constexpr (f == 38) BAR0();
        if constexpr (q >= 0 && k == 5) BAR();
        SB();
        if constexpr (s == 0) mma_c(acc[0][j], z, W[0][s], bias); else mma<false, true>(acc[0][j], z, W[0][s]);
        SB();
        fin(IC<u0 + per - 1>{});
        SB();
      });
      d[3] = gcvt(acc[0][5][2], acc[0][5][3]);
      const int rw2 = opq_v(L_RED2 + l16);
      asm volatile("s_nop 2" ::: "memory");
#pragma unroll
      for (int i = 1; i < 4; i++) pin(acc[i][6]);
      STAMP(sb + 15);
#pragma unroll
      for (int r = 1; r < 4; r++) *lp<f32x4>(rw2 + (((w + r) & 3) * 3 + r - 1) * 1024) = acc[r][6];
      st16(join16(v2u{d[0], d[1]}, v2u{d[2], d[3]}), rws, vo + 2048, so);
      BAR();
#pragma unroll
      for (int r = 1; r < 4; r++) acc[r][6] = ldsf4(rw2 + (w * 3 + r - 1) * 1024);
      pin(acc[0][6]);
      {
        const v2u dd = pk4(((acc[0][6] + acc[1][6]) + (acc[2][6] + acc[3][6])) * z1[6]);
        st16(join16(dd, dd), rws, seloob(vo, M_GEVEN) + 3072, cz < 6 ? so : OOB);
      }
      const int uc = 92160 + w * 11264;
      cp_run<0, 8>(dws, l16, WU_OFF + co * WU_T + uc, uc);
      WAITV(8);
      BAR();
      flag(fl + F5 + pz * 256 + cz * 8);
      STAMP(sb + 16);
      touch(dws, l < (cz < 6 ? 28 : 24) ? l * 128 : OOB, so);
      cp_run<8, 12>(dws, l16, WU_OFF + co * WU_T + uc, uc);
    }
    { LANE();
      f32x4 acc[2][5];
      v2u rr[5];
#pragma unroll
      for (int j = 0; j < 5; j++) rr[j] = ld8<SC1>(rws, (g >> 1) * 256 + c16 * 16 + (g & 1) * 8 + j * 512, act + A_X1 + (4 * po + w) * XT + co * 2560);
      const int vo = opq_v(L == 0 ? (g & 1) * 512 + (g >> 1) * 256 + c16 * 16 : c16 * 1280 + (g & 1) * 32 + (g >> 1) * 16);
      const int vo4 = opq_v(seloob(vo, M_GEVEN)), vs = opq_v(seloob(c16 * 8 + (g >> 1) * 4, M_GEVEN));
      const int xw = opq_v(L_X5 + (w ^ 1) * 5120 + l16), xr = opq_v(L_X5 + w * 5120 + l16);
      const int a0 = opq_s(act + A_G + (4 * po + w) * GT + kh * 14336), a1 = opq_s(act + A_G + (4 * po + (w ^ 1)) * GT + kh * 14336);
      const int k0 = opq_s(kh ? OOB : a0 + 12288), k1 = opq_s(kh ? OOB : a1 + 12288);
      const int b0 = opq_v(l16 + kh * 5120), b1 = opq_v(b0 + 61440), b2 = opq_v(b0 + 122880), b3 = opq_v(l16 + 133120);
      const int qo = opq_s(qslot_off(w)), wq = opq_s((WQ_OFF + h * WQ_T + qo) | kill1);
      const int tile = opq_s(ACT_OFF + ACT_LAY + A_XIN + (4 * po + w) * XT);
      const int pv = opq_v(seloob(fl + F5 + po * 256 + kh * 32 + l * 8, 0xFull));
      v4u A[2][14];
      gemm_m2k2<14, 5, true, 5, true>([&](auto ti) AI { return ti.v < 6 ? b0 + ti.v * 10240 : ti.v < 12 ? b1 + (ti.v - 6) * 10240 : ti.v == 12 ? b2 : b3; }, xw, A, acc,
          [&]() AI {
#pragma unroll
        for (int j = 0; j < 5; j++) { acc[0][j] = unpk4(rr[j]); pin(acc[0][j]); }
        poll(rws, pv, l >= 4, tag_lo, tag_hi);
        STAMP(sb + 17);
      },
          [&](auto ii, auto ti) AI { return ld16<SC1>(rws, l16 + (ti.v & 3) * 1024, ti.v == 13 ? (ii.v ? k1 : k0) : (ii.v ? a1 : a0) + (ti.v >> 2) * 4096); },
          [&](auto fi) AI {
        constexpr int f = fi.v;
        if constexpr (f == 21) BAR0();
        if constexpr (f == 53) { asm volatile("" ::"v"(A[0][13])); cp_arm(qo); }
        if constexpr (f == 61) cp_arm(qo + 4096);
        if constexpr (f == 66 || f == 68) cp_one<(f - 54) / 2>(dws, l16, wq + 4096);
      },
          [&](auto fi) AI {
        constexpr int f = fi.v;
        if constexpr (f >= 54 && !(f & 1)) cp_one<(f - 54) / 2>(dws, l16, wq + (f - 54) / 8 * 4096);
      });
      STAMP(sb + 18);
      BAR();
      xchg_read<5>(xr, acc);
      if (L == 1) {
        const rsrc_t ro_ = mk(out, 2048 * 1280);
        v2u d[5];
#pragma unroll
        for (int j = 0; j < 5; j++) d[j] = pk4(acc[0][j]);
        const int so = (64 * po + 16 * w) * 1280 + co * 160;
        __builtin_amdgcn_raw_buffer_store_b128(join16(d[0], d[1]), ro_, vo, so, 0);
        __builtin_amdgcn_raw_buffer_store_b128(join16(d[2], d[3]), ro_, vo + 64, so, 0);
        __builtin_amdgcn_raw_buffer_store_b128(join16(d[4], d[4]), ro_, vo4 + 128, so, 0);
        flag(b == 0 ? 0 : OOB);
        __builtin_amdgcn_endpgm();
      }
      store_x(acc[0], rws, tile + co * 2560, tile + 20480 + co * 128, vo, vo4, vs);
      cp_run<0, 8>(dws, l16, wq + 30720, qo + 30720);
      WAITV(8);
      BAR();
      flag(fl + F6 + po * 256 + co * 8);
      STAMP(sb + 19);
      touch(dws, l < 20 ? co * 2560 + l * 128 : l == 20 ? 20480 + co * 128 : OOB, tile);
      cp_run<0, 8>(dws, l16, wq + 2 * 30720, qo + 2 * 30720);
      cp_run<0, 8>(dws, l16, wq + 3 * 30720, qo + 3 * 30720);
    }
  }
}
}

extern "C" void kernel_launch(void* const* d_in, const int* in_sizes, int n_in, void* d_out, int out_size, void* d_ws, size_t ws_size, hipStream_t stream) {
  (void)hipFuncSetAttribute((const void*)fused_kernel, hipFuncAttributeMaxDynamicSharedMemorySize, LDS_BYTES);
  fused_kernel<<<256, 256, LDS_BYTES, stream>>>((const char*)d_in[0], (const char*)d_in[1], (const char*)d_in[2], (const char*)d_in[4],
                                                (const char*)d_in[5], (const char*)d_in[6], (const char*)d_in[7], (char*)d_out, (char*)d_ws);
}
```

```cpp
#include <hip/hip_runtime.h>
#include <hip/hip_bf16.h>
#include <stdint.h>

#define AI __attribute__((always_inline))
#define LAS __attribute__((address_space(3)))
#define GAS __attribute__((address_space(1)))
typedef unsigned v6u __attribute__((ext_vector_type(6)));
typedef unsigned v4u __attribute__((ext_vector_type(4)));
typedef unsigned v2u __attribute__((ext_vector_type(2)));
typedef int v4i __attribute__((ext_vector_type(4)));
typedef float f32x4 __attribute__((ext_vector_type(4)));
typedef float f32x2 __attribute__((ext_vector_type(2)));
typedef __bf16 bf2 __attribute__((ext_vector_type(2)));
typedef short s4 __attribute__((ext_vector_type(4)));
typedef unsigned long long u64;
typedef __amdgpu_buffer_rsrc_t rsrc_t;

#define PROF 0

namespace {
constexpr int SC1 = 16;
constexpr int F0C = 4096;
constexpr int F0B = F0C + 16 * 256;
constexpr int F0S = F0B + 32 * 256;
constexpr int FLAY = F0S + 32 * 256;
constexpr int F1 = 0, F2 = F1 + 16 * 256, F4 = F2 + 32 * 256, F5 = F4 + 8 * 256, F6 = F5 + 32 * 256, FLAY_SZ = F6 + 32 * 256;
constexpr int ST_OFF = 1 << 17;
constexpr int ST_LAY = 32 * 8192;
constexpr int CSQ_OFF = ST_OFF + 2 * ST_LAY;
constexpr int CSI_OFF = CSQ_OFF + 16 * 16 * 512;
constexpr int WQ_OFF = 1 << 20, WQ_T = 153600;
constexpr int WO_OFF = WQ_OFF + 16 * WQ_T, WO_T = 102400;
constexpr int WI_OFF = WO_OFF + 8 * WO_T, WI_T = 143360;
constexpr int WU_OFF = WI_OFF + 16 * WI_T, WU_T = 138240;
constexpr int WS_OFF = WU_OFF + 8 * WU_T;
constexpr int ACT_OFF = WS_OFF + 128 * 65536;
constexpr int XT = 21504, OT = 20480, GT = 27648;
constexpr int XSZ = 128 * XT, OSZ = 128 * OT, ZSZ = 8 * 64 * 7168, GSZ = 128 * GT;
constexpr int A_XIN = 0, A_O = A_XIN + XSZ, A_X1 = A_O + OSZ, A_Z = A_X1 + XSZ, A_G = A_Z + ZSZ, ACT_LAY = A_G + GSZ;
constexpr int PROF_OFF = ACT_OFF + 2 * ACT_LAY;
constexpr int WS_USED = PROF_OFF + 256 * 256 * 4;
static_assert(FLAY + 2 * FLAY_SZ <= ST_OFF, "");
static_assert(CSI_OFF + 16 * 16 * 512 <= WQ_OFF, "");
constexpr int OOB = 0x40000000;
constexpr int LDS_BYTES = 163840;
constexpr int L_TOP = 159744;
constexpr int L_CSQ = L_TOP, L_CS2 = L_CSQ + 512, L_CS1 = L_CS2 + 512, L_XST3 = L_CS1 + 512, L_JUNK = L_XST3 + 512, L_PROF = L_JUNK + 256;
constexpr int L_XST1 = 153600, L_HST = L_XST1 + 2048;
constexpr int L_HALO = 0, L_NB = 24576;
constexpr int L_WO = 57344, L_X2 = L_WO;
constexpr int L_Z2B = 73728 - 4 * 14336;
constexpr int L_Z1B = 59392 - 3 * 14336;
constexpr int L_X3 = 43008, L_X3B = 102400, L_X5 = 139264;
constexpr int L_RED0 = 98304, L_RED1 = L_RED0 + 24576, L_RED2 = L_RED1 + 24576;
static_assert(L_RED2 >= 138240 && L_RED2 + 12288 <= L_TOP, "");
#define MAPQ 1
#define MAPO 1
#define MAPZ 1
constexpr int SHQ = MAPQ ? 4 : 2, SHO = MAPO ? 8 : 4, SHZ = MAPZ ? 8 : 4;
constexpr unsigned GTAG = 0x5A5A5A5Bu;
constexpr unsigned ONE2 = 0x3f803f80u;

template <int I> struct IC { static constexpr int v = I; };
template <int I, int N, class F> __device__ __forceinline__ void sfor(F&& f) {
  if constexpr (I < N) { f(IC<I>{}); sfor<I + 1, N>(f); }
}
__device__ __forceinline__ int opq_s(int x) { asm volatile("" : "+s"(x)); return x; }
__device__ __forceinline__ int opq_u(int x) { return opq_s(__builtin_amdgcn_readfirstlane(x)); }
__device__ __forceinline__ int opq_v(int x) { asm volatile("" : "+v"(x)); return x; }
template <class T> __device__ __forceinline__ void pin(T& c) { asm volatile("" : "+v"(c)); }

template <bool FIRST> __device__ __forceinline__ void mma_aa(f32x4& c, v4u a, v4u b) {
  if constexpr (FIRST) asm volatile("v_mfma_f32_16x16x32_bf16 %0, %1, %2, 0" : "=&v"(c) : "a"(a), "v"(b));
  else asm volatile("v_mfma_f32_16x16x32_bf16 %0, %1, %2, %0" : "+v"(c) : "a"(a), "v"(b));
}
template <bool FIRST, bool BA> __device__ __forceinline__ void mma(f32x4& c, v4u a, v4u b) {
  if constexpr (FIRST) {
    if constexpr (BA) asm volatile("v_mfma_f32_16x16x32_bf16 %0, %1, %2, 0" : "=&v"(c) : "v"(a), "a"(b));
    else asm volatile("v_mfma_f32_16x16x32_bf16 %0, %1, %2, 0" : "=&v"(c) : "v"(a), "v"(b));
  } else {
    if constexpr (BA) asm volatile("v_mfma_f32_16x16x32_bf16 %0, %1, %2, %0" : "+v"(c) : "v"(a), "a"(b));
    else asm volatile("v_mfma_f32_16x16x32_bf16 %0, %1, %2, %0" : "+v"(c) : "v"(a), "v"(b));
  }
}
__device__ __forceinline__ void mma_c(f32x4& d, v4u a, v4u b, f32x4 c) { asm volatile("v_mfma_f32_16x16x32_bf16 %0, %1, %2, %3" : "=&v"(d) : "v"(a), "a"(b), "v"(c)); }
#define NOP2() asm volatile("s_nop 1")
__device__ __forceinline__ void mma_settle() { asm volatile("s_nop 7" ::: "memory"); }
__device__ __forceinline__ float dot2(unsigned a, unsigned b, float c) {
  return __builtin_amdgcn_fdot2_f32_bf16(__builtin_bit_cast(bf2, a), __builtin_bit_cast(bf2, b), c, false);
}
__device__ __forceinline__ unsigned pk(float lo, float hi) {
  f32x2 v = {lo, hi};
  return __builtin_bit_cast(unsigned, __builtin_convertvector(v, bf2));
}
__device__ __forceinline__ v2u pk4(f32x4 v) { return v2u{pk(v[0], v[1]), pk(v[2], v[3])}; }
__device__ __forceinline__ float blo(unsigned u) { return __builtin_bit_cast(float, u << 16); }
__device__ __forceinline__ float bhi(unsigned u) { return __builtin_bit_cast(float, u & 0xffff0000u); }
__device__ __forceinline__ f32x4 unpk4(v2u r) { return f32x4{blo(r.x), bhi(r.x), blo(r.y), bhi(r.y)}; }
__device__ __forceinline__ float asf(unsigned u) { return __builtin_bit_cast(float, u); }
__device__ __forceinline__ unsigned asu(float f) { return __builtin_bit_cast(unsigned, f); }
__device__ __forceinline__ unsigned sel(unsigned a, unsigned b, u64 m) { unsigned r; asm("v_cndmask_b32_e64 %0, %1, %2, %3" : "=v"(r) : "v"(a), "v"(b), "s"(m)); return r; }
__device__ __forceinline__ float self(float a, float b, u64 m) { return asf(sel(asu(a), asu(b), m)); }
__device__ __forceinline__ unsigned sel0(unsigned b, u64 m) { unsigned r; asm("v_cndmask_b32_e64 %0, 0, %1, %2" : "=v"(r) : "v"(b), "s"(m)); return r; }
__device__ __forceinline__ int seloob(int b, u64 m) { int r; asm("v_cndmask_b32_e64 %0, 2.0, %1, %2" : "=v"(r) : "v"(b), "s"(m)); return r; }
__device__ __forceinline__ float vmax(float a, float b) { return __builtin_elementwise_maximum(a, b); }
__device__ __forceinline__ float gadd(float a, float b) { float r; asm volatile("v_add_f32 %0, %1, %2" : "=v"(r) : "v"(a), "v"(b)); return r; }
__device__ __forceinline__ float gmul(float a, float b) { float r; asm volatile("v_mul_f32 %0, %1, %2" : "=v"(r) : "v"(a), "v"(b)); return r; }
__device__ __forceinline__ unsigned gcvt(float a, float b) { unsigned r; asm volatile("v_cvt_pk_bf16_f32 %0, %1, %2" : "=v"(r) : "v"(a), "v"(b)); return r; }
template <int C> __device__ __forceinline__ int gaddi(int a) { int r; asm volatile("v_add_u32 %0, %1, %2" : "=v"(r) : "n"(C), "v"(a)); return r; }
constexpr u64 M_ODD = 0xAAAAAAAAAAAAAAAAull, M_G0 = 0xFFFFull, M_G1 = 0xFFFF0000ull, M_GEVEN = 0x0000FFFF0000FFFFull, M_LO = 0xFFFFFFFFull;
constexpr u64 le_mask(int add) { u64 m = 0; for (int l = 0; l < 64; l++) if (4 * (l >> 4) + (l & 1) + add <= (l & 15)) m |= 1ull << l; return m; }
__device__ __forceinline__ rsrc_t mk(const void* p, int bytes) { return __builtin_amdgcn_make_buffer_rsrc((void*)p, 0, bytes, 0x00020000); }
__device__ __forceinline__ v4i mkd(const void* p, int bytes) {
  u64 a = (u64)p; v4i r; r.x = (int)(unsigned)a; r.y = (int)((unsigned)(a >> 32) & 0xffffu); r.z = bytes; r.w = 0x00020000; return r;
}
template <int AUX> __device__ __forceinline__ v4u ld16(rsrc_t r, int v, int s) { return __builtin_amdgcn_raw_buffer_load_b128(r, v, s, AUX); }
template <int AUX> __device__ __forceinline__ v2u ld8(rsrc_t r, int v, int s) { return __builtin_amdgcn_raw_buffer_load_b64(r, v, s, AUX); }
__device__ __forceinline__ void st16(v4u d, rsrc_t r, int v, int s) { __builtin_amdgcn_raw_buffer_store_b128(d, r, v, s, SC1); }
#define BAR() asm volatile("s_waitcnt lgkmcnt(0)\n\ts_barrier" ::: "memory")
#define BAR0() asm volatile("s_barrier" ::: "memory")
#define DRAIN() asm volatile("s_waitcnt vmcnt(0)" ::: "memory")
#define WAITV(n) asm volatile("s_waitcnt vmcnt(%0)" ::"n"(n) : "memory")
#define SB() __builtin_amdgcn_sched_barrier(0)
#define FENCE() do { asm volatile("" ::: "memory"); __builtin_amdgcn_sched_barrier(0); } while (0)
#define GL(o) "\n\tbuffer_load_dwordx4 %1, %2, %3 offen offset:" #o " sc1 lds"
#define GLARGS ::"s"(lds), "v"(voff), "s"(rs), "s"(soff)
__device__ __forceinline__ void glds_in(v4i rs, int voff, int soff, int lds) {
  asm volatile("s_mov_b32 m0, %0\n\ts_nop 0\n\tbuffer_load_dwordx4 %1, %2, %3 offen lds" GLARGS : "memory");
}
template <int N> __device__ __forceinline__ void gldsn(v4i rs, int voff, int soff, int lds) {
  static_assert(N >= 1 && N <= 4, "");
  if constexpr (N == 1) asm volatile("s_mov_b32 m0, %0\n\ts_nop 0" GL(0) GLARGS : "memory");
  if constexpr (N == 2) asm volatile("s_mov_b32 m0, %0\n\ts_nop 0" GL(0) GL(1024) GLARGS : "memory");
  if constexpr (N == 3) asm volatile("s_mov_b32 m0, %0\n\ts_nop 0" GL(0) GL(1024) GL(2048) GLARGS : "memory");
  if constexpr (N == 4) asm volatile("s_mov_b32 m0, %0\n\ts_nop 0" GL(0) GL(1024) GL(2048) GL(3072) GLARGS : "memory");
}
template <int N0, int N1> __device__ __forceinline__ void cp_run(v4i rs, int voff, int src, int dst) {
  if constexpr (N0 < N1) {
    constexpr int n = N1 - N0 < 4 ? N1 - N0 : 4;
    gldsn<n>(rs, voff, src + N0 * 1024, dst + N0 * 1024);
    cp_run<N0 + n, N1>(rs, voff, src, dst);
  }
}
__device__ __forceinline__ void cp_arm(int lds) { asm volatile("s_mov_b32 m0, %0" ::"s"(lds) : "memory"); }
template <int U> __device__ __forceinline__ void cp_one(v4i rs, int voff, int soff) {
  asm volatile("buffer_load_dwordx4 %0, %1, %2 offen offset:%3 sc1 lds" ::"v"(voff), "s"(rs), "s"(soff), "n"((U & 3) * 1024) : "memory");
}
__device__ __forceinline__ void touch(v4i rs, int voff, int soff) {
  asm volatile("s_mov_b32 m0, %0\n\ts_nop 0\n\tbuffer_load_dword %1, %2, %3 offen sc1 lds" ::"s"(L_JUNK), "v"(voff), "s"(rs), "s"(soff) : "memory");
}
template <int OFF, bool WS> __device__ __forceinline__ void lda16(v4u& d, v4i rs, int voff, int soff) {
  if constexpr (WS) asm volatile("buffer_load_dwordx4 %0, %1, %2, %3 offen offset:%4 sc1" : "+a"(d) : "v"(voff), "s"(rs), "s"(soff), "n"(OFF) : "memory");
  else asm volatile("buffer_load_dwordx4 %0, %1, %2, %3 offen offset:%4" : "+a"(d) : "v"(voff), "s"(rs), "s"(soff), "n"(OFF) : "memory");
}
template <int OFF> __device__ __forceinline__ void sta16(const v4u& d, v4i rs, int voff, int soff) {
  asm volatile("buffer_store_dwordx4 %0, %1, %2, %3 offen offset:%4 sc1" ::"a"(d), "v"(voff), "s"(rs), "s"(soff), "n"(OFF) : "memory");
}
template <int OFF> __device__ __forceinline__ void ldsa16(v4u& d, int addr) { asm volatile("ds_read_b128 %0, %1 offset:%2" : "+a"(d) : "v"(addr), "n"(OFF) : "memory"); }
__device__ __forceinline__ void park(v4u& d, v4u v) { asm volatile("" : "=a"(d) : "0"(v)); }
__device__ __forceinline__ void tch(v4i rs, int voff, int soff) {
  asm volatile("buffer_load_dword %0, %1, %2 offen sc1 lds" ::"v"(voff), "s"(rs), "s"(soff) : "memory");
}
__device__ __forceinline__ int qslot_off(int w) { return (w * 7680) & ~1023; }
template <class T> __device__ __forceinline__ LAS T* lp(int off) { return (LAS T*)(size_t)(unsigned)off; }
__device__ __forceinline__ v4u lds16(int off) { return *lp<v4u>(off); }
__device__ __forceinline__ v2u lds8(int off) { return *lp<v2u>(off); }
__device__ __forceinline__ f32x4 ldsf4(int off) { return *lp<f32x4>(off); }
__device__ __forceinline__ float ldsf(int off) { return *lp<float>(off); }
__device__ __forceinline__ void poll(rsrc_t rws, int voff, bool off, unsigned lo, unsigned hi) {
  for (unsigned spins = 0; spins < (1u << 18); ++spins) {
    asm volatile("" ::: "memory");
    const v2u v = ld8<SC1>(rws, voff, 0);
    if (__all(off || (v.x == lo && v.y == hi))) break;
  }
  asm volatile("" ::: "memory");
}
__device__ __forceinline__ v2u sw16(unsigned a, unsigned b) { auto r = __builtin_amdgcn_permlane16_swap(a, b, false, false); return v2u{r[0], r[1]}; }
__device__ __forceinline__ v2u sw32(unsigned a, unsigned b) { auto r = __builtin_amdgcn_permlane32_swap(a, b, false, false); return v2u{r[0], r[1]}; }
__device__ __forceinline__ v4u join16(v2u x, v2u y) { const v2u a = sw16(x.x, y.x), b = sw16(x.y, y.y); return v4u{a.x, b.x, a.y, b.y}; }
template <int CTRL> __device__ __forceinline__ float dpp(float v) {
  return __builtin_bit_cast(float, __builtin_amdgcn_update_dpp(0, __builtin_bit_cast(int, v), CTRL, 0xf, 0xf, true));
}
__device__ __forceinline__ float xsum2(float s, float q) {
  const v2u a = sw32(asu(s), asu(q)); const float t = asf(a.x) + asf(a.y);
  const v2u b = sw16(asu(t), asu(t)); return asf(b.x) + asf(b.y);
}
__device__ __forceinline__ f32x2 finish_stat(float s, float q, float inv_n) {
  const float m = s * inv_n;
  return f32x2{m, __builtin_amdgcn_rsqf(fmaxf(q * inv_n - m * m, 0.f) + 1e-5f)};
}
__device__ __forceinline__ f32x4 gelu4(f32x4 v) {
  const float k1 = -2.0f * 0.7978845608028654f * 1.4426950408889634f, k2 = k1 * 0.044715f;
  f32x4 e = v * ((v * v) * k2 + k1);
  asm("" : "+v"(e));
#pragma unroll
  for (int i = 0; i < 4; i++) e[i] = __builtin_amdgcn_exp2f(e[i]);
  e += 1.0f;
#pragma unroll
  for (int i = 0; i < 4; i++) e[i] = __builtin_amdgcn_rcpf(e[i]);
  return v * e;
}
__device__ __forceinline__ f32x2 rowsum8(f32x4 s, f32x4 q) {
  float a, b, c, d;
  asm volatile("s_nop 1\n\t"
      "v_add_f32_dpp %0, %4, %4 row_ror:8 row_mask:0xf bank_mask:0x3\n\t"
      "v_add_f32_dpp %1, %5, %5 row_ror:8 row_mask:0xf bank_mask:0x3\n\t"
      "v_add_f32_dpp %2, %8, %8 row_ror:8 row_mask:0xf bank_mask:0x3\n\t"
      "v_add_f32_dpp %3, %9, %9 row_ror:8 row_mask:0xf bank_mask:0x3\n\t"
      "v_add_f32_dpp %0, %6, %6 row_ror:8 row_mask:0xf bank_mask:0xc\n\t"
      "v_add_f32_dpp %1, %7, %7 row_ror:8 row_mask:0xf bank_mask:0xc\n\t"
      "v_add_f32_dpp %2, %10, %10 row_ror:8 row_mask:0xf bank_mask:0xc\n\t"
      "v_add_f32_dpp %3, %11, %11 row_ror:8 row_mask:0xf bank_mask:0xc\n\t"
      "v_add_f32_dpp %0, %0, %0 row_half_mirror row_mask:0xf bank_mask:0x5\n\t"
      "v_add_f32_dpp %0, %1, %1 row_half_mirror row_mask:0xf bank_mask:0xa\n\t"
      "v_add_f32_dpp %2, %2, %2 row_half_mirror row_mask:0xf bank_mask:0x5\n\t"
      "v_add_f32_dpp %2, %3, %3 row_half_mirror row_mask:0xf bank_mask:0xa\n\t"
      "v_add_f32_dpp %0, %0, %0 quad_perm:[1,0,3,2] row_mask:0xf bank_mask:0xf\n\t"
      "s_nop 0\n\t"
      "v_add_f32_dpp %2, %2, %2 quad_perm:[1,0,3,2] row_mask:0xf bank_mask:0xf\n\t"
      "v_add_f32_dpp %0, %0, %0 quad_perm:[2,3,0,1] row_mask:0xf bank_mask:0xf\n\t"
      "s_nop 0\n\t"
      "v_add_f32_dpp %2, %2, %2 quad_perm:[2,3,0,1] row_mask:0xf bank_mask:0xf"
      : "=&v"(a), "=&v"(b), "=&v"(c), "=&v"(d)
      : "v"(s[0]), "v"(s[1]), "v"(s[2]), "v"(s[3]), "v"(q[0]), "v"(q[1]), "v"(q[2]), "v"(q[3]));
  return f32x2{a, c};
}
__device__ __forceinline__ v4u bperm4(int idx, v4u v) {
  v4u r;
  r.x = __builtin_amdgcn_ds_bpermute(idx, v.x); r.y = __builtin_amdgcn_ds_bpermute(idx, v.y);
  r.z = __builtin_amdgcn_ds_bpermute(idx, v.z); r.w = __builtin_amdgcn_ds_bpermute(idx, v.w);
  return r;
}
__device__ __forceinline__ v2u trd(int off) { return __builtin_bit_cast(v2u, __builtin_amdgcn_ds_read_tr16_b64_v4i16(lp<s4>(off))); }
#define STAMP(i) do {} while (0)
#define CSTAMP(i) do {} while (0)
#define LANE() const int l = opq_v((int)threadIdx.x) & 63, l16 = l * 16, g = l >> 4, c16 = l & 15; (void)g; (void)c16; (void)l16; \
  const int w = opq_s(w_), po = opq_s(po_), co = opq_s(co_), pz = opq_s(pz_), cz = opq_s(cz_), P = opq_s(P_), h = opq_s(h_); \
  (void)w; (void)po; (void)co; (void)pz; (void)cz; (void)P; (void)h; \
  const int kh = w & 1, w2 = w >> 1; (void)kh; (void)w2; \
  auto flag = [&](int off) AI { __builtin_amdgcn_raw_buffer_store_b64(v2u{tag_lo, tag_hi}, rws, seloob(0, 1ull), w == 0 ? off : OOB, SC1); }; (void)flag;
#define WSP() int wsb[4]; \
  _Pragma("unroll") for (int r = 0; r < 4; r++) wsb[r] = opq_s(WS_OFF + (pz * 256 + ((w + r) & 3) * 64 + 16 * w) * 1024); \
  const int zr = opq_s(MAPZ ? pz + 2 * cz + (cz >> 2) : 2 * pz + cz); \
  auto wsp_ld = [&](auto ni) AI { constexpr int n = ni.v, e = n >> 3, r = (n >> 1) & 3, s_ = n & 1; \
    lda16<s_ * 1024, true>(W[r][2 * e + s_], dws, l16, wsb[r] + ((e + zr) & 7) * 2048); }; (void)wsp_ld;

template <int NS, int NCT, bool TR, int WIN, bool INIT, class BA, class GO, class LD, class HA, class HB>
__device__ __forceinline__ void gemm_m2k2(BA&& badr, int xw, v4u (&A)[2][NS], f32x4 (&acc)[2][NCT], GO&& go, LD&& lda, HA&& ha, HB&& hb) {
  constexpr int NF = NS * NCT, NL = NF - NCT;
  static_assert(NF % 2 == 0, "");
  v4u B[NF];
  auto ldB = [&](auto fi) AI { constexpr int f = fi.v; B[f] = lds16(badr(IC<f / NCT>{}) + (f % NCT) * 1024); };
  auto mm = [&](auto ii, auto fi) AI {
    constexpr int i = ii.v, f = fi.v, t = f / NCT, ct = f % NCT;
    if (TR) mma<t == 0 && !(INIT && i == 0), false>(acc[i][ct], B[f], A[i][t]); else mma<t == 0 && !(INIT && i == 0), false>(acc[i][ct], A[i][t], B[f]);
    SB();
  };
  sfor<0, 6>([&](auto fi) AI { ldB(IC<(fi.v ^ 1)>{}); SB(); });
  go();
  if constexpr (WIN > 0) sfor<0, WIN>([&](auto ti) AI { A[1][ti.v] = lda(IC<1>{}, ti); A[0][ti.v] = lda(IC<0>{}, ti); });
  SB();
  sfor<0, NL>([&](auto fi) AI {
    constexpr int f = fi.v;
    mm(IC<0>{}, fi);
    if constexpr (((f + 6) ^ 1) < NF) ldB(IC<((f + 6) ^ 1)>{});
    ha(fi);
    SB();
    mm(IC<1>{}, fi);
    if constexpr (WIN > 0) {
      constexpr int n = 2 * WIN + f / 2;
      if constexpr (f % 2 == 0 && n < 2 * NS) A[(n & 1) ^ 1][n >> 1] = lda(IC<((n & 1) ^ 1)>{}, IC<(n >> 1)>{});
    }
    if constexpr (f == NL - 1) sfor<NL, NF>([&](auto gi) AI { if constexpr (((gi.v + 6) ^ 1) < NF) ldB(IC<((gi.v + 6) ^ 1)>{}); });
    hb(fi);
    SB();
  });
  sfor<NL, NF>([&](auto fi) AI { mm(IC<1>{}, fi); ha(fi); SB(); });
  sfor<NL, NF>([&](auto fi) AI {
    constexpr int j = fi.v - NL, D = NCT == 5 ? 3 : 1;
    mm(IC<0>{}, fi);
    if constexpr (j >= D) { pin(acc[1][j - D]); *lp<f32x4>(xw + (j - D) * 1024) = acc[1][j - D]; }
    if constexpr (j == NCT - 1) sfor<NCT - D, NCT>([&](auto ki) AI { pin(acc[1][ki.v]); *lp<f32x4>(xw + ki.v * 1024) = acc[1][ki.v]; });
    SB();
  });
}
template <int NCT> __device__ __forceinline__ void xchg_read(int src, f32x4 (&acc)[2][NCT]) {
#pragma unroll
  for (int j = 0; j < NCT; j++) acc[1][j] = ldsf4(src + j * 1024);
  SB();
#pragma unroll
  for (int j = 0; j < NCT; j++) { pin(acc[0][j]); acc[0][j] += acc[1][j]; }
}
__device__ __forceinline__ void store_x(const f32x4 (&x)[5], rsrc_t rws, int so, int ss, int vo, int vo4, int vs) {
  v2u d[5];
#pragma unroll
  for (int j = 0; j < 5; j++) d[j] = pk4(x[j]);
  float s0 = 0.f, q0 = 0.f;
#pragma unroll
  for (int j = 0; j < 5; j++) { s0 = dot2(d[j].x, ONE2, s0); q0 = dot2(d[j].x, d[j].x, q0); s0 = dot2(d[j].y, ONE2, s0); q0 = dot2(d[j].y, d[j].y, q0); }
  st16(join16(d[0], d[1]), rws, vo, so);
  st16(join16(d[2], d[3]), rws, vo + 1024, so);
  st16(join16(d[4], d[4]), rws, vo4 + 2048, so);
  __builtin_amdgcn_raw_buffer_store_b32(asu(xsum2(s0, q0)), rws, vs, ss, SC1);
}
constexpr int QPRE = 9;
constexpr int q_step(int n) { return n % 9 == 0 ? n / 9 * 4 : n / 9 * 4 + (n % 9 - 1) / 2; }
constexpr int q_cnt(int f) { int n = QPRE; for (int i = 0; i <= f; i++) if (i % 2 == 0 && n < 45 && q_step(n) <= i / 8 + 6) n++; return n; }
constexpr int q_free(int f) { int n = 0; for (int i = 0; i < f; i += 2) if (q_cnt(i) == q_cnt(i - 1)) n++; return (f % 2 == 0 && q_cnt(f) == q_cnt(f - 1)) ? n : -100; }
constexpr int l0_vm(int f) { return (f < 72 ? f % 3 == 0 : f < 104 ? f % 4 == 0 : 0) + (f >= 73 && f < 105 && f % 4 == 2) + (f == 1) + (f == 86); }
constexpr int l0_cnt(int f0, int f1) { int n = 0; for (int i = f0; i < f1; i++) n += l0_vm(i); return n; }
constexpr int sp_s(int f) { return f < 14 ? f / 7 : f < 98 ? 2 + ((f - 14) % 28) / 2 : 2 + (f - 98); }
constexpr int sp_j(int f) { return f < 14 ? f % 7 : f < 98 ? (f - 14) / 28 * 2 + (f & 1) : 6; }
#define XA(n) W[(n) / 14][2 + (n) % 14]

__global__ __launch_bounds__(256, 1) void fused_kernel(const char* __restrict__ x0, const char* __restrict__ wqkv, const char* __restrict__ wo,
                                                       const char* __restrict__ win, const char* __restrict__ wsp, const char* __restrict__ bsp,
                                                       const char* __restrict__ wout, char* out, char* ws) {
  const int w_ = __builtin_amdgcn_readfirstlane(threadIdx.x >> 6);
  const int b = blockIdx.x, xcd = b & 7, jb = b >> 3;
  const int pa = xcd * 4 + (jb & 3), ca = jb >> 2, pb = (xcd >> 1) * 8 + (jb & 7), cb = (xcd & 1) * 4 + (jb >> 3);
  const int po_ = MAPO ? pb : pa, co_ = MAPO ? cb : ca, pz_ = MAPZ ? pb : pa, cz_ = MAPZ ? cb : ca;
  const int P_ = MAPQ ? (xcd >> 1) * 4 + (jb & 3) : xcd * 2 + (jb & 1), h_ = MAPQ ? (xcd & 1) * 8 + (jb >> 2) : jb >> 1;
  const rsrc_t rws = mk(ws, WS_USED);
  const v4i dws = mkd(ws, WS_USED);
  STAMP(0); CSTAMP(62);
  unsigned tag_lo, tag_hi;
  v4u W[4][16];
#pragma unroll
  for (int r = 0; r < 4; r++)
#pragma unroll
    for (int s = 0; s < 16; s++) asm volatile("" : "=a"(W[r][s]));
  {
    LANE();
    {
      const v4i dq = mkd(wqkv, 640 * 3840);
      const int vq = g * 3840 + c16 * 16 + (c16 < 5 ? 1280 : c16 < 10 ? -80 : 2400), s0 = (P * 40 + w * 12) * 3840 + 80 * h, kill = w == 3 ? OOB : 0;
      glds_in(dq, vq, s0, w * 3072);
      glds_in(dq, vq, (s0 + 4 * 3840) | kill, w * 3072 + 1024);
      glds_in(dq, vq, (s0 + 8 * 3840) | kill, w * 3072 + 2048);
    }
    {
      u64 pc; asm volatile("s_getpc_b64 %0" : "=s"(pc));
      touch(mkd((const void*)(pc & ~127ull), 51200), (((jb * 4 + w) & 7) * 64 + l) * 128, 0);
    }
    if (w == 0) touch(dws, (jb * 64 + l) * 32768 + 16384, 0);
    else {
      const char* tp = w == 1 ? wsp : w == 2 ? (const char*)out : x0;
      touch(mkd(tp, w == 1 ? 2048 * 4096 : 2048 * 1280), ((jb & 3) * 64 + l) * 32768, 0);
    }
    const int tau = jb, sl = xcd, stg = 32768 + w * 8192;
    const int ty = tau < 8 ? 0 : tau < 24 ? 1 : 2, ot = tau - ty * 8 - (ty >> 1) * 8, cc = ot & 7;
    const int o_kg = opq_u((ty < 2 ? 10 * sl : sl < 4 ? 14 * sl : 4 + 13 * sl) + w2), o_n = opq_u((ty < 2 ? 10 : sl < 4 ? 14 : 13) - w2);
    {
      const char* src = ty == 0 ? wo : ty == 1 ? win : wout;
      const int ldb = ty == 1 ? 3456 : 1280, lim = ty != 1 ? 9 : cc < 6 ? 13 : 11, chi = kh * 8 + (l & 7);
      const int col = ty != 1 ? ot * 160 : (ot < 8 ? 1728 : 0) + (cc < 6 ? 7 * cc : 6 + 6 * cc) * 32;
      const v4i dsrc = mkd(src, (ty == 2 ? 864 : 640) * ldb);
      const int ov = (l >> 3) * ldb + (chi < lim ? chi : lim) * 16;
      const int o0 = opq_u(o_kg * 8 * ldb + col), od = opq_u(16 * ldb), k5 = opq_u(o_n > 10 ? 0 : OOB), k6 = opq_u(o_n > 12 ? 0 : OOB);
      asm volatile("s_nop 4");
#pragma unroll
      for (int i = 0; i < 7; i++) glds_in(dsrc, ov, (o0 + i * od) | (i == 5 ? k5 : i == 6 ? k6 : 0), stg + i * 1024);
    }
    const v2u ep = ld8<SC1>(rws, 0, 0);
    FENCE();
    const rsrc_t rx = mk(x0, 2048 * 1280);
    const int xo = (128 * P + 32 * w) * 1280, ho = P > 0 ? (128 * P - 16) * 1280 : OOB;
    int vx[4];
#pragma unroll
    for (int i = 0; i < 4; i++) vx[i] = (l >> 2) * 1280 + (((l ^ (0 - g)) & 3) + ((i + w) & 3) * 4) * 16;
    v4u R[45];
    auto ldX = [&](auto ni) AI {
      constexpr int n = ni.v, t = q_step(n), i = (n % 9) & 1;
      R[n] = ld16<0>(rx, vx[t & 3] + (t >> 2) * 256, n % 9 == 0 ? ho : xo + i * 16 * 1280);
    };
    const int ct = 2 * w + (g & 1), pq = l & 3;
    const int cb_ = ct == 2 ? 32 + (pq & 1) * 8 + (pq >> 1) * 80 : ct == 7 ? 192 + (pq & 1) * 8 : (ct < 2 ? ct * 48 : ((ct - 1) >> 1) * 80 + ((ct - 1) & 1) * 48) + pq * 8;
    const int trq = opq_v(((g >> 1) * 8 + (c16 >> 2)) * 256 + cb_);
    int qv[3];
#pragma unroll
    for (int i = 0; i < 3; i++) {
      const int k = 2 * i + (g >> 1), kap = 5 * P + k;
      qv[i] = opq_v((k < 5 && (ct < 7 || c16 < 8)) ? (kap >> 2) * 7680 + ct * 1024 + ((kap & 3) * (ct == 7 ? 8 : 16) + c16) * 16 : OOB);
    }
    FENCE();
    sfor<0, 4>(ldX);
    FENCE();
    WAITV(14);
    STAMP(1);
    BAR0();
    {
      v2u lo[3], hi[3];
#pragma unroll
      for (int i = 0; i < 3; i++) { lo[i] = trd(trq + i * 4096); hi[i] = trd(trq + i * 4096 + 1024); }
      SB();
#pragma unroll
      for (int i = 0; i < 3; i++) st16(v4u{lo[i].x, lo[i].y, hi[i].x, hi[i].y}, rws, qv[i], WQ_OFF + h * WQ_T);
      float cs = 0.f;
#pragma unroll
      for (int i = 0; i < 3; i++) { cs = dot2(lo[i].x, ONE2, cs); cs = dot2(lo[i].y, ONE2, cs); cs = dot2(hi[i].x, ONE2, cs); cs = dot2(hi[i].y, ONE2, cs); }
      const v2u t = sw32(asu(cs), asu(cs));
      __builtin_amdgcn_raw_buffer_store_b32(asu(asf(t.x) + asf(t.y)), rws, seloob(l * 4, M_LO), CSQ_OFF + (h * 16 + P) * 512 + w * 128, SC1);
    }
    FENCE();
    sfor<4, 18>(ldX);
    FENCE();
    {
      const u64 epoch = ((u64)(unsigned)__builtin_amdgcn_readfirstlane(ep.y) << 32) | (unsigned)__builtin_amdgcn_readfirstlane(ep.x);
      const u64 tag = epoch * 0x9E3779B97F4A7C15ull + 0xD1B54A32D192ED03ull;
      tag_lo = opq_s((unsigned)tag); tag_hi = opq_s((unsigned)(tag >> 32));
    }
    WAITV(14);
    BAR0();
    flag(F0C + h * 256 + P * 8);
    STAMP(2);
    float xs[3] = {0.f, 0.f, 0.f}, xq[3] = {0.f, 0.f, 0.f};
    const int xwr = opq_v(16384 + w * 4096 + l16), xrd = opq_v(16384 + w * 4096 + c16 * 64 + ((g ^ (0 - (c16 >> 2))) & 3) * 16);
    auto doX = [&](auto ni) AI {
      constexpr int n = ni.v, i = n % 9 == 0 ? 2 : (n % 9) & 1;
      *lp<v4u>(xwr + (n & 3) * 1024) = R[n];
      ldsa16<(n & 3) * 1024>(XA(n), xrd);
#pragma unroll
      for (int e = 0; e < 4; e++) { xs[i] = dot2(R[n][e], ONE2, xs[i]); xq[i] = dot2(R[n][e], R[n][e], xq[i]); }
      pin(xs[i]); pin(xq[i]);
      SB();
    };
    sfor<18, 45>([&](auto ni) AI { ldX(ni); doX(IC<ni.v - 18>{}); });
    {
      const int tro = stg + (c16 >> 2) * 128 + g * 32 + (l & 3) * 8;
      const int o_v = kh * 4 + g < (ty == 1 ? 7 : 5) ? (kh * 4 + g) * 1024 + c16 * 16 : OOB;
      const int o_dst = ty == 0 ? WO_OFF + ot * WO_T : ty == 1 ? WI_OFF + ot * WI_T : WU_OFF + ot * WU_T, kb = ty < 2 ? 10 : 14, kstr = ty == 1 ? 7168 : 5120;
      float cs = 0.f;
#pragma unroll
      for (int i = 0; i < 7; i++) {
        const v2u lo = trd(tro + i * 1024), hi = trd(tro + i * 1024 + 512);
        if (i < 5) { cs = dot2(lo.x, ONE2, cs); cs = dot2(lo.y, ONE2, cs); cs = dot2(hi.x, ONE2, cs); cs = dot2(hi.y, ONE2, cs); }
        const int kap = o_kg + 2 * i, ks = kap >> 2, pos = ks < kb ? 2 * ks : 2 * (ks - kb) + 1;
        st16(v4u{lo.x, lo.y, hi.x, hi.y}, rws, o_v, opq_u(2 * i < o_n ? o_dst + pos * kstr + (kap & 3) * 256 : OOB));
      }
      __builtin_amdgcn_raw_buffer_store_b32(asu(cs), rws, l * 4, opq_u(ty == 1 ? CSI_OFF + (ot * 16 + sl * 2 + w2) * 512 + kh * 256 : OOB), SC1);
      st16(v4u{0, 0, 0, 0}, rws, seloob(g * ST_LAY + c16 * 16, M_LO), ST_OFF + po * 8192 + w * 2048 + co * 256);
    }
    FENCE();
    sfor<27, 45>(doX);
#pragma unroll
    for (int i = 0; i < 3; i++) { xs[i] += dpp<0xB1>(xs[i]); xq[i] += dpp<0xB1>(xq[i]); xs[i] += dpp<0x4E>(xs[i]); xq[i] += dpp<0x4E>(xq[i]); }
#pragma unroll
    for (int i = 0; i < 2; i++) {
      const f32x2 v = finish_stat(xs[i], xq[i], 1.f / 640.f);
      *lp<float>(L_XST1 + w * 512 + i * 64 + (l & 60)) = v.x; *lp<float>(L_XST1 + w * 512 + 256 + i * 64 + (l & 60)) = v.y;
    }
    *lp<f32x2>(L_HST + w * 128 + (l >> 2) * 8) = f32x2{xs[2], xq[2]};
    STAMP(3);
  }

#pragma unroll 1
  for (int Lr = 0;; Lr = 1) {
    const int L = opq_s(Lr), kill1 = L << 30;
    const int act = ACT_OFF + L * ACT_LAY, fl = FLAY + L * FLAY_SZ;
    const int sb = L * 24 + 5;
    f32x4 z1[7];
    unsigned braw;
    constexpr int RZ = 14;
    v4u Z[RZ];
    { LANE();
      const int qo = qslot_off(w), wq = WQ_OFF + h * WQ_T + qo;
      int vb[4], vh[4];
#pragma unroll
      for (int i = 0; i < 4; i++) { const int k = (i + w) & 3; vb[i] = opq_v(l16 + k * 7680); vh[i] = opq_v((g * 8 + (l & 7)) * 16 + k * 7680); }
      const int h2 = w < 2 ? w + 4 : w;
      const int c4 = opq_v(c16 * 4), g16 = opq_v(g * 16);
      const int wos = opq_s(WO_OFF + co * WO_T), wzs = opq_s(WI_OFF + cz * WI_T);
      f32x4 acc[2][8];
      float mt[3], rt_[3], csn[3], hcn; f32x4 mn[3], rn[3], cst[5], ha[2][4], hcs;
      auto qkv = [&](auto l0) AI {
        constexpr bool L0 = l0.v;
        v4u A[2][20], Hh[5], B[8];
        int va[4], spo, xoff, hoff, fbo, cqo, wv, wr, wk; v4i dw;
        auto ldB = [&](auto fi) AI { constexpr int f = fi.v, t = f >> 3, ct = f & 7; B[f & 7] = lds16((ct < 7 ? vb[t & 3] : vh[t & 3]) + ((t >> 2) & 1) * 30720 + ct * 1024); };
        auto ldA = [&](auto ni) AI {
          constexpr int n = ni.v, t = q_step(n), i = (n % 9) & 1;
          if constexpr (n % 9 == 0) Hh[t >> 2] = ld16<SC1>(rws, va[0], hoff + (t >> 2) * 4096);
          else A[i][t] = ld16<SC1>(rws, va[t & 3], xoff + i * XT + (t >> 2) * 4096);
        };
        if constexpr (L0) {
          dw = mkd(wsp, 2048 * 4096);
          wv = opq_v((l >> 2) * 4096 + ((l ^ (0 - g)) & 3) * 16);
          wr = opq_v(24576 + w * 8192 + c16 * 64 + ((g ^ (0 - (c16 >> 2))) & 3) * 16);
          wk = opq_s(16 * w + ((MAPZ ? pz + 2 * cz + (cz >> 2) : 2 * pz + cz) & 7) * 2);
          fbo = opq_v(seloob(F0B + ((l >> 3) * 8 + (((l >> 3) + 1) & 2 ? cz : co)) * 256 + (l & 7) * 8, M_LO));
          cqo = opq_v((l >> 5) * 4096 + (l & 31) * 4);
          poll(rws, seloob(F0C + h * 256 + c16 * 8, M_G0), l >= 16, tag_lo, tag_hi);
          BAR();
          STAMP(sb - 1);
          cp_run<0, 8>(dws, l16, wq, qo); cp_run<0, 8>(dws, l16, wq + 30720, qo + 30720);
          const f32x2 a = (*lp<f32x2>(L_HST + c16 * 8) + *lp<f32x2>(L_HST + 128 + c16 * 8)) + (*lp<f32x2>(L_HST + 256 + c16 * 8) + *lp<f32x2>(L_HST + 384 + c16 * 8));
          const f32x2 v = finish_stat(a.x, a.y, 1.f / 640.f);
          *lp<float>(L_XST1 + w * 512 + 128 + c4) = v.x; *lp<float>(L_XST1 + w * 512 + 384 + c4) = v.y;
          WAITV(8);
          BAR0();
          sfor<0, 6>([&](auto fi) AI { ldB(IC<(fi.v ^ 1)>{}); SB(); });
        } else {
#pragma unroll
          for (int i = 0; i < 4; i++) va[i] = opq_v(l16 + ((i + w) & 3) * 1024);
          spo = opq_v(g < 2 ? (2 * w + 1 + g) * XT + c16 * 8 : (g == 2 && P > 0) ? c16 * 8 : OOB);
          xoff = opq_s(act + A_XIN + (8 * P + 2 * w) * XT); hoff = opq_s(P > 0 ? act + A_XIN + (8 * P - 1) * XT : OOB);
          sfor<0, 6>([&](auto fi) AI { ldB(IC<(fi.v ^ 1)>{}); SB(); });
          const bool on = l < 8 || (l < 16 && P > 0);
          poll(rws, on ? FLAY + F6 + (l < 8 ? 2 * P + w2 : 2 * P - 1) * 256 + (l & 7) * 8 : OOB, !on, tag_lo, tag_hi);
          STAMP(sb - 1);
          sfor<0, QPRE>(ldA);
          SB();
        }
        f32x4 hac[6];
        v2u sp[8], fb; float ss[8], sq[8]; int xa, xb;
        sfor<0, 160>([&](auto fi) AI {
          constexpr int f = fi.v, t = f >> 3, ct = f & 7, hc = ct < 3 ? ct : ct - 2, xn = (t >> 2) * 9 + 1 + (t & 3) * 2;
          constexpr bool halo = (t & 3) == 0 && (ct < 3 || ct >= 5);
          constexpr int fr = L0 ? -100 : q_free(f);
          constexpr int si = f == 33 ? 0 : f == 37 ? 1 : f == 39 ? 2 : f == 62 ? 3 : f == 65 ? 4 : f == 68 ? 5 : f == 69 ? 6 : f == 71 ? 7 : -1; (void)si;
          const v4u bb = B[f & 7];
          auto mm = [&](f32x4& d, const v4u& x) AI {
            if constexpr (ct < 5) mma<t == 0, L0>(d, bb, x); else if constexpr (L0) mma_aa<t == 0>(d, x, bb); else mma<t == 0, false>(d, x, bb);
            SB();
          };
          if constexpr (L0) mm(acc[0][ct], XA(xn + 1)); else mm(acc[0][ct], A[0][t]);
          if constexpr (L0 && f == 26) WAITV(l0_cnt(0, 26));
          if constexpr (L0 && f == 58) WAITV(l0_cnt(22, 58));
          if constexpr (L0 && f == 90) WAITV(l0_cnt(46, 90));
          if constexpr (f == 26 || (L0 && (f == 58 || f == 67 || f == 90)) || (!L0 && f == 122)) BAR0();
          if constexpr (L0 && f == 122) BAR();
          if constexpr (((f + 6) ^ 1) < 160) ldB(IC<((f + 6) ^ 1)>{});
          if constexpr ((t & 7) >= 4 && t < 16 && ct == 3) vb[t & 3] = gaddi<61440>(vb[t & 3]);
          if constexpr ((t & 7) >= 4 && t < 16 && ct == 5) vh[t & 3] = gaddi<61440>(vh[t & 3]);
          if constexpr (L0 && f % 12 == 0 && f < 72) cp_arm(qo + (2 + f / 24) * 30720 + (f / 12 & 1) * 4096);
          if constexpr (L0 && f >= 72 && f < 104 && f % 4 == 0) cp_arm(24576 + w * 8192 + (f - 72) * 256);
          if constexpr (fr == 0 || fr == 4) cp_arm(qo + 4 * 30720 + fr * 1024);
          if constexpr (f == 124 || f == 140) cp_arm(L_WO + w * 8192 + (f - 124) * 256);
          if constexpr (!L0) {
            if constexpr (f >= 96 && f < 104 && !(f & 1)) ss[(f - 96) / 2] = gadd(asf(sp[(f - 96) / 2].x), asf(sp[(f - 96) / 2 + 4].x));
            if constexpr (f == 104 || f == 106) ss[(f - 104) / 2] = gadd(ss[(f - 104) / 2], ss[(f - 104) / 2 + 2]);
            if constexpr (f == 108) ss[0] = gadd(ss[0], ss[1]);
            if constexpr (f == 110) { ss[0] = ss[0] * (1.f / 640.f); pin(ss[0]); }
            if constexpr (f == 111) ss[1] = gmul(ss[0], ss[0]);
            if constexpr (f == 113) { sq[0] = sq[0] * (1.f / 640.f) - ss[1]; pin(sq[0]); }
            if constexpr (f == 114) { sq[0] = fmaxf(sq[0], 0.f); pin(sq[0]); }
            if constexpr (f == 116) { sq[0] += 1e-5f; pin(sq[0]); }
            if constexpr (f == 118) { sq[0] = __builtin_amdgcn_rsqf(sq[0]); pin(sq[0]); }
            if constexpr (f == 119) xa = opq_v(L_XST1 + w * 512 + l * 4);
            if constexpr (f == 120) *lp<float>(xa) = ss[0];
            if constexpr (f == 121) *lp<float>(xa + 256) = sq[0];
          } else {
            if constexpr (f == 118) {
              const float cs = ((ss[0] + ss[1]) + (ss[2] + ss[3])) + ((ss[4] + ss[5]) + (ss[6] + ss[7]));
              const v2u t2 = sw32(asu(cs), asu(cs));
              *lp<float>(L_CSQ + w * 128 + (l & 31) * 4) = asf(t2.x) + asf(t2.y);
            }
          }
          if constexpr (f == 126) xb = opq_v(L_XST1 + w * 512 + g16);
          if constexpr (f >= 127 && f < 130) mn[f - 127] = ldsf4(xb + (f - 127) * 64);
          if constexpr (f == 130 || f == 131 || f == 133) rn[f == 133 ? 2 : f - 130] = ldsf4(xb + 256 + (f == 133 ? 2 : f - 130) * 64);
          if constexpr (f == 134) xa = opq_v(L_HALO + w * 6144 + l16);
          if constexpr (f == 142) xb = opq_v(L_CSQ + g16);
          if constexpr (f >= 143 && f < 148) cst[f - 143] = ldsf4(xb + (f - 143) * 64);
          if constexpr (f == 148) xb = opq_v(xb + w * 64);
          if constexpr (f == 149) hcs = ldsf4(xb);
          if constexpr (f == 150) xa = opq_v(L_HALO + w * 1024 + l16);
          if constexpr (f == 151) xb = opq_v(L_HALO + h2 * 1024 + l16);
          if constexpr (f >= 152 && f < 156) ha[0][f - 152] = ldsf4(xa + (f - 152) * 6144);
          SB();
          if constexpr (L0) mm(acc[1][ct], XA(xn)); else mm(acc[1][ct], A[1][t]);
          if constexpr (L0) {
            if constexpr (f % 3 == 0 && f < 72) cp_one<f / 3>(dws, l16, wq + (2 + f / 24) * 30720 + (f / 12 & 1) * 4096);
            if constexpr (f == 1) flag(F0B + jb * 256 + xcd * 8);
            if constexpr (f >= 72 && f < 104 && f % 4 == 0) {
              constexpr int n = (f - 72) / 4;
              asm volatile("buffer_load_dwordx4 %0, %1, %2 offen lds" ::"v"(wv), "s"(dw), "s"((64 * pz + 16 * ((w + (n >> 1)) & 3)) * 4096 + (wk + (n & 1)) * 64) : "memory");
            }
            if constexpr (f >= 73 && f < 105 && f % 4 == 2) ss[(f - 74) / 4] = asf(__builtin_amdgcn_raw_buffer_load_b32(rws, cqo + (f - 74) * 128, CSQ_OFF + h * 8192 + w * 128, SC1));
            if constexpr (f == 86) fb = ld8<SC1>(rws, fbo, 0);
            if constexpr (f == 119) { if (!__all(l >= 32 || (fb.x == tag_lo && fb.y == tag_hi))) poll(rws, fbo, l >= 32, tag_lo, tag_hi); }
            if constexpr (f == 120 || f == 121) sfor<0, 4>([&](auto ki) AI { constexpr int n = (f - 120) * 4 + ki.v; ldsa16<n * 1024>(W[n >> 1][n & 1], wr); });
            if constexpr (f >= 130 && f < 160 && f % 4 == 2) {
              constexpr int n = (f - 130) / 4;
              sta16<(n & 1) * 1024>(W[n >> 1][n & 1], dws, l16, WS_OFF + ((4 * pz + ((w + (n >> 1)) & 3)) * 64 + wk) * 1024);
            }
          } else {
            if constexpr (q_cnt(f) > q_cnt(f - 1)) ldA(IC<q_cnt(f - 1)>{});
            if constexpr (fr >= 0 && fr < 8) cp_one<fr & 7>(dws, l16, wq + 4 * 30720 + (fr & 4) * 1024);
            if constexpr (si >= 0) sp[si] = ld8<SC1>(rws, spo + si * 128, act + A_XIN + (8 * P - 1) * XT + 20480);
            if constexpr (f >= 96 && f < 104 && (f & 1)) sq[(f - 96) / 2] = gadd(asf(sp[(f - 96) / 2].y), asf(sp[(f - 96) / 2 + 4].y));
            if constexpr (f == 105 || f == 107) sq[(f - 105) / 2] = gadd(sq[(f - 105) / 2], sq[(f - 105) / 2 + 2]);
            if constexpr (f == 109) sq[0] = gadd(sq[0], sq[1]);
          }
          if constexpr (f == 123) xa = opq_v(L_XST1 + w * 512 + c4);
          if constexpr (!L0 && f == 123) asm volatile("" ::"v"(A[0][19]));
          if constexpr (f >= 124 && f < 156 && f % 4 == 0) cp_one<(f - 124) / 4>(dws, l16, wos + w * 8192 + (f - 124) / 16 * 4096);
          if constexpr (f == 125 || f == 127 || f == 131) { constexpr int i = f == 131 ? 2 : (f - 125) / 2; mt[i] = ldsf(xa + i * 64); rt_[i] = ldsf(xa + 256 + i * 64); }
          if constexpr (f >= 136 && f < 142) *lp<f32x4>(xa + (f - 136) * 1024) = hac[f - 136];
          if constexpr (f == 143) xa = opq_v(L_CSQ + c4);
          if constexpr (f == 144) { csn[0] = ldsf(xa + 320); csn[1] = ldsf(xa + 384); }
          if constexpr (f == 146) { csn[2] = ldsf(xa + 448); xa = opq_v(xa + (h2 + 2) * 64); }
          if constexpr (f == 148) hcn = ldsf(xa);
          if constexpr (f == 150) BAR0();
          if constexpr (f >= 152 && f < 156) ha[1][f - 152] = ldsf4(xb + (f - 152) * 6144);
          if constexpr (halo) { SB(); if constexpr (L0) mm(hac[hc], XA(xn - 1)); else mm(hac[hc], Hh[t >> 2]); }
          SB();
        });
        mma_settle();
#pragma unroll
        for (int j = 0; j < 8; j++) { pin(acc[0][j]); pin(acc[1][j]); }
      };
      if (L == 0) qkv(IC<1>{}); else qkv(IC<0>{});
      STAMP(sb + 0);
      const int nbw = opq_v(L_NB + l * 8), w2s = wos + 32768 + w * 8192, w2d = L_WO + 32768 + w * 8192, w3s = wos + 65536 + w * 9216, w3d = L_WO + 65536 + w * 9216;
      cp_run<0, 3>(dws, l16, w2s, w2d);
      {
        const f32x4 a0 = (ha[0][0] + ha[0][1]) + (ha[0][2] + ha[0][3]), a1 = (ha[1][0] + ha[1][1]) + (ha[1][2] + ha[1][3]);
        *lp<v2u>(nbw + (w < 3 ? w * 512 : 15360)) = pk4((a0 - mt[2] * hcs) * rt_[2]);
        *lp<v2u>(nbw + (w != 2 ? h2 * 512 : 15360)) = pk4((a1 - mn[2] * hcn) * rn[2]);
      }
      v2u tk[2][8];
      auto fix = [&](int i, int ct) AI { tk[i][ct] = ct < 5 ? pk4((acc[i][ct] - mt[i] * cst[ct]) * rt_[i]) : pk4((acc[i][ct] - mn[i] * csn[ct - 5]) * rn[i]); };
      fix(1, 0); fix(1, 1); fix(1, 2); fix(1, 5); fix(1, 6); fix(1, 7);
      const int nbr = opq_v(nbw + w * 3072);
#pragma unroll
      for (int i = 0; i < 6; i++) *lp<v2u>(nbr + 3072 + i * 512) = tk[1][i < 3 ? i : i + 2];
      cp_run<3, 6>(dws, l16, w2s, w2d);
      SB();
      fix(0, 0); fix(0, 1); fix(0, 2); fix(1, 3); fix(1, 4);
      BAR();
      v2u nb[6];
#pragma unroll
      for (int i = 0; i < 6; i++) { nb[i] = lds8(nbr + i * 512); asm volatile("" ::: "memory"); }
      cp_run<6, 8>(dws, l16, w2s, w2d);
      STAMP(sb + 1);
      SB();
      fix(0, 3); fix(0, 4);
      v6u K6[3]; v4u Q[2][2];
      {
        const v2u qx = sw32(tk[0][2].x, tk[1][2].x), qy = sw32(tk[0][2].y, tk[1][2].y);
        const unsigned q1x = sw32(qx.y, qx.y).y, q1y = sw32(qy.y, qy.y).y;
        Q[0][0] = v4u{tk[0][3].x, tk[0][3].y, sel0(qx.y, M_G0), sel0(qy.y, M_G0)};
        Q[0][1] = v4u{sel0(qx.y, M_G1), sel0(qy.y, M_G1), tk[0][4].x, tk[0][4].y};
        Q[1][0] = v4u{tk[1][3].x, tk[1][3].y, sel0(q1x, M_G0), sel0(q1y, M_G0)};
        Q[1][1] = v4u{sel0(q1x, M_G1), sel0(q1y, M_G1), tk[1][4].x, tk[1][4].y};
      }
#pragma unroll
      for (int i = 0; i < 2; i++) {
        K6[i + 1] = v6u{tk[i][0].x, tk[i][0].y, tk[i][2].x, tk[i][2].y, tk[i][1].x, tk[i][1].y};
        pin(Q[i][0]); pin(Q[i][1]); pin(K6[i + 1]);
      }
      K6[0] = v6u{nb[0].x, nb[0].y, nb[2].x, nb[2].y, nb[1].x, nb[1].y};
      pin(K6[0]);
      NOP2();
      f32x4 S[4][2];
#pragma unroll
      for (int u = 3; u >= 0; u--)
#pragma unroll
        for (int k = 1; k >= 0; k--) {
          const v6u kk = K6[(u >> 1) + k];
          mma<true, false>(S[u][k], (u & 1) ? __builtin_shufflevector(kk, kk, 2, 3, 4, 5) : __builtin_shufflevector(kk, kk, 0, 1, 2, 3), Q[u >> 1][u & 1]);
        }
      SB();
      cp_run<0, 3>(dws, l16, w3s, w3d);
      unsigned sel[4];
      {
        const unsigned zz = 0x0c0c0c0cu, a = ::sel(0x0c0c0504u, 0x05040c0cu, M_ODD), d = ::sel(0x0c0c0706u, 0x07060c0cu, M_ODD);
        sel[2] = ::sel(zz, a, le_mask(0)); sel[3] = ::sel(zz, d, le_mask(2));
        sel[0] = ::sel(a, zz, le_mask(0)); sel[1] = ::sel(d, zz, le_mask(2));
      }
      const int ovo = (g & 1) * 768 + (g >> 1) * 256 + c16 * 16, ovt = seloob(g * OT + 512 + c16 * 16, M_LO);
      fix(0, 5); fix(0, 6); fix(0, 7);
      v6u V6[3];
#pragma unroll
      for (int j = 0; j < 3; j++) { V6[j] = v6u{nb[3 + j].x, nb[3 + j].y, tk[0][5 + j].x, tk[0][5 + j].y, tk[1][5 + j].x, tk[1][5 + j].y}; pin(V6[j]); }
      SB();
#pragma unroll
      for (int u = 0; u < 8; u++) pin(S[u >> 1][u & 1]);
      if (P + w == 0) { S[0][0] = f32x4{-1e30f, -1e30f, -1e30f, -1e30f}; pin(S[0][0]); S[1][0] = S[0][0]; }
      float s0[4], s1[4], mx[4];
#pragma unroll
      for (int u = 0; u < 4; u++) {
        const f32x4 a = S[u][0], bq = S[u][1];
        s0[u] = self(self(a[0], a[1], M_ODD), self(bq[0], bq[1], M_ODD), le_mask(0));
        s1[u] = self(self(a[2], a[3], M_ODD), self(bq[2], bq[3], M_ODD), le_mask(2));
        mx[u] = vmax(s0[u], s1[u]);
      }
      auto spread = [&](float t, float (&o)[4]) AI {
        const v2u a = sw16(asu(t), asu(t)), x = sw32(a.x, a.x), y = sw32(a.y, a.y);
        o[0] = asf(x.x); o[1] = asf(x.y); o[2] = asf(y.x); o[3] = asf(y.y);
      };
      const float kk = 0.22360679774997896f * 1.4426950408889634f;
      {
        const v2u a = sw32(asu(mx[0]), asu(mx[1])), bq = sw32(asu(mx[2]), asu(mx[3]));
        const v2u d = sw16(asu(vmax(asf(a.x), asf(a.y))), asu(vmax(asf(bq.x), asf(bq.y))));
        spread(vmax(asf(d.x), asf(d.y)), mx);
      }
      SB();
      cp_run<3, 6>(dws, l16, w3s, w3d);
      float sm4[4];
#pragma unroll
      for (int u = 0; u < 4; u++) {
        const f32x2 e = (f32x2{s0[u], s1[u]} - mx[u]) * kk;
        s0[u] = __builtin_amdgcn_exp2f(e.x); s1[u] = __builtin_amdgcn_exp2f(e.y);
        sm4[u] = s0[u] + s1[u];
      }
      {
        const v2u a = sw32(asu(sm4[0]), asu(sm4[1])), bq = sw32(asu(sm4[2]), asu(sm4[3]));
        const v2u d = sw16(asu(asf(a.x) + asf(a.y)), asu(asf(bq.x) + asf(bq.y)));
        spread(__builtin_amdgcn_rcpf(asf(d.x) + asf(d.y)), sm4);
      }
      v4u pf[4];
#pragma unroll
      for (int u = 0; u < 4; u++) {
        const f32x2 pn = f32x2{s0[u], s1[u]} * sm4[u];
        const unsigned pp = pk(pn.x, pn.y);
        pf[u] = v4u{__builtin_amdgcn_perm(pp, 0u, sel[0]), __builtin_amdgcn_perm(pp, 0u, sel[1]), __builtin_amdgcn_perm(pp, 0u, sel[2]), __builtin_amdgcn_perm(pp, 0u, sel[3])};
        pin(pf[u]);
      }
      NOP2();
      f32x4 Om[4], Ot[4];
#pragma unroll
      for (int u = 0; u < 4; u++) { const v6u v = V6[u & 1]; mma<true, false>(Om[u], (u >> 1) ? __builtin_shufflevector(v, v, 2, 3, 4, 5) : __builtin_shufflevector(v, v, 0, 1, 2, 3), pf[u]); }
#pragma unroll
      for (int u = 0; u < 4; u++) { const v6u v = V6[2]; mma<true, false>(Ot[u], (u >> 1) ? __builtin_shufflevector(v, v, 2, 3, 4, 5) : __builtin_shufflevector(v, v, 0, 1, 2, 3), pf[u]); }
      asm volatile("s_nop 3");
#pragma unroll
      for (int u = 0; u < 4; u++) pin(Om[u]);
      const int so = act + A_O + (8 * P + 2 * w) * OT + 5 * h * 256;
      st16(join16(pk4(Om[0]), pk4(Om[1])), rws, ovo, so);
      st16(join16(pk4(Om[2]), pk4(Om[3])), rws, ovo, so + OT);
      SB();
#pragma unroll
      for (int u = 0; u < 4; u++) pin(Ot[u]);
      {
        const v2u x0_ = pk4(Ot[0]), y0_ = pk4(Ot[1]), x1_ = pk4(Ot[2]), y1_ = pk4(Ot[3]);
        st16(v4u{sw16(x0_.x, x1_.x).x, sw16(x0_.y, x1_.y).x, sw16(y0_.x, y1_.x).y, sw16(y0_.y, y1_.y).y}, rws, ovt, so);
      }
      STAMP(sb + 2);
      cp_run<6, 9>(dws, l16, w3s, w3d);
      cp_run<0, 6>(dws, l16, wzs + w * 6144, w * 6144);
      WAITV(9);
      BAR();
      flag(fl + F1 + P * 256 + h * 8);
      flag((F0S + pz * 256 + cz * 8) | kill1);
      STAMP(sb + 3);
      touch(dws, (g < 2 && c16 < 10) ? g * OT + c16 * 128 : OOB, so);
      cp_run<0, 8>(dws, l16, wzs + 24576 + w * 8192, 24576 + w * 8192);
    }
    { LANE();
      f32x4 acc[2][5];
      v2u rr[5];
      const int tile = opq_s(act + A_X1 + (4 * po + w) * XT), wzs = opq_s(WI_OFF + cz * WI_T);
      if (L == 0) {
        const rsrc_t rx = mk(x0, 2048 * 1280);
#pragma unroll
        for (int j = 0; j < 5; j++) rr[j] = ld8<0>(rx, c16 * 1280 + g * 8 + j * 32, (64 * po + 16 * w) * 1280 + co * 160);
        float cs = 0.f;
#pragma unroll
        for (int i = 0; i < 16; i++) cs += asf(__builtin_amdgcn_raw_buffer_load_b32(rws, l * 4 + (i & 7) * 512, CSI_OFF + (w2 * 8 + cz) * 8192 + kh * 256 + (i >> 3) * 4096, SC1));
        *lp<float>(L_CS2 + (w * 64 + l) * 4) = cs;
      } else {
#pragma unroll
        for (int j = 0; j < 5; j++) rr[j] = ld8<SC1>(rws, (g >> 1) * 256 + c16 * 16 + (g & 1) * 8 + j * 512, tile + (A_XIN - A_X1) + co * 2560);
      }
      const int vo = opq_v((g & 1) * 512 + (g >> 1) * 256 + c16 * 16), vo4 = opq_v(seloob(vo, M_GEVEN)), vs = opq_v(seloob(c16 * 8 + (g >> 1) * 4, M_GEVEN));
      const int xw = opq_v(L_X2 + (w ^ 1) * 5120 + l16), xr = opq_v(L_X2 + w * 5120 + l16);
      const int a0 = opq_s(act + A_O + (4 * po + w) * OT + kh * 10240), a1 = opq_s(act + A_O + (4 * po + (w ^ 1)) * OT + kh * 10240);
      const int b0 = opq_v(L_WO + l16 + kh * 5120), b1 = opq_v(b0 + 51200);
      const bool pon = l < 8 || (l < 16 && L == 0);
      const int pv = opq_v(pon ? (l < 8 ? fl + F1 + (po >> 1) * 256 + kh * 64 : F0S + pz * 256 - 64) + l * 8 : OOB);
      v4u A[2][10];
      gemm_m2k2<10, 5, true, 5, true>([&](auto ti) AI { return ti.v < 5 ? b0 + ti.v * 10240 : b1 + (ti.v - 5) * 10240; }, xw, A, acc,
          [&]() AI {
#pragma unroll
        for (int j = 0; j < 5; j++) { acc[0][j] = unpk4(rr[j]); pin(acc[0][j]); }
        poll(rws, pv, !pon, tag_lo, tag_hi);
        STAMP(sb + 4);
      },
          [&](auto ii, auto ti) AI { return ld16<SC1>(rws, l16 + (ti.v & 3) * 1024, (ii.v ? a1 : a0) + (ti.v >> 2) * 4096); },
          [&](auto fi) AI { if constexpr (fi.v == 13) BAR0(); },
          [&](auto) AI {});
      STAMP(sb + 5);
      BAR();
      xchg_read<5>(xr, acc);
      store_x(acc[0], rws, tile + co * 2560, tile + 20480 + co * 128, vo, vo4, vs);
      cp_run<0, 8>(dws, l16, wzs + 71680 + w * 10240, L_Z2B + 71680 + w * 10240);
      WAITV(8);
      BAR();
      flag(fl + F2 + po * 256 + co * 8);
      STAMP(sb + 6);
      touch(dws, l < 20 ? co * 2560 + l * 128 : l == 20 ? 20480 + co * 128 : OOB, tile);
      cp_run<0, 4>(dws, l16, wzs + 57344 + ((w * 3584) & ~1023), L_Z2B + 57344 + ((w * 3584) & ~1023));
      cp_run<8, 10>(dws, l16, wzs + 71680 + w * 10240, L_Z2B + 71680 + w * 10240);
      cp_run<0, 8>(dws, l16, wzs + 112640 + qslot_off(w), L_Z2B + 112640 + qslot_off(w));
    }
    { LANE(); WSP();
      const int wz1 = opq_s(WI_OFF + (8 + cz) * WI_T);
      const int a0 = opq_s(act + A_X1 + (4 * pz + w) * XT + kh * 10240), a1 = opq_s(act + A_X1 + (4 * pz + (w ^ 1)) * XT + kh * 10240);
      int xw = opq_v(L_X3 + (w ^ 1) * 7168 + l16), xr = opq_v(L_X3 + w * 7168 + l16);
      const int gso = opq_v(seloob(l * 4, 0x1111111111111111ull)), gro = opq_v(((l >> 2) & 3) * 256 + (4 * g + (l & 3)) * 16);
      const int zvo = opq_v((g & 1) * 1024 + (g >> 1) * 256 + c16 * 16), spo = opq_v(g * 256 + c16 * 8);
      const int sta = opq_v(L_XST3 + w * 128 + (g & 1) * 64 + c16 * 4), stb = opq_v(L_XST3 + w * 128 + g * 16), tv = opq_v(l * 128);
      const int b0 = opq_v(l16 + kh * 7168), b1 = opq_v(b0 + L_Z1B + 3 * 14336), b2 = opq_v(b1 + 4 * 14336);
      static_assert(L_Z1B == L_Z2B, "");
      const int sps = opq_s(act + A_X1 + (4 * pz + w) * XT + 20480), za0 = opq_s((w * 10752) & ~1023);
      float cs2[7];
#pragma unroll
      for (int j = 0; j < 7; j++) { cs2[j] = ldsf(L_CS2 + c16 * 4 + j * 64); pin(cs2[j]); }
      braw = __builtin_amdgcn_raw_buffer_load_b16(mk(bsp, 4096), c16 * 2, (64 * pz + 16 * w) * 2, 0);
      v4u A[2][10]; f32x4 acc[2][7];
      v2u sp0, sp1, u_;
      float m1, r1, st_; f32x4 m4, r4;
      gemm_m2k2<10, 7, false, 5, false>([&](auto ti) AI { return ti.v < 4 ? b0 + ti.v * 14336 : ti.v < 7 ? b1 + (ti.v - 3) * 14336 : b2 + (ti.v - 7) * 14336; }, xw, A, acc,
          [&]() AI { poll(rws, seloob(fl + F2 + pz * 256 + l * 8, 0xFFull), l >= 8, tag_lo, tag_hi); pin(braw); STAMP(sb + 7); },
          [&](auto ii, auto ti) AI { return ld16<SC1>(rws, l16 + (ti.v & 3) * 1024, (ii.v ? a1 : a0) + (ti.v >> 2) * 4096); },
          [&](auto fi) AI {
        constexpr int f = fi.v;
        if constexpr (f == 15 || f == 31) BAR0();
        if constexpr (f == 46 || f == 54 || f == 62) cp_arm(za0 + (f - 46) * 512);
        if constexpr (f == 47) { u_ = sw32(asu(m1), asu(r1)); pin(u_); }
        if constexpr (f == 48) { st_ = gadd(asf(u_.x), asf(u_.y)); }
        if constexpr (f == 49) { u_ = sw16(asu(st_), asu(st_)); pin(u_); }
        if constexpr (f == 50) { st_ = gadd(asf(u_.x), asf(u_.y)); }
        if constexpr (f == 51) { u_ = sw32(asu(st_), asu(st_)); pin(u_); }
        if constexpr (f == 52) { m1 = asf(u_.x) * (1.f / 640.f); pin(m1); }
        if constexpr (f == 53) { st_ = gmul(m1, m1); }
        if constexpr (f == 55) { r1 = asf(u_.y) * (1.f / 640.f) - st_; pin(r1); }
        if constexpr (f == 56) { r1 = fmaxf(r1, 0.f); pin(r1); }
        if constexpr (f == 57) { r1 += 1e-5f; pin(r1); }
        if constexpr (f == 58) { r1 = __builtin_amdgcn_rsqf(r1); pin(r1); }
        if constexpr (f == 60) *lp<float>(sta) = self(r1, m1, M_GEVEN);
        if constexpr (f == 63) cp_one<1>(dws, l16, wz1 + za0 + 8192);
        if constexpr (f == 65) { m4 = ldsf4(stb); r4 = ldsf4(stb + 64); }
        if constexpr (f == 67) cp_one<2>(dws, l16, wz1 + za0 + 8192);
      },
          [&](auto fi) AI {
        constexpr int f = fi.v;
        if constexpr (f == 20) sp0 = ld8<SC1>(rws, spo, sps);
        if constexpr (f == 22) sp1 = ld8<SC1>(rws, spo + 128, sps);
        if constexpr (f == 44) { m1 = gadd(asf(sp0.x), asf(sp1.x)); }
        if constexpr (f == 45) { r1 = gadd(asf(sp0.y), asf(sp1.y)); }
        if constexpr (f >= 46 && !(f & 1)) cp_one<(f - 46) / 2>(dws, l16, wz1 + za0 + (f - 46) / 8 * 4096);
      });
      STAMP(sb + 8);
      BAR();
      xchg_read<7>(xr, acc);
      BAR();
      f32x4 z2[7], zs, zq;
      const int gs = wz1 + 43008 + w * 14336, gd = L_Z1B + 43008 + w * 14336;
      auto gel = [&](auto l0) AI {
        sfor<0, 7>([&](auto ji) AI {
          constexpr int j = ji.v;
          cp_run<2 * j, 2 * j + 2>(dws, l16, gs, gd);
          if constexpr (l0.v && j < 6) sfor<8 + 2 * j, 10 + 2 * j>(wsp_ld);
          SB();
          f32x4 v = gelu4((acc[0][j] - m4 * cs2[j]) * r4);
          if constexpr (j == 6) v *= cz < 6 ? 1.f : 0.f;
          z2[j] = v;
          if (j == 0) { zs = v; zq = v * v; } else { zs += v; zq += v * v; }
          pin(zs); pin(zq);
          SB();
        });
      };
      if (L == 0) gel(IC<1>{}); else gel(IC<0>{});
      const int sto = ST_OFF + L * ST_LAY + pz * 8192 + w * 2048;
      {
        const f32x2 t = rowsum8(zs, zq);
        st16(v4u{asu(t.x), GTAG, asu(t.y), GTAG}, rws, gso, sto + cz * 256);
      }
      STAMP(sb + 9);
      if (L == 0) sfor<20, 30>(wsp_ld);
      float ts, tq;
      {
        v4u ga, gb;
        for (unsigned spins = 0; spins < (1u << 18); ++spins) {
          asm volatile("" ::: "memory");
          ga = ld16<SC1>(rws, gro, sto); gb = ld16<SC1>(rws, gro + 1024, sto);
          if (__all(ga.y == GTAG && ga.w == GTAG && gb.y == GTAG && gb.w == GTAG)) break;
        }
        ts = asf(ga.x) + asf(gb.x); tq = asf(ga.z) + asf(gb.z);
        ts += dpp<0x128>(ts); tq += dpp<0x128>(tq); ts += dpp<0x124>(ts); tq += dpp<0x124>(tq);
      }
      STAMP(sb + 10);
      const int zo = act + A_Z + (cz * 64 + 2 * pz) * 7168;
      {
        f32x2 zz = finish_stat(ts, tq, 1.f / 864.f);
        zz.x = -zz.x * zz.y;
        const f32x4 mm = {dpp<0x00>(zz.x), dpp<0x55>(zz.x), dpp<0xAA>(zz.x), dpp<0xFF>(zz.x)};
        const f32x4 rr = {dpp<0x00>(zz.y), dpp<0x55>(zz.y), dpp<0xAA>(zz.y), dpp<0xFF>(zz.y)};
        const int so = zo + w2 * 7168 + kh * 512;
        v2u d[7];
#pragma unroll
        for (int j = 0; j < 7; j++) d[j] = pk4(z2[j] * rr + mm);
        st16(join16(d[0], d[1]), rws, zvo, so);
        st16(join16(d[2], d[3]), rws, zvo + 2048, so);
        st16(join16(d[4], d[5]), rws, zvo, so + 4096);
        st16(join16(d[6], d[6]), rws, seloob(zvo, M_GEVEN) + 2048, so + 4096);
      }
      cp_run<0, 8>(dws, l16, wz1 + 100352 + za0, L_Z1B + 100352 + za0);
      WAITV(8);
      BAR();
      flag(fl + F4 + cz * 256 + pz * 8);
      STAMP(sb + 11);
      touch(dws, seloob(tv, (1ull << 28) - 1), zo + w * 3584);
      cp_run<8, 11>(dws, l16, wz1 + 100352 + za0, L_Z1B + 100352 + za0);
      const int us = opq_s(WU_OFF + co * WU_T), ua = opq_s((w * 12800) & ~1023), ub = opq_s(51200 + w * 10240);
      f32x4 cs1[7];
      int tmp;
      xw += L_X3B - L_X3; xr += L_X3B - L_X3;
      v2u fz;
      const int fzo = opq_v(seloob(fl + F4 + cz * 256 + w * 64 + l * 8, 0xFFull));
      auto z1gemm = [&](auto l0) AI {
        constexpr bool L0 = l0.v;
        gemm_m2k2<10, 7, true, 0, false>([&](auto ti) AI { return ti.v < 3 ? b0 + ti.v * 14336 : ti.v < 7 ? b1 + (ti.v - 3) * 14336 : b2 + (ti.v - 7) * 14336; }, xw, A, acc,
            [&]() AI {}, [&](auto, auto) AI { return v4u{}; },
            [&](auto fi) AI {
          constexpr int f = fi.v;
          if constexpr (f == 22) { WAITV(L0 ? 22 : 0); BAR0(); }
          if constexpr (f == 43 || f == 57) BAR0();
          if constexpr (f >= 23 && f < 49 && (f - 23) % 8 == 0) cp_arm(ua + (f - 23) * 512);
          if constexpr (f >= 49 && f < 59 && (f - 49) % 4 == 0) cp_arm(ub + (f - 49) * 1024);
          if constexpr (f == 3) tmp = opq_v(L_CS1 + g * 16);
          if constexpr (f >= 5 && f < 19 && (f & 1)) cs1[(f - 5) / 2] = ldsf4(tmp + (f - 5) * 32);
          if constexpr (f == 63) fz = ld8<SC1>(rws, fzo, 0);
        },
            [&](auto fi) AI {
          constexpr int f = fi.v;
          if constexpr (L0 && f < 22) wsp_ld(IC<30 + f>{});
          if constexpr (f >= 24 && f < 49 && !(f & 1)) cp_one<(f - 24) / 2>(dws, l16, us + ua + (f - 24) / 8 * 4096);
          if constexpr (L0 && f >= 25 && f < 49 && (f & 1)) wsp_ld(IC<52 + (f - 25) / 2>{});
          if constexpr (f >= 49 && f < 59) cp_one<f - 49>(dws, l16, us + ub + (f - 49) / 4 * 4096);
        });
      };
      if (L == 0) z1gemm(IC<1>{}); else z1gemm(IC<0>{});
      STAMP(sb + 12);
      BAR();
      xchg_read<7>(xr, acc);
      if (!__all(l >= 8 || (fz.x == tag_lo && fz.y == tag_hi))) poll(rws, fzo, l >= 8, tag_lo, tag_hi);
      FENCE();
      STAMP(sb + 13);
      const int z0 = act + A_Z + (cz * 64 + 16 * w + (zr & 7) * 2) * 7168;
      auto ldZ0 = [&](auto fi) AI { Z[fi.v] = ld16<SC1>(rws, l16 + (fi.v & 3) * 1024, z0 + (fi.v >> 2) * 4096); };
      sfor<0, 7>([&](auto ji) AI {
        constexpr int j = ji.v;
        if constexpr (j < 4) sfor<3 * j, 3 * j + 3>(ldZ0);
        if constexpr (j == 4) sfor<12, 14>(ldZ0);
        SB();
        z1[j] = gelu4((acc[0][j] - m1 * cs1[j]) * r1);
        pin(z1[j]);
        SB();
      });
    }
    { LANE();
      const int ct0 = cz < 6 ? 7 * cz : 6 + 6 * cz, zr = MAPZ ? pz + 2 * cz + (cz >> 2) : 2 * pz + cz;
      STAMP(sb + 14);
      f32x4 acc[4][7];
      const int so = act + A_G + (4 * pz + w) * GT + ct0 * 512;
      f32x4 bias = blo(braw);
      pin(bias);
      int zp[8], za, rw[3], rd, vo; unsigned d[4];
      constexpr int RN = RZ + 1;
      v4u Y[RN];
      sfor<0, RZ>([&](auto fi) AI { Y[fi.v] = Z[fi.v]; });
      const int zb = opq_s(act + A_Z + (cz * 64 + 16 * w) * 7168);
      zp[1] = opq_s(zb + ((1 + zr) & 7) * 14336);
      NOP2();
      SB();
      sfor<0, 112>([&](auto fi) AI {
        constexpr int f = fi.v, s = sp_s(f), j = sp_j(f), fn = f + RZ;
        constexpr int q = f < 42 ? -1 : (f - 42) / 28, k = f < 42 ? 0 : (f - 42) % 28, buf = (q & 1) ? L_RED1 - L_RED0 : 0;
        auto fin = [&](auto ui) AI {
          constexpr int u = ui.v;
          if constexpr (q >= 0 && u >= 0 && u < 36) {
            constexpr int jj = 2 * q + (u < 32 ? (u >> 2) & 1 : (u - 32) >> 1), e = u & 3, st = u >> 3;
            if constexpr (st == 0) acc[0][jj][e] = gadd(acc[0][jj][e], acc[1][jj][e]);
            if constexpr (st == 1) acc[2][jj][e] = gadd(acc[2][jj][e], acc[3][jj][e]);
            if constexpr (st == 2) acc[0][jj][e] = gadd(acc[0][jj][e], acc[2][jj][e]);
            if constexpr (st == 3) acc[0][jj][e] = gmul(acc[0][jj][e], z1[jj][e]);
            if constexpr (st == 4) d[u - 32] = gcvt(acc[0][jj][(u & 1) * 2], acc[0][jj][(u & 1) * 2 + 1]);
          }
        };
        constexpr int per = q == 2 ? 7 : 4, u0 = (k - (q == 2 ? 9 : 10)) * per;
        const v4u z = Y[f % RN];
        mma<s == 0, true>(acc[1][j], z, W[1][s]);
        SB();
        if constexpr (fn < 112 && (fn >= 98 || !(fn & 1))) za = opq_s(zp[sp_s(fn) / 2] + ((sp_s(fn) & 1) * 7 + (sp_j(fn) & 6)) * 1024);
        if constexpr (q >= 0 && k >= 1 && k < 4) *lp<f32x4>(rw[k - 1] + buf) = acc[k][2 * q];
        if constexpr (q >= 0 && k >= 6 && k < 9) acc[k - 5][2 * q] = ldsf4(rd + buf + (k - 6) * 2048);
        fin(IC<u0>{});
        if constexpr (q == 2) fin(IC<u0 + 1>{});
        SB();
        mma<s == 0, true>(acc[2][j], z, W[2][s]);
        SB();
        if constexpr (fn < 112) Y[fn % RN] = ld16<SC1>(rws, l16 + (sp_j(fn) & 1) * 1024, za);
        if constexpr (q == 2) { fin(IC<u0 + 2>{}); fin(IC<u0 + 3>{}); }
        SB();
        mma<s == 0, true>(acc[3][j], z, W[3][s]);
        SB();
        if constexpr (q >= 0 && k >= 2 && k < 5) *lp<f32x4>(rw[k - 2] + buf + 1024) = acc[k - 1][2 * q + 1];
        if constexpr (q >= 0 && k >= 6 && k < 9) acc[k - 5][2 * q + 1] = ldsf4(rd + buf + (k - 6) * 2048 + 1024);
        fin(IC<u0 + per - 3>{}); fin(IC<u0 + per - 2>{});
        if constexpr (f % 4 == 1 && f / 4 + 2 < 8) zp[f / 4 + 2] = opq_s(zb + ((f / 4 + 2 + zr) & 7) * 14336);
        if constexpr (f >= 28 && f < 31) rw[f - 28] = opq_v(L_RED0 + ((((w + f - 27) & 3) * 3 + f - 28) * 2) * 1024 + l16);
        if constexpr (f == 31) rd = opq_v(L_RED0 + w * 6144 + l16);
        if constexpr (f == 32) vo = opq_v((g & 1) * 512 + c16 * 16);
        if constexpr (f == 34) vo = opq_v(vo + (g >> 1) * 256);
        if constexpr (q >= 0 && q < 2 && k == 20) { const v2u a = sw16(d[0], d[2]), e = sw16(d[1], d[3]); d[0] = a.x; d[2] = a.y; d[1] = e.x; d[3] = e.y; pin(d[0]); pin(d[1]); }
        if constexpr (q >= 0 && q < 2 && k == 21) st16(v4u{d[0], d[1], d[2], d[3]}, rws, vo + q * 1024, so);
        if constexpr (f == 38) BAR0();
        if constexpr (q >= 0 && k == 5) BAR();
        SB();
        if constexpr (s == 0) mma_c(acc[0][j], z, W[0][s], bias); else mma<false, true>(acc[0][j], z, W[0][s]);
        SB();
        fin(IC<u0 + per - 1>{});
        SB();
      });
      d[3] = gcvt(acc[0][5][2], acc[0][5][3]);
      const int rw2 = opq_v(L_RED2 + l16);
      asm volatile("s_nop 2" ::: "memory");
#pragma unroll
      for (int i = 1; i < 4; i++) pin(acc[i][6]);
      STAMP(sb + 15);
#pragma unroll
      for (int r = 1; r < 4; r++) *lp<f32x4>(rw2 + (((w + r) & 3) * 3 + r - 1) * 1024) = acc[r][6];
      st16(join16(v2u{d[0], d[1]}, v2u{d[2], d[3]}), rws, vo + 2048, so);
      BAR();
#pragma unroll
      for (int r = 1; r < 4; r++) acc[r][6] = ldsf4(rw2 + (w * 3 + r - 1) * 1024);
      pin(acc[0][6]);
      {
        const v2u dd = pk4(((acc[0][6] + acc[1][6]) + (acc[2][6] + acc[3][6])) * z1[6]);
        st16(join16(dd, dd), rws, seloob(vo, M_GEVEN) + 3072, cz < 6 ? so : OOB);
      }
      const int uc = 92160 + w * 11264;
      cp_run<0, 8>(dws, l16, WU_OFF + co * WU_T + uc, uc);
      WAITV(8);
      BAR();
      flag(fl + F5 + pz * 256 + cz * 8);
      STAMP(sb + 16);
      touch(dws, l < (cz < 6 ? 28 : 24) ? l * 128 : OOB, so);
      cp_run<8, 12>(dws, l16, WU_OFF + co * WU_T + uc, uc);
    }
    { LANE();
      f32x4 acc[2][5];
      v2u rr[5];
#pragma unroll
      for (int j = 0; j < 5; j++) rr[j] = ld8<SC1>(rws, (g >> 1) * 256 + c16 * 16 + (g & 1) * 8 + j * 512, act + A_X1 + (4 * po + w) * XT + co * 2560);
      const int vo = opq_v(L == 0 ? (g & 1) * 512 + (g >> 1) * 256 + c16 * 16 : c16 * 1280 + (g & 1) * 32 + (g >> 1) * 16);
      const int vo4 = opq_v(seloob(vo, M_GEVEN)), vs = opq_v(seloob(c16 * 8 + (g >> 1) * 4, M_GEVEN));
      const int xw = opq_v(L_X5 + (w ^ 1) * 5120 + l16), xr = opq_v(L_X5 + w * 5120 + l16);
      const int a0 = opq_s(act + A_G + (4 * po + w) * GT + kh * 14336), a1 = opq_s(act + A_G + (4 * po + (w ^ 1)) * GT + kh * 14336);
      const int k0 = opq_s(kh ? OOB : a0 + 12288), k1 = opq_s(kh ? OOB : a1 + 12288);
      const int b0 = opq_v(l16 + kh * 5120), b1 = opq_v(b0 + 61440), b2 = opq_v(b0 + 122880), b3 = opq_v(l16 + 133120);
      const int qo = opq_s(qslot_off(w)), wq = opq_s((WQ_OFF + h * WQ_T + qo) | kill1);
      const int tile = opq_s(ACT_OFF + ACT_LAY + A_XIN + (4 * po + w) * XT);
      const int pv = opq_v(seloob(fl + F5 + po * 256 + kh * 32 + l * 8, 0xFull));
      v4u A[2][14];
      gemm_m2k2<14, 5, true, 5, true>([&](auto ti) AI { return ti.v < 6 ? b0 + ti.v * 10240 : ti.v < 12 ? b1 + (ti.v - 6) * 10240 : ti.v == 12 ? b2 : b3; }, xw, A, acc,
          [&]() AI {
#pragma unroll
        for (int j = 0; j < 5; j++) { acc[0][j] = unpk4(rr[j]); pin(acc[0][j]); }
        poll(rws, pv, l >= 4, tag_lo, tag_hi);
        STAMP(sb + 17);
      },
          [&](auto ii, auto ti) AI { return ld16<SC1>(rws, l16 + (ti.v & 3) * 1024, ti.v == 13 ? (ii.v ? k1 : k0) : (ii.v ? a1 : a0) + (ti.v >> 2) * 4096); },
          [&](auto fi) AI {
        constexpr int f = fi.v;
        if constexpr (f == 21) BAR0();
        if constexpr (f == 53) { asm volatile("" ::"v"(A[0][13])); cp_arm(qo); }
        if constexpr (f == 61) cp_arm(qo + 4096);
        if constexpr (f == 66 || f == 68) cp_one<(f - 54) / 2>(dws, l16, wq + 4096);
      },
          [&](auto fi) AI {
        constexpr int f = fi.v;
        if constexpr (f >= 54 && !(f & 1)) cp_one<(f - 54) / 2>(dws, l16, wq + (f - 54) / 8 * 4096);
      });
      STAMP(sb + 18);
      BAR();
      xchg_read<5>(xr, acc);
      if (L == 1) {
        const rsrc_t ro_ = mk(out, 2048 * 1280);
        v2u d[5];
#pragma unroll
        for (int j = 0; j < 5; j++) d[j] = pk4(acc[0][j]);
        const int so = (64 * po + 16 * w) * 1280 + co * 160;
        __builtin_amdgcn_raw_buffer_store_b128(join16(d[0], d[1]), ro_, vo, so, 0);
        __builtin_amdgcn_raw_buffer_store_b128(join16(d[2], d[3]), ro_, vo + 64, so, 0);
        __builtin_amdgcn_raw_buffer_store_b128(join16(d[4], d[4]), ro_, vo4 + 128, so, 0);
        flag(b == 0 ? 0 : OOB);
        __builtin_amdgcn_endpgm();
      }
      store_x(acc[0], rws, tile + co * 2560, tile + 20480 + co * 128, vo, vo4, vs);
      cp_run<0, 8>(dws, l16, wq + 30720, qo + 30720);
      WAITV(8);
      BAR();
      flag(fl + F6 + po * 256 + co * 8);
      STAMP(sb + 19);
      touch(dws, l < 20 ? co * 2560 + l * 128 : l == 20 ? 20480 + co * 128 : OOB, tile);
      cp_run<0, 8>(dws, l16, wq + 2 * 30720, qo + 2 * 30720);
      cp_run<0, 8>(dws, l16, wq + 3 * 30720, qo + 3 * 30720);
    }
  }
}
}

extern "C" void kernel_launch(void* const* d_in, const int* in_sizes, int n_in, void* d_out, int out_size, void* d_ws, size_t ws_size, hipStream_t stream) {
  (void)hipFuncSetAttribute((const void*)fused_kernel, hipFuncAttributeMaxDynamicSharedMemorySize, LDS_BYTES);
  fused_kernel<<<256, 256, LDS_BYTES, stream>>>((const char*)d_in[0], (const char*)d_in[1], (const char*)d_in[2], (const char*)d_in[4],
                                                (const char*)d_in[5], (const char*)d_in[6], (const char*)d_in[7], (char*)d_out, (char*)d_ws);
}
```

```cpp
#include <hip/hip_runtime.h>
#include <hip/hip_bf16.h>
#include <stdint.h>

#define AI __attribute__((always_inline))
#define LAS __attribute__((address_space(3)))
#define GAS __attribute__((address_space(1)))
typedef unsigned v6u __attribute__((ext_vector_type(6)));
typedef unsigned v4u __attribute__((ext_vector_type(4)));
typedef unsigned v2u __attribute__((ext_vector_type(2)));
typedef int v4i __attribute__((ext_vector_type(4)));
typedef float f32x4 __attribute__((ext_vector_type(4)));
typedef float f32x2 __attribute__((ext_vector_type(2)));
typedef __bf16 bf2 __attribute__((ext_vector_type(2)));
typedef short s4 __attribute__((ext_vector_type(4)));
typedef unsigned long long u64;
typedef __amdgpu_buffer_rsrc_t rsrc_t;

#define PROF 0

namespace {
constexpr int SC1 = 16;
constexpr int F0C = 4096;
constexpr int F0B = F0C + 16 * 256;
constexpr int F0S = F0B + 32 * 256;
constexpr int FLAY = F0S + 32 * 256;
constexpr int F1 = 0, F2 = F1 + 16 * 256, F4 = F2 + 32 * 256, F5 = F4 + 8 * 256, F6 = F5 + 32 * 256, FLAY_SZ = F6 + 32 * 256;
constexpr int ST_OFF = 1 << 17;
constexpr int ST_LAY = 32 * 8192;
constexpr int CSQ_OFF = ST_OFF + 2 * ST_LAY;
constexpr int CSI_OFF = CSQ_OFF + 16 * 16 * 512;
constexpr int WQ_OFF = 1 << 20, WQ_T = 153600;
constexpr int WO_OFF = WQ_OFF + 16 * WQ_T, WO_T = 102400;
constexpr int WI_OFF = WO_OFF + 8 * WO_T, WI_T = 143360;
constexpr int WU_OFF = WI_OFF + 16 * WI_T, WU_T = 138240;
constexpr int WS_OFF = WU_OFF + 8 * WU_T;
constexpr int ACT_OFF = WS_OFF + 128 * 65536;
constexpr int XT = 21504, OT = 20480, GT = 27648;
constexpr int XSZ = 128 * XT, OSZ = 128 * OT, ZSZ = 8 * 64 * 7168, GSZ = 128 * GT;
constexpr int A_XIN = 0, A_O = A_XIN + XSZ, A_X1 = A_O + OSZ, A_Z = A_X1 + XSZ, A_G = A_Z + ZSZ, ACT_LAY = A_G + GSZ;
constexpr int PROF_OFF = ACT_OFF + 2 * ACT_LAY;
constexpr int WS_USED = PROF_OFF + 256 * 256 * 4;
static_assert(FLAY + 2 * FLAY_SZ <= ST_OFF, "");
static_assert(CSI_OFF + 16 * 16 * 512 <= WQ_OFF, "");
constexpr int OOB = 0x40000000;
constexpr int LDS_BYTES = 163840;
constexpr int L_TOP = 159744;
constexpr int L_CSQ = L_TOP, L_CS2 = L_CSQ + 512, L_CS1 = L_CS2 + 512, L_XST3 = L_CS1 + 512, L_JUNK = L_XST3 + 512, L_PROF = L_JUNK + 256;
constexpr int L_XST1 = 153600, L_HST = L_XST1 + 2048;
constexpr int L_HALO = 0, L_NB = 24576;
constexpr int L_WO = 57344, L_X2 = L_WO;
constexpr int L_Z2B = 73728 - 4 * 14336;
constexpr int L_Z1B = 59392 - 3 * 14336;
constexpr int L_X3 = 43008, L_X3B = 102400, L_X5 = 139264;
constexpr int L_RED0 = 98304, L_RED1 = L_RED0 + 24576, L_RED2 = L_RED1 + 24576;
static_assert(L_RED2 >= 138240 && L_RED2 + 12288 <= L_TOP, "");
#define MAPQ 1
#define MAPO 1
#define MAPZ 1
constexpr int SHQ = MAPQ ? 4 : 2, SHO = MAPO ? 8 : 4, SHZ = MAPZ ? 8 : 4;
constexpr unsigned GTAG = 0x5A5A5A5Bu;
constexpr unsigned ONE2 = 0x3f803f80u;

template <int I> struct IC { static constexpr int v = I; };
template <int I, int N, class F> __device__ __forceinline__ void sfor(F&& f) {
  if constexpr (I < N) { f(IC<I>{}); sfor<I + 1, N>(f); }
}
__device__ __forceinline__ int opq_s(int x) { asm volatile("" : "+s"(x)); return x; }
__device__ __forceinline__ int opq_u(int x) { return opq_s(__builtin_amdgcn_readfirstlane(x)); }
__device__ __forceinline__ int opq_v(int x) { asm volatile("" : "+v"(x)); return x; }
template <class T> __device__ __forceinline__ void pin(T& c) { asm volatile("" : "+v"(c)); }

template <bool FIRST> __device__ __forceinline__ void mma_aa(f32x4& c, v4u a, v4u b) {
  if constexpr (FIRST) asm volatile("v_mfma_f32_16x16x32_bf16 %0, %1, %2, 0" : "=&v"(c) : "a"(a), "v"(b));
  else asm volatile("v_mfma_f32_16x16x32_bf16 %0, %1, %2, %0" : "+v"(c) : "a"(a), "v"(b));
}
template <bool FIRST, bool BA> __device__ __forceinline__ void mma(f32x4& c, v4u a, v4u b) {
  if constexpr (FIRST) {
    if constexpr (BA) asm volatile("v_mfma_f32_16x16x32_bf16 %0, %1, %2, 0" : "=&v"(c) : "v"(a), "a"(b));
    else asm volatile("v_mfma_f32_16x16x32_bf16 %0, %1, %2, 0" : "=&v"(c) : "v"(a), "v"(b));
  } else {
    if constexpr (BA) asm volatile("v_mfma_f32_16x16x32_bf16 %0, %1, %2, %0" : "+v"(c) : "v"(a), "a"(b));
    else asm volatile("v_mfma_f32_16x16x32_bf16 %0, %1, %2, %0" : "+v"(c) : "v"(a), "v"(b));
  }
}
__device__ __forceinline__ void mma_c(f32x4& d, v4u a, v4u b, f32x4 c) { asm volatile("v_mfma_f32_16x16x32_bf16 %0, %1, %2, %3" : "=&v"(d) : "v"(a), "a"(b), "v"(c)); }
#define NOP2() asm volatile("s_nop 1")
__device__ __forceinline__ void mma_settle() { asm volatile("s_nop 7" ::: "memory"); }
__device__ __forceinline__ float dot2(unsigned a, unsigned b, float c) {
  return __builtin_amdgcn_fdot2_f32_bf16(__builtin_bit_cast(bf2, a), __builtin_bit_cast(bf2, b), c, false);
}
__device__ __forceinline__ unsigned pk(float lo, float hi) {
  f32x2 v = {lo, hi};
  return __builtin_bit_cast(unsigned, __builtin_convertvector(v, bf2));
}
__device__ __forceinline__ v2u pk4(f32x4 v) { return v2u{pk(v[0], v[1]), pk(v[2], v[3])}; }
__device__ __forceinline__ float blo(unsigned u) { return __builtin_bit_cast(float, u << 16); }
__device__ __forceinline__ float bhi(unsigned u) { return __builtin_bit_cast(float, u & 0xffff0000u); }
__device__ __forceinline__ f32x4 unpk4(v2u r) { return f32x4{blo(r.x), bhi(r.x), blo(r.y), bhi(r.y)}; }
__device__ __forceinline__ float asf(unsigned u) { return __builtin_bit_cast(float, u); }
__device__ __forceinline__ unsigned asu(float f) { return __builtin_bit_cast(unsigned, f); }
__device__ __forceinline__ unsigned sel(unsigned a, unsigned b, u64 m) { unsigned r; asm("v_cndmask_b32_e64 %0, %1, %2, %3" : "=v"(r) : "v"(a), "v"(b), "s"(m)); return r; }
__device__ __forceinline__ float self(float a, float b, u64 m) { return asf(sel(asu(a), asu(b), m)); }
__device__ __forceinline__ unsigned sel0(unsigned b, u64 m) { unsigned r; asm("v_cndmask_b32_e64 %0, 0, %1, %2" : "=v"(r) : "v"(b), "s"(m)); return r; }
__device__ __forceinline__ int seloob(int b, u64 m) { int r; asm("v_cndmask_b32_e64 %0, 2.0, %1, %2" : "=v"(r) : "v"(b), "s"(m)); return r; }
__device__ __forceinline__ float vmax(float a, float b) { return __builtin_elementwise_maximum(a, b); }
__device__ __forceinline__ float gadd(float a, float b) { float r; asm volatile("v_add_f32 %0, %1, %2" : "=v"(r) : "v"(a), "v"(b)); return r; }
__device__ __forceinline__ float gmul(float a, float b) { float r; asm volatile("v_mul_f32 %0, %1, %2" : "=v"(r) : "v"(a), "v"(b)); return r; }
__device__ __forceinline__ unsigned gcvt(float a, float b) { unsigned r; asm volatile("v_cvt_pk_bf16_f32 %0, %1, %2" : "=v"(r) : "v"(a), "v"(b)); return r; }
template <int C> __device__ __forceinline__ int gaddi(int a) { int r; asm volatile("v_add_u32 %0, %1, %2" : "=v"(r) : "n"(C), "v"(a)); return r; }
constexpr u64 M_ODD = 0xAAAAAAAAAAAAAAAAull, M_G0 = 0xFFFFull, M_G1 = 0xFFFF0000ull, M_GEVEN = 0x0000FFFF0000FFFFull, M_LO = 0xFFFFFFFFull;
constexpr u64 le_mask(int add) { u64 m = 0; for (int l = 0; l < 64; l++) if (4 * (l >> 4) + (l & 1) + add <= (l & 15)) m |= 1ull << l; return m; }
__device__ __forceinline__ rsrc_t mk(const void* p, int bytes) { return __builtin_amdgcn_make_buffer_rsrc((void*)p, 0, bytes, 0x00020000); }
__device__ __forceinline__ v4i mkd(const void* p, int bytes) {
  u64 a = (u64)p; v4i r; r.x = (int)(unsigned)a; r.y = (int)((unsigned)(a >> 32) & 0xffffu); r.z = bytes; r.w = 0x00020000; return r;
}
template <int AUX> __device__ __forceinline__ v4u ld16(rsrc_t r, int v, int s) { return __builtin_amdgcn_raw_buffer_load_b128(r, v, s, AUX); }
template <int AUX> __device__ __forceinline__ v2u ld8(rsrc_t r, int v, int s) { return __builtin_amdgcn_raw_buffer_load_b64(r, v, s, AUX); }
__device__ __forceinline__ void st16(v4u d, rsrc_t r, int v, int s) { __builtin_amdgcn_raw_buffer_store_b128(d, r, v, s, SC1); }
#define BAR() asm volatile("s_waitcnt lgkmcnt(0)\n\ts_barrier" ::: "memory")
#define BAR0() asm volatile("s_barrier" ::: "memory")
#define DRAIN() asm volatile("s_waitcnt vmcnt(0)" ::: "memory")
#define WAITV(n) asm volatile("s_waitcnt vmcnt(%0)" ::"n"(n) : "memory")
#define SB() __builtin_amdgcn_sched_barrier(0)
#define FENCE() do { asm volatile("" ::: "memory"); __builtin_amdgcn_sched_barrier(0); } while (0)
#define GL(o) "\n\tbuffer_load_dwordx4 %1, %2, %3 offen offset:" #o " sc1 lds"
#define GLARGS ::"s"(lds), "v"(voff), "s"(rs), "s"(soff)
__device__ __forceinline__ void glds_in(v4i rs, int voff, int soff, int lds) {
  asm volatile("s_mov_b32 m0, %0\n\ts_nop 0\n\tbuffer_load_dwordx4 %1, %2, %3 offen lds" GLARGS : "memory");
}
template <int N> __device__ __forceinline__ void gldsn(v4i rs, int voff, int soff, int lds) {
  static_assert(N >= 1 && N <= 4, "");
  if constexpr (N == 1) asm volatile("s_mov_b32 m0, %0\n\ts_nop 0" GL(0) GLARGS : "memory");
  if constexpr (N == 2) asm volatile("s_mov_b32 m0, %0\n\ts_nop 0" GL(0) GL(1024) GLARGS : "memory");
  if constexpr (N == 3) asm volatile("s_mov_b32 m0, %0\n\ts_nop 0" GL(0) GL(1024) GL(2048) GLARGS : "memory");
  if constexpr (N == 4) asm volatile("s_mov_b32 m0, %0\n\ts_nop 0" GL(0) GL(1024) GL(2048) GL(3072) GLARGS : "memory");
}
template <int N0, int N1> __device__ __forceinline__ void cp_run(v4i rs, int voff, int src, int dst) {
  if constexpr (N0 < N1) {
    constexpr int n = N1 - N0 < 4 ? N1 - N0 : 4;
    gldsn<n>(rs, voff, src + N0 * 1024, dst + N0 * 1024);
    cp_run<N0 + n, N1>(rs, voff, src, dst);
  }
}
__device__ __forceinline__ void cp_arm(int lds) { asm volatile("s_mov_b32 m0, %0" ::"s"(lds) : "memory"); }
template <int U> __device__ __forceinline__ void cp_one(v4i rs, int voff, int soff) {
  asm volatile("buffer_load_dwordx4 %0, %1, %2 offen offset:%3 sc1 lds" ::"v"(voff), "s"(rs), "s"(soff), "n"((U & 3) * 1024) : "memory");
}
__device__ __forceinline__ void touch(v4i rs, int voff, int soff) {
  asm volatile("s_mov_b32 m0, %0\n\ts_nop 0\n\tbuffer_load_dword %1, %2, %3 offen sc1 lds" ::"s"(L_JUNK), "v"(voff), "s"(rs), "s"(soff) : "memory");
}
template <int OFF, bool WS> __device__ __forceinline__ void lda16(v4u& d, v4i rs, int voff, int soff) {
  if constexpr (WS) asm volatile("buffer_load_dwordx4 %0, %1, %2, %3 offen offset:%4 sc1" : "+a"(d) : "v"(voff), "s"(rs), "s"(soff), "n"(OFF) : "memory");
  else asm volatile("buffer_load_dwordx4 %0, %1, %2, %3 offen offset:%4" : "+a"(d) : "v"(voff), "s"(rs), "s"(soff), "n"(OFF) : "memory");
}
template <int OFF> __device__ __forceinline__ void sta16(const v4u& d, v4i rs, int voff, int soff) {
  asm volatile("buffer_store_dwordx4 %0, %1, %2, %3 offen offset:%4 sc1" ::"a"(d), "v"(voff), "s"(rs), "s"(soff), "n"(OFF) : "memory");
}
template <int OFF> __device__ __forceinline__ void ldsa16(v4u& d, int addr) { asm volatile("ds_read_b128 %0, %1 offset:%2" : "+a"(d) : "v"(addr), "n"(OFF) : "memory"); }
__device__ __forceinline__ void park(v4u& d, v4u v) { asm volatile("" : "=a"(d) : "0"(v)); }
__device__ __forceinline__ void tch(v4i rs, int voff, int soff) {
  asm volatile("buffer_load_dword %0, %1, %2 offen sc1 lds" ::"v"(voff), "s"(rs), "s"(soff) : "memory");
}
__device__ __forceinline__ int qslot_off(int w) { return (w * 7680) & ~1023; }
template <class T> __device__ __forceinline__ LAS T* lp(int off) { return (LAS T*)(size_t)(unsigned)off; }
__device__ __forceinline__ v4u lds16(int off) { return *lp<v4u>(off); }
__device__ __forceinline__ v2u lds8(int off) { return *lp<v2u>(off); }
__device__ __forceinline__ f32x4 ldsf4(int off) { return *lp<f32x4>(off); }
__device__ __forceinline__ float ldsf(int off) { return *lp<float>(off); }
__device__ __forceinline__ void poll(rsrc_t rws, int voff, bool off, unsigned lo, unsigned hi) {
  for (unsigned spins = 0; spins < (1u << 18); ++spins) {
    asm volatile("" ::: "memory");
    const v2u v = ld8<SC1>(rws, voff, 0);
    if (__all(off || (v.x == lo && v.y == hi))) break;
  }
  asm volatile("" ::: "memory");
}
__device__ __forceinline__ v2u sw16(unsigned a, unsigned b) { auto r = __builtin_amdgcn_permlane16_swap(a, b, false, false); return v2u{r[0], r[1]}; }
__device__ __forceinline__ v2u sw32(unsigned a, unsigned b) { auto r = __builtin_amdgcn_permlane32_swap(a, b, false, false); return v2u{r[0], r[1]}; }
__device__ __forceinline__ v4u join16(v2u x, v2u y) { const v2u a = sw16(x.x, y.x), b = sw16(x.y, y.y); return v4u{a.x, b.x, a.y, b.y}; }
template <int CTRL> __device__ __forceinline__ float dpp(float v) {
  return __builtin_bit_cast(float, __builtin_amdgcn_update_dpp(0, __builtin_bit_cast(int, v), CTRL, 0xf, 0xf, true));
}
__device__ __forceinline__ float xsum2(float s, float q) {
  const v2u a = sw32(asu(s), asu(q)); const float t = asf(a.x) + asf(a.y);
  const v2u b = sw16(asu(t), asu(t)); return asf(b.x) + asf(b.y);
}
__device__ __forceinline__ f32x2 finish_stat(float s, float q, float inv_n) {
  const float m = s * inv_n;
  return f32x2{m, __builtin_amdgcn_rsqf(fmaxf(q * inv_n - m * m, 0.f) + 1e-5f)};
}
__device__ __forceinline__ f32x4 gelu4(f32x4 v) {
  const float k1 = -2.0f * 0.7978845608028654f * 1.4426950408889634f, k2 = k1 * 0.044715f;
  f32x4 e = v * ((v * v) * k2 + k1);
  asm("" : "+v"(e));
#pragma unroll
  for (int i = 0; i < 4; i++) e[i] = __builtin_amdgcn_exp2f(e[i]);
  e += 1.0f;
#pragma unroll
  for (int i = 0; i < 4; i++) e[i] = __builtin_amdgcn_rcpf(e[i]);
  return v * e;
}
__device__ __forceinline__ f32x2 rowsum8(f32x4 s, f32x4 q) {
  float a, b, c, d;
  asm volatile("s_nop 1\n\t"
      "v_add_f32_dpp %0, %4, %4 row_ror:8 row_mask:0xf bank_mask:0x3\n\t"
      "v_add_f32_dpp %1, %5, %5 row_ror:8 row_mask:0xf bank_mask:0x3\n\t"
      "v_add_f32_dpp %2, %8, %8 row_ror:8 row_mask:0xf bank_mask:0x3\n\t"
      "v_add_f32_dpp %3, %9, %9 row_ror:8 row_mask:0xf bank_mask:0x3\n\t"
      "v_add_f32_dpp %0, %6, %6 row_ror:8 row_mask:0xf bank_mask:0xc\n\t"
      "v_add_f32_dpp %1, %7, %7 row_ror:8 row_mask:0xf bank_mask:0xc\n\t"
      "v_add_f32_dpp %2, %10, %10 row_ror:8 row_mask:0xf bank_mask:0xc\n\t"
      "v_add_f32_dpp %3, %11, %11 row_ror:8 row_mask:0xf bank_mask:0xc\n\t"
      "v_add_f32_dpp %0, %0, %0 row_half_mirror row_mask:0xf bank_mask:0x5\n\t"
      "v_add_f32_dpp %0, %1, %1 row_half_mirror row_mask:0xf bank_mask:0xa\n\t"
      "v_add_f32_dpp %2, %2, %2 row_half_mirror row_mask:0xf bank_mask:0x5\n\t"
      "v_add_f32_dpp %2, %3, %3 row_half_mirror row_mask:0xf bank_mask:0xa\n\t"
      "v_add_f32_dpp %0, %0, %0 quad_perm:[1,0,3,2] row_mask:0xf bank_mask:0xf\n\t"
      "s_nop 0\n\t"
      "v_add_f32_dpp %2, %2, %2 quad_perm:[1,0,3,2] row_mask:0xf bank_mask:0xf\n\t"
      "v_add_f32_dpp %0, %0, %0 quad_perm:[2,3,0,1] row_mask:0xf bank_mask:0xf\n\t"
      "s_nop 0\n\t"
      "v_add_f32_dpp %2, %2, %2 quad_perm:[2,3,0,1] row_mask:0xf bank_mask:0xf"
      : "=&v"(a), "=&v"(b), "=&v"(c), "=&v"(d)
      : "v"(s[0]), "v"(s[1]), "v"(s[2]), "v"(s[3]), "v"(q[0]), "v"(q[1]), "v"(q[2]), "v"(q[3]));
  return f32x2{a, c};
}
__device__ __forceinline__ v4u bperm4(int idx, v4u v) {
  v4u r;
  r.x = __builtin_amdgcn_ds_bpermute(idx, v.x); r.y = __builtin_amdgcn_ds_bpermute(idx, v.y);
  r.z = __builtin_amdgcn_ds_bpermute(idx, v.z); r.w = __builtin_amdgcn_ds_bpermute(idx, v.w);
  return r;
}
__device__ __forceinline__ v2u trd(int off) { return __builtin_bit_cast(v2u, __builtin_amdgcn_ds_read_tr16_b64_v4i16(lp<s4>(off))); }
#define STAMP(i) do {} while (0)
#define CSTAMP(i) do {} while (0)
#define LANE() const int l = opq_v((int)threadIdx.x) & 63, l16 = l * 16, g = l >> 4, c16 = l & 15; (void)g; (void)c16; (void)l16; \
  const int w = opq_s(w_), po = opq_s(po_), co = opq_s(co_), pz = opq_s(pz_), cz = opq_s(cz_), P = opq_s(P_), h = opq_s(h_); \
  (void)w; (void)po; (void)co; (void)pz; (void)cz; (void)P; (void)h; \
  const int kh = w & 1, w2 = w >> 1; (void)kh; (void)w2; \
  auto flag = [&](int off) AI { __builtin_amdgcn_raw_buffer_store_b64(v2u{tag_lo, tag_hi}, rws, seloob(0, 1ull), w == 0 ? off : OOB, SC1); }; (void)flag;
#define WSP() int wsb[4]; \
  _Pragma("unroll") for (int r = 0; r < 4; r++) wsb[r] = opq_s(WS_OFF + (pz * 256 + ((w + r) & 3) * 64 + 16 * w) * 1024); \
  const int zr = opq_s(MAPZ ? pz + 2 * cz + (cz >> 2) : 2 * pz + cz); \
  auto wsp_ld = [&](auto ni) AI { constexpr int n = ni.v, e = n >> 3, r = (n >> 1) & 3, s_ = n & 1; \
    lda16<s_ * 1024, true>(W[r][2 * e + s_], dws, l16, wsb[r] + ((e + zr) & 7) * 2048); }; (void)wsp_ld;

template <int NS, int NCT, bool TR, int WIN, bool INIT, class BA, class GO, class LD, class HA, class HB>
__device__ __forceinline__ void gemm_m2k2(BA&& badr, int xw, v4u (&A)[2][NS], f32x4 (&acc)[2][NCT], GO&& go, LD&& lda, HA&& ha, HB&& hb) {
  constexpr int NF = NS * NCT, NL = NF - NCT;
  static_assert(NF % 2 == 0, "");
  v4u B[NF];
  auto ldB = [&](auto fi) AI { constexpr int f = fi.v; B[f] = lds16(badr(IC<f / NCT>{}) + (f % NCT) * 1024); };
  auto mm = [&](auto ii, auto fi) AI {
    constexpr int i = ii.v, f = fi.v, t = f / NCT, ct = f % NCT;
    if (TR) mma<t == 0 && !(INIT && i == 0), false>(acc[i][ct], B[f], A[i][t]); else mma<t == 0 && !(INIT && i == 0), false>(acc[i][ct], A[i][t], B[f]);
    SB();
  };
  sfor<0, 6>([&](auto fi) AI { ldB(IC<(fi.v ^ 1)>{}); SB(); });
  go();
  if constexpr (WIN > 0) sfor<0, WIN>([&](auto ti) AI { A[1][ti.v] = lda(IC<1>{}, ti); A[0][ti.v] = lda(IC<0>{}, ti); });
  SB();
  sfor<0, NL>([&](auto fi) AI {
    constexpr int f = fi.v;
    mm(IC<0>{}, fi);
    if constexpr (((f + 6) ^ 1) < NF) ldB(IC<((f + 6) ^ 1)>{});
    ha(fi);
    SB();
    mm(IC<1>{}, fi);
    if constexpr (WIN > 0) {
      constexpr int n = 2 * WIN + f / 2;
      if constexpr (f % 2 == 0 && n < 2 * NS) A[(n & 1) ^ 1][n >> 1] = lda(IC<((n & 1) ^ 1)>{}, IC<(n >> 1)>{});
    }
    if constexpr (f == NL - 1) sfor<NL, NF>([&](auto gi) AI { if constexpr (((gi.v + 6) ^ 1) < NF) ldB(IC<((gi.v + 6) ^ 1)>{}); });
    hb(fi);
    SB();
  });
  sfor<NL, NF>([&](auto fi) AI { mm(IC<1>{}, fi); ha(fi); SB(); });
  sfor<NL, NF>([&](auto fi) AI {
    constexpr int j = fi.v - NL, D = NCT == 5 ? 3 : 1;
    mm(IC<0>{}, fi);
    if constexpr (j >= D) { pin(acc[1][j - D]); *lp<f32x4>(xw + (j - D) * 1024) = acc[1][j - D]; }
    if constexpr (j == NCT - 1) sfor<NCT - D, NCT>([&](auto ki) AI { pin(acc[1][ki.v]); *lp<f32x4>(xw + ki.v * 1024) = acc[1][ki.v]; });
    SB();
  });
}
template <int NCT> __device__ __forceinline__ void xchg_read(int src, f32x4 (&acc)[2][NCT]) {
#pragma unroll
  for (int j = 0; j < NCT; j++) acc[1][j] = ldsf4(src + j * 1024);
  SB();
#pragma unroll
  for (int j = 0; j < NCT; j++) { pin(acc[0][j]); acc[0][j] += acc[1][j]; }
}
__device__ __forceinline__ void store_x(const f32x4 (&x)[5], rsrc_t rws, int so, int ss, int vo, int vo4, int vs) {
  v2u d[5];
#pragma unroll
  for (int j = 0; j < 5; j++) d[j] = pk4(x[j]);
  float s0 = 0.f, q0 = 0.f;
#pragma unroll
  for (int j = 0; j < 5; j++) { s0 = dot2(d[j].x, ONE2, s0); q0 = dot2(d[j].x, d[j].x, q0); s0 = dot2(d[j].y, ONE2, s0); q0 = dot2(d[j].y, d[j].y, q0); }
  st16(join16(d[0], d[1]), rws, vo, so);
  st16(join16(d[2], d[3]), rws, vo + 1024, so);
  st16(join16(d[4], d[4]), rws, vo4 + 2048, so);
  __builtin_amdgcn_raw_buffer_store_b32(asu(xsum2(s0, q0)), rws, vs, ss, SC1);
}
constexpr int QPRE = 9;
constexpr int q_step(int n) { return n % 9 == 0 ? n / 9 * 4 : n / 9 * 4 + (n % 9 - 1) / 2; }
constexpr int q_cnt(int f) { int n = QPRE; for (int i = 0; i <= f; i++) if (i % 2 == 0 && n < 45 && q_step(n) <= i / 8 + 6) n++; return n; }
constexpr int q_free(int f) { int n = 0; for (int i = 0; i < f; i += 2) if (q_cnt(i) == q_cnt(i - 1)) n++; return (f % 2 == 0 && q_cnt(f) == q_cnt(f - 1)) ? n : -100; }
constexpr int l0_vm(int f) { return (f < 72 ? f % 3 == 0 : f < 104 ? f % 4 == 0 : 0) + (f >= 73 && f < 105 && f % 4 == 2) + (f == 1) + (f == 86); }
constexpr int l0_cnt(int f0, int f1) { int n = 0; for (int i = f0; i < f1; i++) n += l0_vm(i); return n; }
constexpr int sp_s(int f) { return f < 14 ? f / 7 : f < 98 ? 2 + ((f - 14) % 28) / 2 : 2 + (f - 98); }
constexpr int sp_j(int f) { return f < 14 ? f % 7 : f < 98 ? (f - 14) / 28 * 2 + (f & 1) : 6; }
#define XA(n) W[(n) / 14][2 + (n) % 14]

__global__ __launch_bounds__(256, 1) void fused_kernel(const char* __restrict__ x0, const char* __restrict__ wqkv, const char* __restrict__ wo,
                                                       const char* __restrict__ win, const char* __restrict__ wsp, const char* __restrict__ bsp,
                                                       const char* __restrict__ wout, char* out, char* ws) {
  const int w_ = __builtin_amdgcn_readfirstlane(threadIdx.x >> 6);
  const int b = blockIdx.x, xcd = b & 7, jb = b >> 3;
  const int pa = xcd * 4 + (jb & 3), ca = jb >> 2, pb = (xcd >> 1) * 8 + (jb & 7), cb = (xcd & 1) * 4 + (jb >> 3);
  const int po_ = MAPO ? pb : pa, co_ = MAPO ? cb : ca, pz_ = MAPZ ? pb : pa, cz_ = MAPZ ? cb : ca;
  const int P_ = MAPQ ? (xcd >> 1) * 4 + (jb & 3) : xcd * 2 + (jb & 1), h_ = MAPQ ? (xcd & 1) * 8 + (jb >> 2) : jb >> 1;
  const rsrc_t rws = mk(ws, WS_USED);
  const v4i dws = mkd(ws, WS_USED);
  STAMP(0); CSTAMP(62);
  unsigned tag_lo, tag_hi;
  v4u W[4][16];
#pragma unroll
  for (int r = 0; r < 4; r++)
#pragma unroll
    for (int s = 0; s < 16; s++) asm volatile("" : "=a"(W[r][s]));
  {
    LANE();
    {
      const v4i dq = mkd(wqkv, 640 * 3840);
      const int vq = g * 3840 + c16 * 16 + (c16 < 5 ? 1280 : c16 < 10 ? -80 : 2400), s0 = (P * 40 + w * 12) * 3840 + 80 * h, kill = w == 3 ? OOB : 0;
      glds_in(dq, vq, s0, w * 3072);
      glds_in(dq, vq, (s0 + 4 * 3840) | kill, w * 3072 + 1024);
      glds_in(dq, vq, (s0 + 8 * 3840) | kill, w * 3072 + 2048);
    }
    {
      u64 pc; asm volatile("s_getpc_b64 %0" : "=s"(pc));
      touch(mkd((const void*)(pc & ~127ull), 51200), (((jb * 4 + w) & 7) * 64 + l) * 128, 0);
    }
    const int tau = jb, sl = xcd, stg = 32768 + w * 8192;
    const int ty = tau < 8 ? 0 : tau < 24 ? 1 : 2, ot = tau - ty * 8 - (ty >> 1) * 8, cc = ot & 7;
    const int o_kg = opq_u((ty < 2 ? 10 * sl : sl < 4 ? 14 * sl : 4 + 13 * sl) + w2), o_n = opq_u((ty < 2 ? 10 : sl < 4 ? 14 : 13) - w2);
    {
      const char* src = ty == 0 ? wo : ty == 1 ? win : wout;
      const int ldb = ty == 1 ? 3456 : 1280, lim = ty != 1 ? 9 : cc < 6 ? 13 : 11, chi = kh * 8 + (l & 7);
      const int col = ty != 1 ? ot * 160 : (ot < 8 ? 1728 : 0) + (cc < 6 ? 7 * cc : 6 + 6 * cc) * 32;
      const v4i dsrc = mkd(src, (ty == 2 ? 864 : 640) * ldb);
      const int ov = (l >> 3) * ldb + (chi < lim ? chi : lim) * 16;
      const int o0 = opq_u(o_kg * 8 * ldb + col), od = opq_u(16 * ldb), k5 = opq_u(o_n > 10 ? 0 : OOB), k6 = opq_u(o_n > 12 ? 0 : OOB);
      asm volatile("s_nop 4");
#pragma unroll
      for (int i = 0; i < 7; i++) glds_in(dsrc, ov, (o0 + i * od) | (i == 5 ? k5 : i == 6 ? k6 : 0), stg + i * 1024);
    }
    const v2u ep = ld8<SC1>(rws, 0, 0);
    FENCE();
    const rsrc_t rx = mk(x0, 2048 * 1280);
    const int xo = (128 * P + 32 * w) * 1280, ho = P > 0 ? (128 * P - 16) * 1280 : OOB;
    int vx[4];
#pragma unroll
    for (int i = 0; i < 4; i++) vx[i] = (l >> 2) * 1280 + (((l ^ (0 - g)) & 3) + ((i + w) & 3) * 4) * 16;
    v4u R[45];
    auto ldX = [&](auto ni) AI {
      constexpr int n = ni.v, t = q_step(n), i = (n % 9) & 1;
      R[n] = ld16<0>(rx, vx[t & 3] + (t >> 2) * 256, n % 9 == 0 ? ho : xo + i * 16 * 1280);
    };
    const int ct = 2 * w + (g & 1), pq = l & 3;
    const int cb_ = ct == 2 ? 32 + (pq & 1) * 8 + (pq >> 1) * 80 : ct == 7 ? 192 + (pq & 1) * 8 : (ct < 2 ? ct * 48 : ((ct - 1) >> 1) * 80 + ((ct - 1) & 1) * 48) + pq * 8;
    const int trq = opq_v(((g >> 1) * 8 + (c16 >> 2)) * 256 + cb_);
    int qv[3];
#pragma unroll
    for (int i = 0; i < 3; i++) {
      const int k = 2 * i + (g >> 1), kap = 5 * P + k;
      qv[i] = opq_v((k < 5 && (ct < 7 || c16 < 8)) ? (kap >> 2) * 7680 + ct * 1024 + ((kap & 3) * (ct == 7 ? 8 : 16) + c16) * 16 : OOB);
    }
    FENCE();
    sfor<0, 4>(ldX);
    FENCE();
    WAITV(13);
    STAMP(1);
    BAR0();
    {
      v2u lo[3], hi[3];
#pragma unroll
      for (int i = 0; i < 3; i++) { lo[i] = trd(trq + i * 4096); hi[i] = trd(trq + i * 4096 + 1024); }
      SB();
#pragma unroll
      for (int i = 0; i < 3; i++) st16(v4u{lo[i].x, lo[i].y, hi[i].x, hi[i].y}, rws, qv[i], WQ_OFF + h * WQ_T);
      float cs = 0.f;
#pragma unroll
      for (int i = 0; i < 3; i++) { cs = dot2(lo[i].x, ONE2, cs); cs = dot2(lo[i].y, ONE2, cs); cs = dot2(hi[i].x, ONE2, cs); cs = dot2(hi[i].y, ONE2, cs); }
      const v2u t = sw32(asu(cs), asu(cs));
      __builtin_amdgcn_raw_buffer_store_b32(asu(asf(t.x) + asf(t.y)), rws, seloob(l * 4, M_LO), CSQ_OFF + (h * 16 + P) * 512 + w * 128, SC1);
    }
    FENCE();
    sfor<4, 18>(ldX);
    FENCE();
    {
      const u64 epoch = ((u64)(unsigned)__builtin_amdgcn_readfirstlane(ep.y) << 32) | (unsigned)__builtin_amdgcn_readfirstlane(ep.x);
      const u64 tag = epoch * 0x9E3779B97F4A7C15ull + 0xD1B54A32D192ED03ull;
      tag_lo = opq_s((unsigned)tag); tag_hi = opq_s((unsigned)(tag >> 32));
    }
    WAITV(14);
    BAR0();
    flag(F0C + h * 256 + P * 8);
    STAMP(2);
    float xs[3] = {0.f, 0.f, 0.f}, xq[3] = {0.f, 0.f, 0.f};
    const int xwr = opq_v(16384 + w * 4096 + l16), xrd = opq_v(16384 + w * 4096 + c16 * 64 + ((g ^ (0 - (c16 >> 2))) & 3) * 16);
    auto doX = [&](auto ni) AI {
      constexpr int n = ni.v, i = n % 9 == 0 ? 2 : (n % 9) & 1;
      *lp<v4u>(xwr + (n & 3) * 1024) = R[n];
      ldsa16<(n & 3) * 1024>(XA(n), xrd);
#pragma unroll
      for (int e = 0; e < 4; e++) { xs[i] = dot2(R[n][e], ONE2, xs[i]); xq[i] = dot2(R[n][e], R[n][e], xq[i]); }
      pin(xs[i]); pin(xq[i]);
      SB();
    };
    sfor<18, 45>([&](auto ni) AI { ldX(ni); doX(IC<ni.v - 18>{}); });
    {
      const int tro = stg + (c16 >> 2) * 128 + g * 32 + (l & 3) * 8;
      const int o_v = kh * 4 + g < (ty == 1 ? 7 : 5) ? (kh * 4 + g) * 1024 + c16 * 16 : OOB;
      const int o_dst = ty == 0 ? WO_OFF + ot * WO_T : ty == 1 ? WI_OFF + ot * WI_T : WU_OFF + ot * WU_T, kb = ty < 2 ? 10 : 14, kstr = ty == 1 ? 7168 : 5120;
      float cs = 0.f;
#pragma unroll
      for (int i = 0; i < 7; i++) {
        const v2u lo = trd(tro + i * 1024), hi = trd(tro + i * 1024 + 512);
        if (i < 5) { cs = dot2(lo.x, ONE2, cs); cs = dot2(lo.y, ONE2, cs); cs = dot2(hi.x, ONE2, cs); cs = dot2(hi.y, ONE2, cs); }
        const int kap = o_kg + 2 * i, ks = kap >> 2, pos = ks < kb ? 2 * ks : 2 * (ks - kb) + 1;
        st16(v4u{lo.x, lo.y, hi.x, hi.y}, rws, o_v, opq_u(2 * i < o_n ? o_dst + pos * kstr + (kap & 3) * 256 : OOB));
      }
      __builtin_amdgcn_raw_buffer_store_b32(asu(cs), rws, l * 4, opq_u(ty == 1 ? CSI_OFF + (ot * 16 + sl * 2 + w2) * 512 + kh * 256 : OOB), SC1);
      st16(v4u{0, 0, 0, 0}, rws, seloob(g * ST_LAY + c16 * 16, M_LO), ST_OFF + po * 8192 + w * 2048 + co * 256);
    }
    FENCE();
    sfor<27, 45>(doX);
#pragma unroll
    for (int i = 0; i < 3; i++) { xs[i] += dpp<0xB1>(xs[i]); xq[i] += dpp<0xB1>(xq[i]); xs[i] += dpp<0x4E>(xs[i]); xq[i] += dpp<0x4E>(xq[i]); }
#pragma unroll
    for (int i = 0; i < 2; i++) {
      const f32x2 v = finish_stat(xs[i], xq[i], 1.f / 640.f);
      *lp<float>(L_XST1 + w * 512 + i * 64 + (l & 60)) = v.x; *lp<float>(L_XST1 + w * 512 + 256 + i * 64 + (l & 60)) = v.y;
    }
    *lp<f32x2>(L_HST + w * 128 + (l >> 2) * 8) = f32x2{xs[2], xq[2]};
    STAMP(3);
  }

#pragma unroll 1
  for (int Lr = 0;; Lr = 1) {
    const int L = opq_s(Lr), kill1 = L << 30;
    const int act = ACT_OFF + L * ACT_LAY, fl = FLAY + L * FLAY_SZ;
    const int sb = L * 24 + 5;
    f32x4 z1[7];
    unsigned braw;
    constexpr int RZ = 14;
    v4u Z[RZ];
    { LANE();
      const int qo = qslot_off(w), wq = WQ_OFF + h * WQ_T + qo;
      int vb[4], vh[4];
#pragma unroll
      for (int i = 0; i < 4; i++) { const int k = (i + w) & 3; vb[i] = opq_v(l16 + k * 7680); vh[i] = opq_v((g * 8 + (l & 7)) * 16 + k * 7680); }
      const int h2 = w < 2 ? w + 4 : w;
      const int c4 = opq_v(c16 * 4), g16 = opq_v(g * 16);
      const int wos = opq_s(WO_OFF + co * WO_T), wzs = opq_s(WI_OFF + cz * WI_T);
      f32x4 acc[2][8];
      float mt[3], rt_[3], csn[3], hcn; f32x4 mn[3], rn[3], cst[5], ha[2][4], hcs;
      auto qkv = [&](auto l0) AI {
        constexpr bool L0 = l0.v;
        v4u A[2][20], Hh[5], B[8];
        int va[4], spo, xoff, hoff, fbo, cqo, wv, wr, wk; v4i dw;
        auto ldB = [&](auto fi) AI { constexpr int f = fi.v, t = f >> 3, ct = f & 7; B[f & 7] = lds16((ct < 7 ? vb[t & 3] : vh[t & 3]) + ((t >> 2) & 1) * 30720 + ct * 1024); };
        auto ldA = [&](auto ni) AI {
          constexpr int n = ni.v, t = q_step(n), i = (n % 9) & 1;
          if constexpr (n % 9 == 0) Hh[t >> 2] = ld16<SC1>(rws, va[0], hoff + (t >> 2) * 4096);
          else A[i][t] = ld16<SC1>(rws, va[t & 3], xoff + i * XT + (t >> 2) * 4096);
        };
        if constexpr (L0) {
          dw = mkd(wsp, 2048 * 4096);
          wv = opq_v((l >> 2) * 4096 + ((l ^ (0 - g)) & 3) * 16);
          wr = opq_v(24576 + w * 8192 + c16 * 64 + ((g ^ (0 - (c16 >> 2))) & 3) * 16);
          wk = opq_s(16 * w + ((MAPZ ? pz + 2 * cz + (cz >> 2) : 2 * pz + cz) & 7) * 2);
          fbo = opq_v(seloob(F0B + ((l >> 3) * 8 + (((l >> 3) + 1) & 2 ? cz : co)) * 256 + (l & 7) * 8, M_LO));
          cqo = opq_v((l >> 5) * 4096 + (l & 31) * 4);
          poll(rws, seloob(F0C + h * 256 + c16 * 8, M_G0), l >= 16, tag_lo, tag_hi);
          BAR();
          STAMP(sb - 1);
          cp_run<0, 8>(dws, l16, wq, qo); cp_run<0, 8>(dws, l16, wq + 30720, qo + 30720);
          const f32x2 a = (*lp<f32x2>(L_HST + c16 * 8) + *lp<f32x2>(L_HST + 128 + c16 * 8)) + (*lp<f32x2>(L_HST + 256 + c16 * 8) + *lp<f32x2>(L_HST + 384 + c16 * 8));
          const f32x2 v = finish_stat(a.x, a.y, 1.f / 640.f);
          *lp<float>(L_XST1 + w * 512 + 128 + c4) = v.x; *lp<float>(L_XST1 + w * 512 + 384 + c4) = v.y;
          WAITV(8);
          BAR0();
          sfor<0, 6>([&](auto fi) AI { ldB(IC<(fi.v ^ 1)>{}); SB(); });
        } else {
#pragma unroll
          for (int i = 0; i < 4; i++) va[i] = opq_v(l16 + ((i + w) & 3) * 1024);
          spo = opq_v(g < 2 ? (2 * w + 1 + g) * XT + c16 * 8 : (g == 2 && P > 0) ? c16 * 8 : OOB);
          xoff = opq_s(act + A_XIN + (8 * P + 2 * w) * XT); hoff = opq_s(P > 0 ? act + A_XIN + (8 * P - 1) * XT : OOB);
          sfor<0, 6>([&](auto fi) AI { ldB(IC<(fi.v ^ 1)>{}); SB(); });
          const bool on = l < 8 || (l < 16 && P > 0);
          poll(rws, on ? FLAY + F6 + (l < 8 ? 2 * P + w2 : 2 * P - 1) * 256 + (l & 7) * 8 : OOB, !on, tag_lo, tag_hi);
          STAMP(sb - 1);
          sfor<0, QPRE>(ldA);
          SB();
        }
        f32x4 hac[6];
        v2u sp[8], fb; float ss[8], sq[8]; int xa, xb;
        sfor<0, 160>([&](auto fi) AI {
          constexpr int f = fi.v, t = f >> 3, ct = f & 7, hc = ct < 3 ? ct : ct - 2, xn = (t >> 2) * 9 + 1 + (t & 3) * 2;
          constexpr bool halo = (t & 3) == 0 && (ct < 3 || ct >= 5);
          constexpr int fr = L0 ? -100 : q_free(f);
          constexpr int si = f == 33 ? 0 : f == 37 ? 1 : f == 39 ? 2 : f == 62 ? 3 : f == 65 ? 4 : f == 68 ? 5 : f == 69 ? 6 : f == 71 ? 7 : -1; (void)si;
          const v4u bb = B[f & 7];
          auto mm = [&](f32x4& d, const v4u& x) AI {
            if constexpr (ct < 5) mma<t == 0, L0>(d, bb, x); else if constexpr (L0) mma_aa<t == 0>(d, x, bb); else mma<t == 0, false>(d, x, bb);
            SB();
          };
          if constexpr (L0) mm(acc[0][ct], XA(xn + 1)); else mm(acc[0][ct], A[0][t]);
          if constexpr (L0 && f == 26) WAITV(l0_cnt(0, 26));
          if constexpr (L0 && f == 58) WAITV(l0_cnt(22, 58));
          if constexpr (L0 && f == 90) WAITV(l0_cnt(46, 90));
          if constexpr (f == 26 || (L0 && (f == 58 || f == 67 || f == 90)) || (!L0 && f == 122)) BAR0();
          if constexpr (L0 && f == 122) BAR();
          if constexpr (((f + 6) ^ 1) < 160) ldB(IC<((f + 6) ^ 1)>{});
          if constexpr ((t & 7) >= 4 && t < 16 && ct == 3) vb[t & 3] = gaddi<61440>(vb[t & 3]);
          if constexpr ((t & 7) >= 4 && t < 16 && ct == 5) vh[t & 3] = gaddi<61440>(vh[t & 3]);
          if constexpr (L0 && f % 12 == 0 && f < 72) cp_arm(qo + (2 + f / 24) * 30720 + (f / 12 & 1) * 4096);
          if constexpr (L0 && f >= 72 && f < 104 && f % 4 == 0) cp_arm(24576 + w * 8192 + (f - 72) * 256);
          if constexpr (fr == 0 || fr == 4) cp_arm(qo + 4 * 30720 + fr * 1024);
          if constexpr (f == 124 || f == 140) cp_arm(L_WO + w * 8192 + (f - 124) * 256);
          if constexpr (!L0) {
            if constexpr (f >= 96 && f < 104 && !(f & 1)) ss[(f - 96) / 2] = gadd(asf(sp[(f - 96) / 2].x), asf(sp[(f - 96) / 2 + 4].x));
            if constexpr (f == 104 || f == 106) ss[(f - 104) / 2] = gadd(ss[(f - 104) / 2], ss[(f - 104) / 2 + 2]);
            if constexpr (f == 108) ss[0] = gadd(ss[0], ss[1]);
            if constexpr (f == 110) { ss[0] = ss[0] * (1.f / 640.f); pin(ss[0]); }
            if constexpr (f == 111) ss[1] = gmul(ss[0], ss[0]);
            if constexpr (f == 113) { sq[0] = sq[0] * (1.f / 640.f) - ss[1]; pin(sq[0]); }
            if constexpr (f == 114) { sq[0] = fmaxf(sq[0], 0.f); pin(sq[0]); }
            if constexpr (f == 116) { sq[0] += 1e-5f; pin(sq[0]); }
            if constexpr (f == 118) { sq[0] = __builtin_amdgcn_rsqf(sq[0]); pin(sq[0]); }
            if constexpr (f == 119) xa = opq_v(L_XST1 + w * 512 + l * 4);
            if constexpr (f == 120) *lp<float>(xa) = ss[0];
            if constexpr (f == 121) *lp<float>(xa + 256) = sq[0];
          } else {
            if constexpr (f == 118) {
              const float cs = ((ss[0] + ss[1]) + (ss[2] + ss[3])) + ((ss[4] + ss[5]) + (ss[6] + ss[7]));
              const v2u t2 = sw32(asu(cs), asu(cs));
              *lp<float>(L_CSQ + w * 128 + (l & 31) * 4) = asf(t2.x) + asf(t2.y);
            }
          }
          if constexpr (f == 126) xb = opq_v(L_XST1 + w * 512 + g16);
          if constexpr (f >= 127 && f < 130) mn[f - 127] = ldsf4(xb + (f - 127) * 64);
          if constexpr (f == 130 || f == 131 || f == 133) rn[f == 133 ? 2 : f - 130] = ldsf4(xb + 256 + (f == 133 ? 2 : f - 130) * 64);
          if constexpr (f == 134) xa = opq_v(L_HALO + w * 6144 + l16);
          if constexpr (f == 142) xb = opq_v(L_CSQ + g16);
          if constexpr (f >= 143 && f < 148) cst[f - 143] = ldsf4(xb + (f - 143) * 64);
          if constexpr (f == 148) xb = opq_v(xb + w * 64);
          if constexpr (f == 149) hcs = ldsf4(xb);
          if constexpr (f == 150) xa = opq_v(L_HALO + w * 1024 + l16);
          if constexpr (f == 151) xb = opq_v(L_HALO + h2 * 1024 + l16);
          if constexpr (f >= 152 && f < 156) ha[0][f - 152] = ldsf4(xa + (f - 152) * 6144);
          SB();
          if constexpr (L0) mm(acc[1][ct], XA(xn)); else mm(acc[1][ct], A[1][t]);
          if constexpr (L0) {
            if constexpr (f % 3 == 0 && f < 72) cp_one<f / 3>(dws, l16, wq + (2 + f / 24) * 30720 + (f / 12 & 1) * 4096);
            if constexpr (f == 1) flag(F0B + jb * 256 + xcd * 8);
            if constexpr (f >= 72 && f < 104 && f % 4 == 0) {
              constexpr int n = (f - 72) / 4;
              asm volatile("buffer_load_dwordx4 %0, %1, %2 offen lds" ::"v"(wv), "s"(dw), "s"((64 * pz + 16 * ((w + (n >> 1)) & 3)) * 4096 + (wk + (n & 1)) * 64) : "memory");
            }
            if constexpr (f >= 73 && f < 105 && f % 4 == 2) ss[(f - 74) / 4] = asf(__builtin_amdgcn_raw_buffer_load_b32(rws, cqo + (f - 74) * 128, CSQ_OFF + h * 8192 + w * 128, SC1));
            if constexpr (f == 86) fb = ld8<SC1>(rws, fbo, 0);
            if constexpr (f == 119) { if (!__all(l >= 32 || (fb.x == tag_lo && fb.y == tag_hi))) poll(rws, fbo, l >= 32, tag_lo, tag_hi); }
            if constexpr (f == 120 || f == 121) sfor<0, 4>([&](auto ki) AI { constexpr int n = (f - 120) * 4 + ki.v; ldsa16<n * 1024>(W[n >> 1][n & 1], wr); });
            if constexpr (f >= 130 && f < 160 && f % 4 == 2) {
              constexpr int n = (f - 130) / 4;
              sta16<(n & 1) * 1024>(W[n >> 1][n & 1], dws, l16, WS_OFF + ((4 * pz + ((w + (n >> 1)) & 3)) * 64 + wk) * 1024);
            }
          } else {
            if constexpr (q_cnt(f) > q_cnt(f - 1)) ldA(IC<q_cnt(f - 1)>{});
            if constexpr (fr >= 0 && fr < 8) cp_one<fr & 7>(dws, l16, wq + 4 * 30720 + (fr & 4) * 1024);
            if constexpr (si >= 0) sp[si] = ld8<SC1>(rws, spo + si * 128, act + A_XIN + (8 * P - 1) * XT + 20480);
            if constexpr (f >= 96 && f < 104 && (f & 1)) sq[(f - 96) / 2] = gadd(asf(sp[(f - 96) / 2].y), asf(sp[(f - 96) / 2 + 4].y));
            if constexpr (f == 105 || f == 107) sq[(f - 105) / 2] = gadd(sq[(f - 105) / 2], sq[(f - 105) / 2 + 2]);
            if constexpr (f == 109) sq[0] = gadd(sq[0], sq[1]);
          }
          if constexpr (f == 123) xa = opq_v(L_XST1 + w * 512 + c4);
          if constexpr (!L0 && f == 123) asm volatile("" ::"v"(A[0][19]));
          if constexpr (f >= 124 && f < 156 && f % 4 == 0) cp_one<(f - 124) / 4>(dws, l16, wos + w * 8192 + (f - 124) / 16 * 4096);
          if constexpr (f == 125 || f == 127 || f == 131) { constexpr int i = f == 131 ? 2 : (f - 125) / 2; mt[i] = ldsf(xa + i * 64); rt_[i] = ldsf(xa + 256 + i * 64); }
          if constexpr (f >= 136 && f < 142) *lp<f32x4>(xa + (f - 136) * 1024) = hac[f - 136];
          if constexpr (f == 143) xa = opq_v(L_CSQ + c4);
          if constexpr (f == 144) { csn[0] = ldsf(xa + 320); csn[1] = ldsf(xa + 384); }
          if constexpr (f == 146) { csn[2] = ldsf(xa + 448); xa = opq_v(xa + (h2 + 2) * 64); }
          if constexpr (f == 148) hcn = ldsf(xa);
          if constexpr (f == 150) BAR0();
          if constexpr (f >= 152 && f < 156) ha[1][f - 152] = ldsf4(xb + (f - 152) * 6144);
          if constexpr (halo) { SB(); if constexpr (L0) mm(hac[hc], XA(xn - 1)); else mm(hac[hc], Hh[t >> 2]); }
          SB();
        });
        mma_settle();
#pragma unroll
        for (int j = 0; j < 8; j++) { pin(acc[0][j]); pin(acc[1][j]); }
      };
      if (L == 0) qkv(IC<1>{}); else qkv(IC<0>{});
      STAMP(sb + 0);
      const int nbw = opq_v(L_NB + l * 8), w2s = wos + 32768 + w * 8192, w2d = L_WO + 32768 + w * 8192, w3s = wos + 65536 + w * 9216, w3d = L_WO + 65536 + w * 9216;
      cp_run<0, 3>(dws, l16, w2s, w2d);
      {
        const f32x4 a0 = (ha[0][0] + ha[0][1]) + (ha[0][2] + ha[0][3]), a1 = (ha[1][0] + ha[1][1]) + (ha[1][2] + ha[1][3]);
        *lp<v2u>(nbw + (w < 3 ? w * 512 : 15360)) = pk4((a0 - mt[2] * hcs) * rt_[2]);
        *lp<v2u>(nbw + (w != 2 ? h2 * 512 : 15360)) = pk4((a1 - mn[2] * hcn) * rn[2]);
      }
      v2u tk[2][8];
      auto fix = [&](int i, int ct) AI { tk[i][ct] = ct < 5 ? pk4((acc[i][ct] - mt[i] * cst[ct]) * rt_[i]) : pk4((acc[i][ct] - mn[i] * csn[ct - 5]) * rn[i]); };
      fix(1, 0); fix(1, 1); fix(1, 2); fix(1, 5); fix(1, 6); fix(1, 7);
      const int nbr = opq_v(nbw + w * 3072);
#pragma unroll
      for (int i = 0; i < 6; i++) *lp<v2u>(nbr + 3072 + i * 512) = tk[1][i < 3 ? i : i + 2];
      cp_run<3, 6>(dws, l16, w2s, w2d);
      SB();
      fix(0, 0); fix(0, 1); fix(0, 2); fix(1, 3); fix(1, 4);
      BAR();
      v2u nb[6];
#pragma unroll
      for (int i = 0; i < 6; i++) { nb[i] = lds8(nbr + i * 512); asm volatile("" ::: "memory"); }
      cp_run<6, 8>(dws, l16, w2s, w2d);
      STAMP(sb + 1);
      SB();
      fix(0, 3); fix(0, 4);
      v6u K6[3]; v4u Q[2][2];
      {
        const v2u qx = sw32(tk[0][2].x, tk[1][2].x), qy = sw32(tk[0][2].y, tk[1][2].y);
        const unsigned q1x = sw32(qx.y, qx.y).y, q1y = sw32(qy.y, qy.y).y;
        Q[0][0] = v4u{tk[0][3].x, tk[0][3].y, sel0(qx.y, M_G0), sel0(qy.y, M_G0)};
        Q[0][1] = v4u{sel0(qx.y, M_G1), sel0(qy.y, M_G1), tk[0][4].x, tk[0][4].y};
        Q[1][0] = v4u{tk[1][3].x, tk[1][3].y, sel0(q1x, M_G0), sel0(q1y, M_G0)};
        Q[1][1] = v4u{sel0(q1x, M_G1), sel0(q1y, M_G1), tk[1][4].x, tk[1][4].y};
      }
#pragma unroll
      for (int i = 0; i < 2; i++) {
        K6[i + 1] = v6u{tk[i][0].x, tk[i][0].y, tk[i][2].x, tk[i][2].y, tk[i][1].x, tk[i][1].y};
        pin(Q[i][0]); pin(Q[i][1]); pin(K6[i + 1]);
      }
      K6[0] = v6u{nb[0].x, nb[0].y, nb[2].x, nb[2].y, nb[1].x, nb[1].y};
      pin(K6[0]);
      NOP2();
      f32x4 S[4][2];
#pragma unroll
      for (int u = 3; u >= 0; u--)
#pragma unroll
        for (int k = 1; k >= 0; k--) {
          const v6u kk = K6[(u >> 1) + k];
          mma<true, false>(S[u][k], (u & 1) ? __builtin_shufflevector(kk, kk, 2, 3, 4, 5) : __builtin_shufflevector(kk, kk, 0, 1, 2, 3), Q[u >> 1][u & 1]);
        }
      SB();
      cp_run<0, 3>(dws, l16, w3s, w3d);
      unsigned sel[4];
      {
        const unsigned zz = 0x0c0c0c0cu, a = ::sel(0x0c0c0504u, 0x05040c0cu, M_ODD), d = ::sel(0x0c0c0706u, 0x07060c0cu, M_ODD);
        sel[2] = ::sel(zz, a, le_mask(0)); sel[3] = ::sel(zz, d, le_mask(2));
        sel[0] = ::sel(a, zz, le_mask(0)); sel[1] = ::sel(d, zz, le_mask(2));
      }
      const int ovo = (g & 1) * 768 + (g >> 1) * 256 + c16 * 16, ovt = seloob(g * OT + 512 + c16 * 16, M_LO);
      fix(0, 5); fix(0, 6); fix(0, 7);
      v6u V6[3];
#pragma unroll
      for (int j = 0; j < 3; j++) { V6[j] = v6u{nb[3 + j].x, nb[3 + j].y, tk[0][5 + j].x, tk[0][5 + j].y, tk[1][5 + j].x, tk[1][5 + j].y}; pin(V6[j]); }
      SB();
#pragma unroll
      for (int u = 0; u < 8; u++) pin(S[u >> 1][u & 1]);
      if (P + w == 0) { S[0][0] = f32x4{-1e30f, -1e30f, -1e30f, -1e30f}; pin(S[0][0]); S[1][0] = S[0][0]; }
      float s0[4], s1[4], mx[4];
#pragma unroll
      for (int u = 0; u < 4; u++) {
        const f32x4 a = S[u][0], bq = S[u][1];
        s0[u] = self(self(a[0], a[1], M_ODD), self(bq[0], bq[1], M_ODD), le_mask(0));
        s1[u] = self(self(a[2], a[3], M_ODD), self(bq[2], bq[3], M_ODD), le_mask(2));
        mx[u] = vmax(s0[u], s1[u]);
      }
      auto spread = [&](float t, float (&o)[4]) AI {
        const v2u a = sw16(asu(t), asu(t)), x = sw32(a.x, a.x), y = sw32(a.y, a.y);
        o[0] = asf(x.x); o[1] = asf(x.y); o[2] = asf(y.x); o[3] = asf(y.y);
      };
      const float kk = 0.22360679774997896f * 1.4426950408889634f;
      {
        const v2u a = sw32(asu(mx[0]), asu(mx[1])), bq = sw32(asu(mx[2]), asu(mx[3]));
        const v2u d = sw16(asu(vmax(asf(a.x), asf(a.y))), asu(vmax(asf(bq.x), asf(bq.y))));
        spread(vmax(asf(d.x), asf(d.y)), mx);
      }
      SB();
      cp_run<3, 6>(dws, l16, w3s, w3d);
      float sm4[4];
#pragma unroll
      for (int u = 0; u < 4; u++) {
        const f32x2 e = (f32x2{s0[u], s1[u]} - mx[u]) * kk;
        s0[u] = __builtin_amdgcn_exp2f(e.x); s1[u] = __builtin_amdgcn_exp2f(e.y);
        sm4[u] = s0[u] + s1[u];
      }
      {
        const v2u a = sw32(asu(sm4[0]), asu(sm4[1])), bq = sw32(asu(sm4[2]), asu(sm4[3]));
        const v2u d = sw16(asu(asf(a.x) + asf(a.y)), asu(asf(bq.x) + asf(bq.y)));
        spread(__builtin_amdgcn_rcpf(asf(d.x) + asf(d.y)), sm4);
      }
      v4u pf[4];
#pragma unroll
      for (int u = 0; u < 4; u++) {
        const f32x2 pn = f32x2{s0[u], s1[u]} * sm4[u];
        const unsigned pp = pk(pn.x, pn.y);
        pf[u] = v4u{__builtin_amdgcn_perm(pp, 0u, sel[0]), __builtin_amdgcn_perm(pp, 0u, sel[1]), __builtin_amdgcn_perm(pp, 0u, sel[2]), __builtin_amdgcn_perm(pp, 0u, sel[3])};
        pin(pf[u]);
      }
      NOP2();
      f32x4 Om[4], Ot[4];
#pragma unroll
      for (int u = 0; u < 4; u++) { const v6u v = V6[u & 1]; mma<true, false>(Om[u], (u >> 1) ? __builtin_shufflevector(v, v, 2, 3, 4, 5) : __builtin_shufflevector(v, v, 0, 1, 2, 3), pf[u]); }
#pragma unroll
      for (int u = 0; u < 4; u++) { const v6u v = V6[2]; mma<true, false>(Ot[u], (u >> 1) ? __builtin_shufflevector(v, v, 2, 3, 4, 5) : __builtin_shufflevector(v, v, 0, 1, 2, 3), pf[u]); }
      asm volatile("s_nop 3");
#pragma unroll
      for (int u = 0; u < 4; u++) pin(Om[u]);
      const int so = act + A_O + (8 * P + 2 * w) * OT + 5 * h * 256;
      st16(join16(pk4(Om[0]), pk4(Om[1])), rws, ovo, so);
      st16(join16(pk4(Om[2]), pk4(Om[3])), rws, ovo, so + OT);
      SB();
#pragma unroll
      for (int u = 0; u < 4; u++) pin(Ot[u]);
      {
        const v2u x0_ = pk4(Ot[0]), y0_ = pk4(Ot[1]), x1_ = pk4(Ot[2]), y1_ = pk4(Ot[3]);
        st16(v4u{sw16(x0_.x, x1_.x).x, sw16(x0_.y, x1_.y).x, sw16(y0_.x, y1_.x).y, sw16(y0_.y, y1_.y).y}, rws, ovt, so);
      }
      STAMP(sb + 2);
      cp_run<6, 9>(dws, l16, w3s, w3d);
      cp_run<0, 6>(dws, l16, wzs + w * 6144, w * 6144);
      WAITV(9);
      BAR();
      flag(fl + F1 + P * 256 + h * 8);
      flag((F0S + pz * 256 + cz * 8) | kill1);
      STAMP(sb + 3);
      touch(dws, (g < 2 && c16 < 10) ? g * OT + c16 * 128 : OOB, so);
      cp_run<0, 8>(dws, l16, wzs + 24576 + w * 8192, 24576 + w * 8192);
    }
    { LANE();
      f32x4 acc[2][5];
      v2u rr[5];
      const int tile = opq_s(act + A_X1 + (4 * po + w) * XT), wzs = opq_s(WI_OFF + cz * WI_T);
      if (L == 0) {
        const rsrc_t rx = mk(x0, 2048 * 1280);
#pragma unroll
        for (int j = 0; j < 5; j++) rr[j] = ld8<0>(rx, c16 * 1280 + g * 8 + j * 32, (64 * po + 16 * w) * 1280 + co * 160);
        float cs = 0.f;
#pragma unroll
        for (int i = 0; i < 16; i++) cs += asf(__builtin_amdgcn_raw_buffer_load_b32(rws, l * 4 + (i & 7) * 512, CSI_OFF + (w2 * 8 + cz) * 8192 + kh * 256 + (i >> 3) * 4096, SC1));
        *lp<float>(L_CS2 + (w * 64 + l) * 4) = cs;
      } else {
#pragma unroll
        for (int j = 0; j < 5; j++) rr[j] = ld8<SC1>(rws, (g >> 1) * 256 + c16 * 16 + (g & 1) * 8 + j * 512, tile + (A_XIN - A_X1) + co * 2560);
      }
      const int vo = opq_v((g & 1) * 512 + (g >> 1) * 256 + c16 * 16), vo4 = opq_v(seloob(vo, M_GEVEN)), vs = opq_v(seloob(c16 * 8 + (g >> 1) * 4, M_GEVEN));
      const int xw = opq_v(L_X2 + (w ^ 1) * 5120 + l16), xr = opq_v(L_X2 + w * 5120 + l16);
      const int a0 = opq_s(act + A_O + (4 * po + w) * OT + kh * 10240), a1 = opq_s(act + A_O + (4 * po + (w ^ 1)) * OT + kh * 10240);
      const int b0 = opq_v(L_WO + l16 + kh * 5120), b1 = opq_v(b0 + 51200);
      const bool pon = l < 8 || (l < 16 && L == 0);
      const int pv = opq_v(pon ? (l < 8 ? fl + F1 + (po >> 1) * 256 + kh * 64 : F0S + pz * 256 - 64) + l * 8 : OOB);
      v4u A[2][10];
      gemm_m2k2<10, 5, true, 5, true>([&](auto ti) AI { return ti.v < 5 ? b0 + ti.v * 10240 : b1 + (ti.v - 5) * 10240; }, xw, A, acc,
          [&]() AI {
#pragma unroll
        for (int j = 0; j < 5; j++) { acc[0][j] = unpk4(rr[j]); pin(acc[0][j]); }
        poll(rws, pv, !pon, tag_lo, tag_hi);
        STAMP(sb + 4);
      },
          [&](auto ii, auto ti) AI { return ld16<SC1>(rws, l16 + (ti.v & 3) * 1024, (ii.v ? a1 : a0) + (ti.v >> 2) * 4096); },
          [&](auto fi) AI { if constexpr (fi.v == 13) BAR0(); },
          [&](auto) AI {});
      STAMP(sb + 5);
      BAR();
      xchg_read<5>(xr, acc);
      store_x(acc[0], rws, tile + co * 2560, tile + 20480 + co * 128, vo, vo4, vs);
      cp_run<0, 8>(dws, l16, wzs + 71680 + w * 10240, L_Z2B + 71680 + w * 10240);
      WAITV(8);
      BAR();
      flag(fl + F2 + po * 256 + co * 8);
      STAMP(sb + 6);
      touch(dws, l < 20 ? co * 2560 + l * 128 : l == 20 ? 20480 + co * 128 : OOB, tile);
      cp_run<0, 4>(dws, l16, wzs + 57344 + ((w * 3584) & ~1023), L_Z2B + 57344 + ((w * 3584) & ~1023));
      cp_run<8, 10>(dws, l16, wzs + 71680 + w * 10240, L_Z2B + 71680 + w * 10240);
      cp_run<0, 8>(dws, l16, wzs + 112640 + qslot_off(w), L_Z2B + 112640 + qslot_off(w));
    }
    { LANE(); WSP();
      const int wz1 = opq_s(WI_OFF + (8 + cz) * WI_T);
      const int a0 = opq_s(act + A_X1 + (4 * pz + w) * XT + kh * 10240), a1 = opq_s(act + A_X1 + (4 * pz + (w ^ 1)) * XT + kh * 10240);
      int xw = opq_v(L_X3 + (w ^ 1) * 7168 + l16), xr = opq_v(L_X3 + w * 7168 + l16);
      const int gso = opq_v(seloob(l * 4, 0x1111111111111111ull)), gro = opq_v(((l >> 2) & 3) * 256 + (4 * g + (l & 3)) * 16);
      const int zvo = opq_v((g & 1) * 1024 + (g >> 1) * 256 + c16 * 16), spo = opq_v(g * 256 + c16 * 8);
      const int sta = opq_v(L_XST3 + w * 128 + (g & 1) * 64 + c16 * 4), stb = opq_v(L_XST3 + w * 128 + g * 16), tv = opq_v(l * 128);
      const int b0 = opq_v(l16 + kh * 7168), b1 = opq_v(b0 + L_Z1B + 3 * 14336), b2 = opq_v(b1 + 4 * 14336);
      static_assert(L_Z1B == L_Z2B, "");
      const int sps = opq_s(act + A_X1 + (4 * pz + w) * XT + 20480), za0 = opq_s((w * 10752) & ~1023);
      float cs2[7];
#pragma unroll
      for (int j = 0; j < 7; j++) { cs2[j] = ldsf(L_CS2 + c16 * 4 + j * 64); pin(cs2[j]); }
      braw = __builtin_amdgcn_raw_buffer_load_b16(mk(bsp, 4096), c16 * 2, (64 * pz + 16 * w) * 2, 0);
      v4u A[2][10]; f32x4 acc[2][7];
      v2u sp0, sp1, u_;
      float m1, r1, st_; f32x4 m4, r4;
      gemm_m2k2<10, 7, false, 5, false>([&](auto ti) AI { return ti.v < 4 ? b0 + ti.v * 14336 : ti.v < 7 ? b1 + (ti.v - 3) * 14336 : b2 + (ti.v - 7) * 14336; }, xw, A, acc,
          [&]() AI { poll(rws, seloob(fl + F2 + pz * 256 + l * 8, 0xFFull), l >= 8, tag_lo, tag_hi); pin(braw); STAMP(sb + 7); },
          [&](auto ii, auto ti) AI { return ld16<SC1>(rws, l16 + (ti.v & 3) * 1024, (ii.v ? a1 : a0) + (ti.v >> 2) * 4096); },
          [&](auto fi) AI {
        constexpr int f = fi.v;
        if constexpr (f == 15 || f == 31) BAR0();
        if constexpr (f == 46 || f == 54 || f == 62) cp_arm(za0 + (f - 46) * 512);
        if constexpr (f == 47) { u_ = sw32(asu(m1), asu(r1)); pin(u_); }
        if constexpr (f == 48) { st_ = gadd(asf(u_.x), asf(u_.y)); }
        if constexpr (f == 49) { u_ = sw16(asu(st_), asu(st_)); pin(u_); }
        if constexpr (f == 50) { st_ = gadd(asf(u_.x), asf(u_.y)); }
        if constexpr (f == 51) { u_ = sw32(asu(st_), asu(st_)); pin(u_); }
        if constexpr (f == 52) { m1 = asf(u_.x) * (1.f / 640.f); pin(m1); }
        if constexpr (f == 53) { st_ = gmul(m1, m1); }
        if constexpr (f == 55) { r1 = asf(u_.y) * (1.f / 640.f) - st_; pin(r1); }
        if constexpr (f == 56) { r1 = fmaxf(r1, 0.f); pin(r1); }
        if constexpr (f == 57) { r1 += 1e-5f; pin(r1); }
        if constexpr (f == 58) { r1 = __builtin_amdgcn_rsqf(r1); pin(r1); }
        if constexpr (f == 60) *lp<float>(sta) = self(r1, m1, M_GEVEN);
        if constexpr (f == 63) cp_one<1>(dws, l16, wz1 + za0 + 8192);
        if constexpr (f == 65) { m4 = ldsf4(stb); r4 = ldsf4(stb + 64); }
        if constexpr (f == 67) cp_one<2>(dws, l16, wz1 + za0 + 8192);
      },
          [&](auto fi) AI {
        constexpr int f = fi.v;
        if constexpr (f == 20) sp0 = ld8<SC1>(rws, spo, sps);
        if constexpr (f == 22) sp1 = ld8<SC1>(rws, spo + 128, sps);
        if constexpr (f == 44) { m1 = gadd(asf(sp0.x), asf(sp1.x)); }
        if constexpr (f == 45) { r1 = gadd(asf(sp0.y), asf(sp1.y)); }
        if constexpr (f >= 46 && !(f & 1)) cp_one<(f - 46) / 2>(dws, l16, wz1 + za0 + (f - 46) / 8 * 4096);
      });
      STAMP(sb + 8);
      BAR();
      xchg_read<7>(xr, acc);
      BAR();
      f32x4 z2[7], zs, zq;
      const int gs = wz1 + 43008 + w * 14336, gd = L_Z1B + 43008 + w * 14336;
      auto gel = [&](auto l0) AI {
        sfor<0, 7>([&](auto ji) AI {
          constexpr int j = ji.v;
          cp_run<2 * j, 2 * j + 2>(dws, l16, gs, gd);
          if constexpr (l0.v && j < 6) sfor<8 + 2 * j, 10 + 2 * j>(wsp_ld);
          SB();
          f32x4 v = gelu4((acc[0][j] - m4 * cs2[j]) * r4);
          if constexpr (j == 6) v *= cz < 6 ? 1.f : 0.f;
          z2[j] = v;
          if (j == 0) { zs = v; zq = v * v; } else { zs += v; zq += v * v; }
          pin(zs); pin(zq);
          SB();
        });
      };
      if (L == 0) gel(IC<1>{}); else gel(IC<0>{});
      const int sto = ST_OFF + L * ST_LAY + pz * 8192 + w * 2048;
      {
        const f32x2 t = rowsum8(zs, zq);
        st16(v4u{asu(t.x), GTAG, asu(t.y), GTAG}, rws, gso, sto + cz * 256);
      }
      STAMP(sb + 9);
      if (L == 0) sfor<20, 30>(wsp_ld);
      float ts, tq;
      {
        v4u ga, gb;
        for (unsigned spins = 0; spins < (1u << 18); ++spins) {
          asm volatile("" ::: "memory");
          ga = ld16<SC1>(rws, gro, sto); gb = ld16<SC1>(rws, gro + 1024, sto);
          if (__all(ga.y == GTAG && ga.w == GTAG && gb.y == GTAG && gb.w == GTAG)) break;
        }
        ts = asf(ga.x) + asf(gb.x); tq = asf(ga.z) + asf(gb.z);
        ts += dpp<0x128>(ts); tq += dpp<0x128>(tq); ts += dpp<0x124>(ts); tq += dpp<0x124>(tq);
      }
      STAMP(sb + 10);
      const int zo = act + A_Z + (cz * 64 + 2 * pz) * 7168;
      {
        f32x2 zz = finish_stat(ts, tq, 1.f / 864.f);
        zz.x = -zz.x * zz.y;
        const f32x4 mm = {dpp<0x00>(zz.x), dpp<0x55>(zz.x), dpp<0xAA>(zz.x), dpp<0xFF>(zz.x)};
        const f32x4 rr = {dpp<0x00>(zz.y), dpp<0x55>(zz.y), dpp<0xAA>(zz.y), dpp<0xFF>(zz.y)};
        const int so = zo + w2 * 7168 + kh * 512;
        v2u d[7];
#pragma unroll
        for (int j = 0; j < 7; j++) d[j] = pk4(z2[j] * rr + mm);
        st16(join16(d[0], d[1]), rws, zvo, so);
        st16(join16(d[2], d[3]), rws, zvo + 2048, so);
        st16(join16(d[4], d[5]), rws, zvo, so + 4096);
        st16(join16(d[6], d[6]), rws, seloob(zvo, M_GEVEN) + 2048, so + 4096);
      }
      cp_run<0, 8>(dws, l16, wz1 + 100352 + za0, L_Z1B + 100352 + za0);
      WAITV(8);
      BAR();
      flag(fl + F4 + cz * 256 + pz * 8);
      STAMP(sb + 11);
      touch(dws, seloob(tv, (1ull << 28) - 1), zo + w * 3584);
      cp_run<8, 11>(dws, l16, wz1 + 100352 + za0, L_Z1B + 100352 + za0);
      const int us = opq_s(WU_OFF + co * WU_T), ua = opq_s((w * 12800) & ~1023), ub = opq_s(51200 + w * 10240);
      f32x4 cs1[7];
      int tmp;
      xw += L_X3B - L_X3; xr += L_X3B - L_X3;
      v2u fz;
      const int fzo = opq_v(seloob(fl + F4 + cz * 256 + w * 64 + l * 8, 0xFFull));
      auto z1gemm = [&](auto l0) AI {
        constexpr bool L0 = l0.v;
        gemm_m2k2<10, 7, true, 0, false>([&](auto ti) AI { return ti.v < 3 ? b0 + ti.v * 14336 : ti.v < 7 ? b1 + (ti.v - 3) * 14336 : b2 + (ti.v - 7) * 14336; }, xw, A, acc,
            [&]() AI {}, [&](auto, auto) AI { return v4u{}; },
            [&](auto fi) AI {
          constexpr int f = fi.v;
          if constexpr (f == 22) { WAITV(L0 ? 22 : 0); BAR0(); }
          if constexpr (f == 43 || f == 57) BAR0();
          if constexpr (f >= 23 && f < 49 && (f - 23) % 8 == 0) cp_arm(ua + (f - 23) * 512);
          if constexpr (f >= 49 && f < 59 && (f - 49) % 4 == 0) cp_arm(ub + (f - 49) * 1024);
          if constexpr (f == 3) tmp = opq_v(L_CS1 + g * 16);
          if constexpr (f >= 5 && f < 19 && (f & 1)) cs1[(f - 5) / 2] = ldsf4(tmp + (f - 5) * 32);
          if constexpr (f == 63) fz = ld8<SC1>(rws, fzo, 0);
        },
            [&](auto fi) AI {
          constexpr int f = fi.v;
          if constexpr (L0 && f < 22) wsp_ld(IC<30 + f>{});
          if constexpr (f >= 24 && f < 49 && !(f & 1)) cp_one<(f - 24) / 2>(dws, l16, us + ua + (f - 24) / 8 * 4096);
          if constexpr (L0 && f >= 25 && f < 49 && (f & 1)) wsp_ld(IC<52 + (f - 25) / 2>{});
          if constexpr (f >= 49 && f < 59) cp_one<f - 49>(dws, l16, us + ub + (f - 49) / 4 * 4096);
        });
      };
      if (L == 0) z1gemm(IC<1>{}); else z1gemm(IC<0>{});
      STAMP(sb + 12);
      BAR();
      xchg_read<7>(xr, acc);
      if (!__all(l >= 8 || (fz.x == tag_lo && fz.y == tag_hi))) poll(rws, fzo, l >= 8, tag_lo, tag_hi);
      FENCE();
      STAMP(sb + 13);
      const int z0 = act + A_Z + (cz * 64 + 16 * w + (zr & 7) * 2) * 7168;
      auto ldZ0 = [&](auto fi) AI { Z[fi.v] = ld16<SC1>(rws, l16 + (fi.v & 3) * 1024, z0 + (fi.v >> 2) * 4096); };
      sfor<0, 7>([&](auto ji) AI {
        constexpr int j = ji.v;
        if constexpr (j < 4) sfor<3 * j, 3 * j + 3>(ldZ0);
        if constexpr (j == 4) sfor<12, 14>(ldZ0);
        SB();
        z1[j] = gelu4((acc[0][j] - m1 * cs1[j]) * r1);
        pin(z1[j]);
        SB();
      });
    }
    { LANE();
      const int ct0 = cz < 6 ? 7 * cz : 6 + 6 * cz, zr = MAPZ ? pz + 2 * cz + (cz >> 2) : 2 * pz + cz;
      STAMP(sb + 14);
      f32x4 acc[4][7];
      const int so = act + A_G + (4 * pz + w) * GT + ct0 * 512;
      f32x4 bias = blo(braw);
      pin(bias);
      int zp[8], za, rw[3], rd, vo; unsigned d[4];
      constexpr int RN = RZ + 1;
      v4u Y[RN];
      sfor<0, RZ>([&](auto fi) AI { Y[fi.v] = Z[fi.v]; });
      const int zb = opq_s(act + A_Z + (cz * 64 + 16 * w) * 7168);
      zp[1] = opq_s(zb + ((1 + zr) & 7) * 14336);
      NOP2();
      SB();
      sfor<0, 112>([&](auto fi) AI {
        constexpr int f = fi.v, s = sp_s(f), j = sp_j(f), fn = f + RZ;
        constexpr int q = f < 42 ? -1 : (f - 42) / 28, k = f < 42 ? 0 : (f - 42) % 28, buf = (q & 1) ? L_RED1 - L_RED0 : 0;
        auto fin = [&](auto ui) AI {
          constexpr int u = ui.v;
          if constexpr (q >= 0 && u >= 0 && u < 36) {
            constexpr int jj = 2 * q + (u < 32 ? (u >> 2) & 1 : (u - 32) >> 1), e = u & 3, st = u >> 3;
            if constexpr (st == 0) acc[0][jj][e] = gadd(acc[0][jj][e], acc[1][jj][e]);
            if constexpr (st == 1) acc[2][jj][e] = gadd(acc[2][jj][e], acc[3][jj][e]);
            if constexpr (st == 2) acc[0][jj][e] = gadd(acc[0][jj][e], acc[2][jj][e]);
            if constexpr (st == 3) acc[0][jj][e] = gmul(acc[0][jj][e], z1[jj][e]);
            if constexpr (st == 4) d[u - 32] = gcvt(acc[0][jj][(u & 1) * 2], acc[0][jj][(u & 1) * 2 + 1]);
          }
        };
        constexpr int per = q == 2 ? 7 : 4, u0 = (k - (q == 2 ? 9 : 10)) * per;
        const v4u z = Y[f % RN];
        mma<s == 0, true>(acc[1][j], z, W[1][s]);
        SB();
        if constexpr (fn < 112 && (fn >= 98 || !(fn & 1))) za = opq_s(zp[sp_s(fn) / 2] + ((sp_s(fn) & 1) * 7 + (sp_j(fn) & 6)) * 1024);
        if constexpr (q >= 0 && k >= 1 && k < 4) *lp<f32x4>(rw[k - 1] + buf) = acc[k][2 * q];
        if constexpr (q >= 0 && k >= 6 && k < 9) acc[k - 5][2 * q] = ldsf4(rd + buf + (k - 6) * 2048);
        fin(IC<u0>{});
        if constexpr (q == 2) fin(IC<u0 + 1>{});
        SB();
        mma<s == 0, true>(acc[2][j], z, W[2][s]);
        SB();
        if constexpr (fn < 112) Y[fn % RN] = ld16<SC1>(rws, l16 + (sp_j(fn) & 1) * 1024, za);
        if constexpr (q == 2) { fin(IC<u0 + 2>{}); fin(IC<u0 + 3>{}); }
        SB();
        mma<s == 0, true>(acc[3][j], z, W[3][s]);
        SB();
        if constexpr (q >= 0 && k >= 2 && k < 5) *lp<f32x4>(rw[k - 2] + buf + 1024) = acc[k - 1][2 * q + 1];
        if constexpr (q >= 0 && k >= 6 && k < 9) acc[k - 5][2 * q + 1] = ldsf4(rd + buf + (k - 6) * 2048 + 1024);
        fin(IC<u0 + per - 3>{}); fin(IC<u0 + per - 2>{});
        if constexpr (f % 4 == 1 && f / 4 + 2 < 8) zp[f / 4 + 2] = opq_s(zb + ((f / 4 + 2 + zr) & 7) * 14336);
        if constexpr (f >= 28 && f < 31) rw[f - 28] = opq_v(L_RED0 + ((((w + f - 27) & 3) * 3 + f - 28) * 2) * 1024 + l16);
        if constexpr (f == 31) rd = opq_v(L_RED0 + w * 6144 + l16);
        if constexpr (f == 32) vo = opq_v((g & 1) * 512 + c16 * 16);
        if constexpr (f == 34) vo = opq_v(vo + (g >> 1) * 256);
        if constexpr (q >= 0 && q < 2 && k == 20) { const v2u a = sw16(d[0], d[2]), e = sw16(d[1], d[3]); d[0] = a.x; d[2] = a.y; d[1] = e.x; d[3] = e.y; pin(d[0]); pin(d[1]); }
        if constexpr (q >= 0 && q < 2 && k == 21) st16(v4u{d[0], d[1], d[2], d[3]}, rws, vo + q * 1024, so);
        if constexpr (f == 38) BAR0();
        if constexpr (q >= 0 && k == 5) BAR();
        SB();
        if constexpr (s == 0) mma_c(acc[0][j], z, W[0][s], bias); else mma<false, true>(acc[0][j], z, W[0][s]);
        SB();
        fin(IC<u0 + per - 1>{});
        SB();
      });
      d[3] = gcvt(acc[0][5][2], acc[0][5][3]);
      const int rw2 = opq_v(L_RED2 + l16);
      asm volatile("s_nop 2" ::: "memory");
#pragma unroll
      for (int i = 1; i < 4; i++) pin(acc[i][6]);
      STAMP(sb + 15);
#pragma unroll
      for (int r = 1; r < 4; r++) *lp<f32x4>(rw2 + (((w + r) & 3) * 3 + r - 1) * 1024) = acc[r][6];
      st16(join16(v2u{d[0], d[1]}, v2u{d[2], d[3]}), rws, vo + 2048, so);
      BAR();
#pragma unroll
      for (int r = 1; r < 4; r++) acc[r][6] = ldsf4(rw2 + (w * 3 + r - 1) * 1024);
      pin(acc[0][6]);
      {
        const v2u dd = pk4(((acc[0][6] + acc[1][6]) + (acc[2][6] + acc[3][6])) * z1[6]);
        st16(join16(dd, dd), rws, seloob(vo, M_GEVEN) + 3072, cz < 6 ? so : OOB);
      }
      const int uc = 92160 + w * 11264;
      cp_run<0, 8>(dws, l16, WU_OFF + co * WU_T + uc, uc);
      WAITV(8);
      BAR();
      flag(fl + F5 + pz * 256 + cz * 8);
      STAMP(sb + 16);
      touch(dws, l < (cz < 6 ? 28 : 24) ? l * 128 : OOB, so);
      cp_run<8, 12>(dws, l16, WU_OFF + co * WU_T + uc, uc);
    }
    { LANE();
      f32x4 acc[2][5];
      v2u rr[5];
#pragma unroll
      for (int j = 0; j < 5; j++) rr[j] = ld8<SC1>(rws, (g >> 1) * 256 + c16 * 16 + (g & 1) * 8 + j * 512, act + A_X1 + (4 * po + w) * XT + co * 2560);
      const int vo = opq_v(L == 0 ? (g & 1) * 512 + (g >> 1) * 256 + c16 * 16 : c16 * 1280 + (g & 1) * 32 + (g >> 1) * 16);
      const int vo4 = opq_v(seloob(vo, M_GEVEN)), vs = opq_v(seloob(c16 * 8 + (g >> 1) * 4, M_GEVEN));
      const int xw = opq_v(L_X5 + (w ^ 1) * 5120 + l16), xr = opq_v(L_X5 + w * 5120 + l16);
      const int a0 = opq_s(act + A_G + (4 * po + w) * GT + kh * 14336), a1 = opq_s(act + A_G + (4 * po + (w ^ 1)) * GT + kh * 14336);
      const int k0 = opq_s(kh ? OOB : a0 + 12288), k1 = opq_s(kh ? OOB : a1 + 12288);
      const int b0 = opq_v(l16 + kh * 5120), b1 = opq_v(b0 + 61440), b2 = opq_v(b0 + 122880), b3 = opq_v(l16 + 133120);
      const int qo = opq_s(qslot_off(w)), wq = opq_s((WQ_OFF + h * WQ_T + qo) | kill1);
      const int tile = opq_s(ACT_OFF + ACT_LAY + A_XIN + (4 * po + w) * XT);
      const int pv = opq_v(seloob(fl + F5 + po * 256 + kh * 32 + l * 8, 0xFull));
      v4u A[2][14];
      gemm_m2k2<14, 5, true, 5, true>([&](auto ti) AI { return ti.v < 6 ? b0 + ti.v * 10240 : ti.v < 12 ? b1 + (ti.v - 6) * 10240 : ti.v == 12 ? b2 : b3; }, xw, A, acc,
          [&]() AI {
#pragma unroll
        for (int j = 0; j < 5; j++) { acc[0][j] = unpk4(rr[j]); pin(acc[0][j]); }
        poll(rws, pv, l >= 4, tag_lo, tag_hi);
        STAMP(sb + 17);
      },
          [&](auto ii, auto ti) AI { return ld16<SC1>(rws, l16 + (ti.v & 3) * 1024, ti.v == 13 ? (ii.v ? k1 : k0) : (ii.v ? a1 : a0) + (ti.v >> 2) * 4096); },
          [&](auto fi) AI {
        constexpr int f = fi.v;
        if constexpr (f == 21) BAR0();
        if constexpr (f == 53) { asm volatile("" ::"v"(A[0][13])); cp_arm(qo); }
        if constexpr (f == 61) cp_arm(qo + 4096);
        if constexpr (f == 66 || f == 68) cp_one<(f - 54) / 2>(dws, l16, wq + 4096);
      },
          [&](auto fi) AI {
        constexpr int f = fi.v;
        if constexpr (f >= 54 && !(f & 1)) cp_one<(f - 54) / 2>(dws, l16, wq + (f - 54) / 8 * 4096);
      });
      STAMP(sb + 18);
      BAR();
      xchg_read<5>(xr, acc);
      if (L == 1) {
        const rsrc_t ro_ = mk(out, 2048 * 1280);
        v2u d[5];
#pragma unroll
        for (int j = 0; j < 5; j++) d[j] = pk4(acc[0][j]);
        const int so = (64 * po + 16 * w) * 1280 + co * 160;
        __builtin_amdgcn_raw_buffer_store_b128(join16(d[0], d[1]), ro_, vo, so, 0);
        __builtin_amdgcn_raw_buffer_store_b128(join16(d[2], d[3]), ro_, vo + 64, so, 0);
        __builtin_amdgcn_raw_buffer_store_b128(join16(d[4], d[4]), ro_, vo4 + 128, so, 0);
        flag(b == 0 ? 0 : OOB);
        __builtin_amdgcn_endpgm();
      }
      store_x(acc[0], rws, tile + co * 2560, tile + 20480 + co * 128, vo, vo4, vs);
      cp_run<0, 8>(dws, l16, wq + 30720, qo + 30720);
      WAITV(8);
      BAR();
      flag(fl + F6 + po * 256 + co * 8);
      STAMP(sb + 19);
      touch(dws, l < 20 ? co * 2560 + l * 128 : l == 20 ? 20480 + co * 128 : OOB, tile);
      cp_run<0, 8>(dws, l16, wq + 2 * 30720, qo + 2 * 30720);
      cp_run<0, 8>(dws, l16, wq + 3 * 30720, qo + 3 * 30720);
    }
  }
}
}

extern "C" void kernel_launch(void* const* d_in, const int* in_sizes, int n_in, void* d_out, int out_size, void* d_ws, size_t ws_size, hipStream_t stream) {
  (void)hipFuncSetAttribute((const void*)fused_kernel, hipFuncAttributeMaxDynamicSharedMemorySize, LDS_BYTES);
  fused_kernel<<<256, 256, LDS_BYTES, stream>>>((const char*)d_in[0], (const char*)d_in[1], (const char*)d_in[2], (const char*)d_in[4],
                                                (const char*)d_in[5], (const char*)d_in[6], (const char*)d_in[7], (char*)d_out, (char*)d_ws);
}
```

```cpp
#define DEADLINE_ID 48
#include <hip/hip_runtime.h>
#include <hip/hip_bf16.h>
#include <stdint.h>

#define AI __attribute__((always_inline))
#define LAS __attribute__((address_space(3)))
#define GAS __attribute__((address_space(1)))
typedef unsigned v6u __attribute__((ext_vector_type(6)));
typedef unsigned v4u __attribute__((ext_vector_type(4)));
typedef unsigned v2u __attribute__((ext_vector_type(2)));
typedef int v4i __attribute__((ext_vector_type(4)));
typedef float f32x4 __attribute__((ext_vector_type(4)));
typedef float f32x2 __attribute__((ext_vector_type(2)));
typedef __bf16 bf2 __attribute__((ext_vector_type(2)));
typedef short s4 __attribute__((ext_vector_type(4)));
typedef unsigned long long u64;
typedef __amdgpu_buffer_rsrc_t rsrc_t;

#define PROF 0

namespace {
constexpr int SC1 = 16;
constexpr int F0C = 4096;
constexpr int F0B = F0C + 16 * 256;
constexpr int F0S = F0B + 32 * 256;
constexpr int FLAY = F0S + 32 * 256;
constexpr int F1 = 0, F2 = F1 + 16 * 256, F4 = F2 + 32 * 256, F5 = F4 + 8 * 256, F6 = F5 + 32 * 256, FLAY_SZ = F6 + 32 * 256;
constexpr int ST_OFF = 1 << 17;
constexpr int ST_LAY = 32 * 8192;
constexpr int CSQ_OFF = ST_OFF + 2 * ST_LAY;
constexpr int CSI_OFF = CSQ_OFF + 16 * 16 * 512;
constexpr int WQ_OFF = 1 << 20, WQ_T = 153600;
constexpr int WO_OFF = WQ_OFF + 16 * WQ_T, WO_T = 102400;
constexpr int WI_OFF = WO_OFF + 8 * WO_T, WI_T = 143360;
constexpr int WU_OFF = WI_OFF + 16 * WI_T, WU_T = 138240;
constexpr int WS_OFF = WU_OFF + 8 * WU_T;
constexpr int ACT_OFF = WS_OFF + 128 * 65536;
constexpr int XT = 21504, OT = 20480, GT = 27648;
constexpr int XSZ = 128 * XT, OSZ = 128 * OT, ZSZ = 8 * 64 * 7168, GSZ = 128 * GT;
constexpr int A_XIN = 0, A_O = A_XIN + XSZ, A_X1 = A_O + OSZ, A_Z = A_X1 + XSZ, A_G = A_Z + ZSZ, ACT_LAY = A_G + GSZ;
constexpr int PROF_OFF = ACT_OFF + 2 * ACT_LAY;
constexpr int WS_USED = PROF_OFF + 256 * 256 * 4;
static_assert(FLAY + 2 * FLAY_SZ <= ST_OFF, "");
static_assert(CSI_OFF + 16 * 16 * 512 <= WQ_OFF, "");
constexpr int OOB = 0x40000000;
constexpr int LDS_BYTES = 163840;
constexpr int L_TOP = 159744;
constexpr int L_CSQ = L_TOP, L_CS2 = L_CSQ + 512, L_CS1 = L_CS2 + 512, L_XST3 = L_CS1 + 512, L_JUNK = L_XST3 + 512, L_PROF = L_JUNK + 256;
constexpr int L_XST1 = 153600, L_HST = L_XST1 + 2048;
constexpr int L_HALO = 0, L_NB = 24576;
constexpr int L_WO = 57344, L_X2 = L_WO;
constexpr int L_Z2B = 73728 - 4 * 14336;
constexpr int L_Z1B = 59392 - 3 * 14336;
constexpr int L_X3 = 43008, L_X3B = 102400, L_X5 = 139264;
constexpr int L_RED0 = 98304, L_RED1 = L_RED0 + 24576, L_RED2 = L_RED1 + 24576;
static_assert(L_RED2 >= 138240 && L_RED2 + 12288 <= L_TOP, "");
#define MAPQ 1
#define MAPO 1
#define MAPZ 1
constexpr int SHQ = MAPQ ? 4 : 2, SHO = MAPO ? 8 : 4, SHZ = MAPZ ? 8 : 4;
constexpr unsigned GTAG = 0x5A5A5A5Bu;
constexpr unsigned ONE2 = 0x3f803f80u;

template <int I> struct IC { static constexpr int v = I; };
template <int I, int N, class F> __device__ __forceinline__ void sfor(F&& f) {
  if constexpr (I < N) { f(IC<I>{}); sfor<I + 1, N>(f); }
}
__device__ __forceinline__ int opq_s(int x) { asm volatile("" : "+s"(x)); return x; }
__device__ __forceinline__ int opq_u(int x) { return opq_s(__builtin_amdgcn_readfirstlane(x)); }
__device__ __forceinline__ int opq_v(int x) { asm volatile("" : "+v"(x)); return x; }
template <class T> __device__ __forceinline__ void pin(T& c) { asm volatile("" : "+v"(c)); }

template <bool FIRST> __device__ __forceinline__ void mma_aa(f32x4& c, v4u a, v4u b) {
  if constexpr (FIRST) asm volatile("v_mfma_f32_16x16x32_bf16 %0, %1, %2, 0" : "=&v"(c) : "a"(a), "v"(b));
  else asm volatile("v_mfma_f32_16x16x32_bf16 %0, %1, %2, %0" : "+v"(c) : "a"(a), "v"(b));
}
template <bool FIRST, bool BA> __device__ __forceinline__ void mma(f32x4& c, v4u a, v4u b) {
  if constexpr (FIRST) {
    if constexpr (BA) asm volatile("v_mfma_f32_16x16x32_bf16 %0, %1, %2, 0" : "=&v"(c) : "v"(a), "a"(b));
    else asm volatile("v_mfma_f32_16x16x32_bf16 %0, %1, %2, 0" : "=&v"(c) : "v"(a), "v"(b));
  } else {
    if constexpr (BA) asm volatile("v_mfma_f32_16x16x32_bf16 %0, %1, %2, %0" : "+v"(c) : "v"(a), "a"(b));
    else asm volatile("v_mfma_f32_16x16x32_bf16 %0, %1, %2, %0" : "+v"(c) : "v"(a), "v"(b));
  }
}
__device__ __forceinline__ void mma_c(f32x4& d, v4u a, v4u b, f32x4 c) { asm volatile("v_mfma_f32_16x16x32_bf16 %0, %1, %2, %3" : "=&v"(d) : "v"(a), "a"(b), "v"(c)); }
#define NOP2() asm volatile("s_nop 1")
__device__ __forceinline__ void mma_settle() { asm volatile("s_nop 7" ::: "memory"); }
__device__ __forceinline__ float dot2(unsigned a, unsigned b, float c) {
  return __builtin_amdgcn_fdot2_f32_bf16(__builtin_bit_cast(bf2, a), __builtin_bit_cast(bf2, b), c, false);
}
__device__ __forceinline__ unsigned pk(float lo, float hi) {
  f32x2 v = {lo, hi};
  return __builtin_bit_cast(unsigned, __builtin_convertvector(v, bf2));
}
__device__ __forceinline__ v2u pk4(f32x4 v) { return v2u{pk(v[0], v[1]), pk(v[2], v[3])}; }
__device__ __forceinline__ float blo(unsigned u) { return __builtin_bit_cast(float, u << 16); }
__device__ __forceinline__ float bhi(unsigned u) { return __builtin_bit_cast(float, u & 0xffff0000u); }
__device__ __forceinline__ f32x4 unpk4(v2u r) { return f32x4{blo(r.x), bhi(r.x), blo(r.y), bhi(r.y)}; }
__device__ __forceinline__ float asf(unsigned u) { return __builtin_bit_cast(float, u); }
__device__ __forceinline__ unsigned asu(float f) { return __builtin_bit_cast(unsigned, f); }
__device__ __forceinline__ unsigned sel(unsigned a, unsigned b, u64 m) { unsigned r; asm("v_cndmask_b32_e64 %0, %1, %2, %3" : "=v"(r) : "v"(a), "v"(b), "s"(m)); return r; }
__device__ __forceinline__ float self(float a, float b, u64 m) { return asf(sel(asu(a), asu(b), m)); }
__device__ __forceinline__ unsigned sel0(unsigned b, u64 m) { unsigned r; asm("v_cndmask_b32_e64 %0, 0, %1, %2" : "=v"(r) : "v"(b), "s"(m)); return r; }
__device__ __forceinline__ int seloob(int b, u64 m) { int r; asm("v_cndmask_b32_e64 %0, 2.0, %1, %2" : "=v"(r) : "v"(b), "s"(m)); return r; }
__device__ __forceinline__ float vmax(float a, float b) { return __builtin_elementwise_maximum(a, b); }
__device__ __forceinline__ float gadd(float a, float b) { float r; asm volatile("v_add_f32 %0, %1, %2" : "=v"(r) : "v"(a), "v"(b)); return r; }
__device__ __forceinline__ float gmul(float a, float b) { float r; asm volatile("v_mul_f32 %0, %1, %2" : "=v"(r) : "v"(a), "v"(b)); return r; }
__device__ __forceinline__ unsigned gcvt(float a, float b) { unsigned r; asm volatile("v_cvt_pk_bf16_f32 %0, %1, %2" : "=v"(r) : "v"(a), "v"(b)); return r; }
template <int C> __device__ __forceinline__ int gaddi(int a) { int r; asm volatile("v_add_u32 %0, %1, %2" : "=v"(r) : "n"(C), "v"(a)); return r; }
constexpr u64 M_ODD = 0xAAAAAAAAAAAAAAAAull, M_G0 = 0xFFFFull, M_G1 = 0xFFFF0000ull, M_GEVEN = 0x0000FFFF0000FFFFull, M_LO = 0xFFFFFFFFull;
constexpr u64 le_mask(int add) { u64 m = 0; for (int l = 0; l < 64; l++) if (4 * (l >> 4) + (l & 1) + add <= (l & 15)) m |= 1ull << l; return m; }
__device__ __forceinline__ rsrc_t mk(const void* p, int bytes) { return __builtin_amdgcn_make_buffer_rsrc((void*)p, 0, bytes, 0x00020000); }
__device__ __forceinline__ v4i mkd(const void* p, int bytes) {
  u64 a = (u64)p; v4i r; r.x = (int)(unsigned)a; r.y = (int)((unsigned)(a >> 32) & 0xffffu); r.z = bytes; r.w = 0x00020000; return r;
}
template <int AUX> __device__ __forceinline__ v4u ld16(rsrc_t r, int v, int s) { return __builtin_amdgcn_raw_buffer_load_b128(r, v, s, AUX); }
template <int AUX> __device__ __forceinline__ v2u ld8(rsrc_t r, int v, int s) { return __builtin_amdgcn_raw_buffer_load_b64(r, v, s, AUX); }
__device__ __forceinline__ void st16(v4u d, rsrc_t r, int v, int s) { __builtin_amdgcn_raw_buffer_store_b128(d, r, v, s, SC1); }
#define BAR() asm volatile("s_waitcnt lgkmcnt(0)\n\ts_barrier" ::: "memory")
#define BAR0() asm volatile("s_barrier" ::: "memory")
#define DRAIN() asm volatile("s_waitcnt vmcnt(0)" ::: "memory")
#define WAITV(n) asm volatile("s_waitcnt vmcnt(%0)" ::"n"(n) : "memory")
#define SB() __builtin_amdgcn_sched_barrier(0)
#define FENCE() do { asm volatile("" ::: "memory"); __builtin_amdgcn_sched_barrier(0); } while (0)
#define GL(o) "\n\tbuffer_load_dwordx4 %1, %2, %3 offen offset:" #o " sc1 lds"
#define GLARGS ::"s"(lds), "v"(voff), "s"(rs), "s"(soff)
__device__ __forceinline__ void glds_in(v4i rs, int voff, int soff, int lds) {
  asm volatile("s_mov_b32 m0, %0\n\ts_nop 0\n\tbuffer_load_dwordx4 %1, %2, %3 offen lds" GLARGS : "memory");
}
template <int N> __device__ __forceinline__ void gldsn(v4i rs, int voff, int soff, int lds) {
  static_assert(N >= 1 && N <= 4, "");
  if constexpr (N == 1) asm volatile("s_mov_b32 m0, %0\n\ts_nop 0" GL(0) GLARGS : "memory");
  if constexpr (N == 2) asm volatile("s_mov_b32 m0, %0\n\ts_nop 0" GL(0) GL(1024) GLARGS : "memory");
  if constexpr (N == 3) asm volatile("s_mov_b32 m0, %0\n\ts_nop 0" GL(0) GL(1024) GL(2048) GLARGS : "memory");
  if constexpr (N == 4) asm volatile("s_mov_b32 m0, %0\n\ts_nop 0" GL(0) GL(1024) GL(2048) GL(3072) GLARGS : "memory");
}
template <int N0, int N1> __device__ __forceinline__ void cp_run(v4i rs, int voff, int src, int dst) {
  if constexpr (N0 < N1) {
    constexpr int n = N1 - N0 < 4 ? N1 - N0 : 4;
    gldsn<n>(rs, voff, src + N0 * 1024, dst + N0 * 1024);
    cp_run<N0 + n, N1>(rs, voff, src, dst);
  }
}
__device__ __forceinline__ void cp_arm(int lds) { asm volatile("s_mov_b32 m0, %0" ::"s"(lds) : "memory"); }
template <int U> __device__ __forceinline__ void cp_one(v4i rs, int voff, int soff) {
  asm volatile("buffer_load_dwordx4 %0, %1, %2 offen offset:%3 sc1 lds" ::"v"(voff), "s"(rs), "s"(soff), "n"((U & 3) * 1024) : "memory");
}
__device__ __forceinline__ void touch(v4i rs, int voff, int soff) {
  asm volatile("s_mov_b32 m0, %0\n\ts_nop 0\n\tbuffer_load_dword %1, %2, %3 offen sc1 lds" ::"s"(L_JUNK), "v"(voff), "s"(rs), "s"(soff) : "memory");
}
template <int OFF, bool WS> __device__ __forceinline__ void lda16(v4u& d, v4i rs, int voff, int soff) {
  if constexpr (WS) asm volatile("buffer_load_dwordx4 %0, %1, %2, %3 offen offset:%4 sc1" : "+a"(d) : "v"(voff), "s"(rs), "s"(soff), "n"(OFF) : "memory");
  else asm volatile("buffer_load_dwordx4 %0, %1, %2, %3 offen offset:%4" : "+a"(d) : "v"(voff), "s"(rs), "s"(soff), "n"(OFF) : "memory");
}
template <int OFF> __device__ __forceinline__ void sta16(const v4u& d, v4i rs, int voff, int soff) {
  asm volatile("buffer_store_dwordx4 %0, %1, %2, %3 offen offset:%4 sc1" ::"a"(d), "v"(voff), "s"(rs), "s"(soff), "n"(OFF) : "memory");
}
template <int OFF> __device__ __forceinline__ void ldsa16(v4u& d, int addr) { asm volatile("ds_read_b128 %0, %1 offset:%2" : "+a"(d) : "v"(addr), "n"(OFF) : "memory"); }
__device__ __forceinline__ void park(v4u& d, v4u v) { asm volatile("" : "=a"(d) : "0"(v)); }
__device__ __forceinline__ void tch(v4i rs, int voff, int soff) {
  asm volatile("buffer_load_dword %0, %1, %2 offen sc1 lds" ::"v"(voff), "s"(rs), "s"(soff) : "memory");
}
__device__ __forceinline__ int qslot_off(int w) { return (w * 7680) & ~1023; }
template <class T> __device__ __forceinline__ LAS T* lp(int off) { return (LAS T*)(size_t)(unsigned)off; }
__device__ __forceinline__ v4u lds16(int off) { return *lp<v4u>(off); }
__device__ __forceinline__ v2u lds8(int off) { return *lp<v2u>(off); }
__device__ __forceinline__ f32x4 ldsf4(int off) { return *lp<f32x4>(off); }
__device__ __forceinline__ float ldsf(int off) { return *lp<float>(off); }
__device__ __forceinline__ void poll(rsrc_t rws, int voff, bool off, unsigned lo, unsigned hi) {
  for (unsigned spins = 0; spins < (1u << 18); ++spins) {
    asm volatile("" ::: "memory");
    const v2u v = ld8<SC1>(rws, voff, 0);
    if (__all(off || (v.x == lo && v.y == hi))) break;
  }
  asm volatile("" ::: "memory");
}
__device__ __forceinline__ v2u sw16(unsigned a, unsigned b) { auto r = __builtin_amdgcn_permlane16_swap(a, b, false, false); return v2u{r[0], r[1]}; }
__device__ __forceinline__ v2u sw32(unsigned a, unsigned b) { auto r = __builtin_amdgcn_permlane32_swap(a, b, false, false); return v2u{r[0], r[1]}; }
__device__ __forceinline__ v4u join16(v2u x, v2u y) { const v2u a = sw16(x.x, y.x), b = sw16(x.y, y.y); return v4u{a.x, b.x, a.y, b.y}; }
template <int CTRL> __device__ __forceinline__ float dpp(float v) {
  return __builtin_bit_cast(float, __builtin_amdgcn_update_dpp(0, __builtin_bit_cast(int, v), CTRL, 0xf, 0xf, true));
}
__device__ __forceinline__ float xsum2(float s, float q) {
  const v2u a = sw32(asu(s), asu(q)); const float t = asf(a.x) + asf(a.y);
  const v2u b = sw16(asu(t), asu(t)); return asf(b.x) + asf(b.y);
}
__device__ __forceinline__ f32x2 finish_stat(float s, float q, float inv_n) {
  const float m = s * inv_n;
  return f32x2{m, __builtin_amdgcn_rsqf(fmaxf(q * inv_n - m * m, 0.f) + 1e-5f)};
}
__device__ __forceinline__ f32x4 gelu4(f32x4 v) {
  const float k1 = -2.0f * 0.7978845608028654f * 1.4426950408889634f, k2 = k1 * 0.044715f;
  f32x4 e = v * ((v * v) * k2 + k1);
  asm("" : "+v"(e));
#pragma unroll
  for (int i = 0; i < 4; i++) e[i] = __builtin_amdgcn_exp2f(e[i]);
  e += 1.0f;
#pragma unroll
  for (int i = 0; i < 4; i++) e[i] = __builtin_amdgcn_rcpf(e[i]);
  return v * e;
}
__device__ __forceinline__ f32x2 rowsum8(f32x4 s, f32x4 q) {
  float a, b, c, d;
  asm volatile("s_nop 1\n\t"
      "v_add_f32_dpp %0, %4, %4 row_ror:8 row_mask:0xf bank_mask:0x3\n\t"
      "v_add_f32_dpp %1, %5, %5 row_ror:8 row_mask:0xf bank_mask:0x3\n\t"
      "v_add_f32_dpp %2, %8, %8 row_ror:8 row_mask:0xf bank_mask:0x3\n\t"
      "v_add_f32_dpp %3, %9, %9 row_ror:8 row_mask:0xf bank_mask:0x3\n\t"
      "v_add_f32_dpp %0, %6, %6 row_ror:8 row_mask:0xf bank_mask:0xc\n\t"
      "v_add_f32_dpp %1, %7, %7 row_ror:8 row_mask:0xf bank_mask:0xc\n\t"
      "v_add_f32_dpp %2, %10, %10 row_ror:8 row_mask:0xf bank_mask:0xc\n\t"
      "v_add_f32_dpp %3, %11, %11 row_ror:8 row_mask:0xf bank_mask:0xc\n\t"
      "v_add_f32_dpp %0, %0, %0 row_half_mirror row_mask:0xf bank_mask:0x5\n\t"
      "v_add_f32_dpp %0, %1, %1 row_half_mirror row_mask:0xf bank_mask:0xa\n\t"
      "v_add_f32_dpp %2, %2, %2 row_half_mirror row_mask:0xf bank_mask:0x5\n\t"
      "v_add_f32_dpp %2, %3, %3 row_half_mirror row_mask:0xf bank_mask:0xa\n\t"
      "v_add_f32_dpp %0, %0, %0 quad_perm:[1,0,3,2] row_mask:0xf bank_mask:0xf\n\t"
      "s_nop 0\n\t"
      "v_add_f32_dpp %2, %2, %2 quad_perm:[1,0,3,2] row_mask:0xf bank_mask:0xf\n\t"
      "v_add_f32_dpp %0, %0, %0 quad_perm:[2,3,0,1] row_mask:0xf bank_mask:0xf\n\t"
      "s_nop 0\n\t"
      "v_add_f32_dpp %2, %2, %2 quad_perm:[2,3,0,1] row_mask:0xf bank_mask:0xf"
      : "=&v"(a), "=&v"(b), "=&v"(c), "=&v"(d)
      : "v"(s[0]), "v"(s[1]), "v"(s[2]), "v"(s[3]), "v"(q[0]), "v"(q[1]), "v"(q[2]), "v"(q[3]));
  return f32x2{a, c};
}
__device__ __forceinline__ v4u bperm4(int idx, v4u v) {
  v4u r;
  r.x = __builtin_amdgcn_ds_bpermute(idx, v.x); r.y = __builtin_amdgcn_ds_bpermute(idx, v.y);
  r.z = __builtin_amdgcn_ds_bpermute(idx, v.z); r.w = __builtin_amdgcn_ds_bpermute(idx, v.w);
  return r;
}
__device__ __forceinline__ v2u trd(int off) { return __builtin_bit_cast(v2u, __builtin_amdgcn_ds_read_tr16_b64_v4i16(lp<s4>(off))); }
#define STAMP(i) do { if ((i) == DEADLINE_ID) { while (__builtin_amdgcn_s_memrealtime() - t_start < 10000ull) __builtin_amdgcn_s_sleep(2); } } while (0)
#define CSTAMP(i) do {} while (0)
#define LANE() const int l = opq_v((int)threadIdx.x) & 63, l16 = l * 16, g = l >> 4, c16 = l & 15; (void)g; (void)c16; (void)l16; \
  const int w = opq_s(w_), po = opq_s(po_), co = opq_s(co_), pz = opq_s(pz_), cz = opq_s(cz_), P = opq_s(P_), h = opq_s(h_); \
  (void)w; (void)po; (void)co; (void)pz; (void)cz; (void)P; (void)h; \
  const int kh = w & 1, w2 = w >> 1; (void)kh; (void)w2; \
  auto flag = [&](int off) AI { __builtin_amdgcn_raw_buffer_store_b64(v2u{tag_lo, tag_hi}, rws, seloob(0, 1ull), w == 0 ? off : OOB, SC1); }; (void)flag;
#define WSP() int wsb[4]; \
  _Pragma("unroll") for (int r = 0; r < 4; r++) wsb[r] = opq_s(WS_OFF + (pz * 256 + ((w + r) & 3) * 64 + 16 * w) * 1024); \
  const int zr = opq_s(MAPZ ? pz + 2 * cz + (cz >> 2) : 2 * pz + cz); \
  auto wsp_ld = [&](auto ni) AI { constexpr int n = ni.v, e = n >> 3, r = (n >> 1) & 3, s_ = n & 1; \
    lda16<s_ * 1024, true>(W[r][2 * e + s_], dws, l16, wsb[r] + ((e + zr) & 7) * 2048); }; (void)wsp_ld;

template <int NS, int NCT, bool TR, int WIN, bool INIT, class BA, class GO, class LD, class HA, class HB>
__device__ __forceinline__ void gemm_m2k2(BA&& badr, int xw, v4u (&A)[2][NS], f32x4 (&acc)[2][NCT], GO&& go, LD&& lda, HA&& ha, HB&& hb) {
  constexpr int NF = NS * NCT, NL = NF - NCT;
  static_assert(NF % 2 == 0, "");
  v4u B[NF];
  auto ldB = [&](auto fi) AI { constexpr int f = fi.v; B[f] = lds16(badr(IC<f / NCT>{}) + (f % NCT) * 1024); };
  auto mm = [&](auto ii, auto fi) AI {
    constexpr int i = ii.v, f = fi.v, t = f / NCT, ct = f % NCT;
    if (TR) mma<t == 0 && !(INIT && i == 0), false>(acc[i][ct], B[f], A[i][t]); else mma<t == 0 && !(INIT && i == 0), false>(acc[i][ct], A[i][t], B[f]);
    SB();
  };
  sfor<0, 6>([&](auto fi) AI { ldB(IC<(fi.v ^ 1)>{}); SB(); });
  go();
  if constexpr (WIN > 0) sfor<0, WIN>([&](auto ti) AI { A[1][ti.v] = lda(IC<1>{}, ti); A[0][ti.v] = lda(IC<0>{}, ti); });
  SB();
  sfor<0, NL>([&](auto fi) AI {
    constexpr int f = fi.v;
    mm(IC<0>{}, fi);
    if constexpr (((f + 6) ^ 1) < NF) ldB(IC<((f + 6) ^ 1)>{});
    ha(fi);
    SB();
    mm(IC<1>{}, fi);
    if constexpr (WIN > 0) {
      constexpr int n = 2 * WIN + f / 2;
      if constexpr (f % 2 == 0 && n < 2 * NS) A[(n & 1) ^ 1][n >> 1] = lda(IC<((n & 1) ^ 1)>{}, IC<(n >> 1)>{});
    }
    if constexpr (f == NL - 1) sfor<NL, NF>([&](auto gi) AI { if constexpr (((gi.v + 6) ^ 1) < NF) ldB(IC<((gi.v + 6) ^ 1)>{}); });
    hb(fi);
    SB();
  });
  sfor<NL, NF>([&](auto fi) AI { mm(IC<1>{}, fi); ha(fi); SB(); });
  sfor<NL, NF>([&](auto fi) AI {
    constexpr int j = fi.v - NL, D = NCT == 5 ? 3 : 1;
    mm(IC<0>{}, fi);
    if constexpr (j >= D) { pin(acc[1][j - D]); *lp<f32x4>(xw + (j - D) * 1024) = acc[1][j - D]; }
    if constexpr (j == NCT - 1) sfor<NCT - D, NCT>([&](auto ki) AI { pin(acc[1][ki.v]); *lp<f32x4>(xw + ki.v * 1024) = acc[1][ki.v]; });
    SB();
  });
}
template <int NCT> __device__ __forceinline__ void xchg_read(int src, f32x4 (&acc)[2][NCT]) {
#pragma unroll
  for (int j = 0; j < NCT; j++) acc[1][j] = ldsf4(src + j * 1024);
  SB();
#pragma unroll
  for (int j = 0; j < NCT; j++) { pin(acc[0][j]); acc[0][j] += acc[1][j]; }
}
__device__ __forceinline__ void store_x(const f32x4 (&x)[5], rsrc_t rws, int so, int ss, int vo, int vo4, int vs) {
  v2u d[5];
#pragma unroll
  for (int j = 0; j < 5; j++) d[j] = pk4(x[j]);
  float s0 = 0.f, q0 = 0.f;
#pragma unroll
  for (int j = 0; j < 5; j++) { s0 = dot2(d[j].x, ONE2, s0); q0 = dot2(d[j].x, d[j].x, q0); s0 = dot2(d[j].y, ONE2, s0); q0 = dot2(d[j].y, d[j].y, q0); }
  st16(join16(d[0], d[1]), rws, vo, so);
  st16(join16(d[2], d[3]), rws, vo + 1024, so);
  st16(join16(d[4], d[4]), rws, vo4 + 2048, so);
  __builtin_amdgcn_raw_buffer_store_b32(asu(xsum2(s0, q0)), rws, vs, ss, SC1);
}
constexpr int QPRE = 9;
constexpr int q_step(int n) { return n % 9 == 0 ? n / 9 * 4 : n / 9 * 4 + (n % 9 - 1) / 2; }
constexpr int q_cnt(int f) { int n = QPRE; for (int i = 0; i <= f; i++) if (i % 2 == 0 && n < 45 && q_step(n) <= i / 8 + 6) n++; return n; }
constexpr int q_free(int f) { int n = 0; for (int i = 0; i < f; i += 2) if (q_cnt(i) == q_cnt(i - 1)) n++; return (f % 2 == 0 && q_cnt(f) == q_cnt(f - 1)) ? n : -100; }
constexpr int l0_vm(int f) { return (f < 72 ? f % 3 == 0 : f < 104 ? f % 4 == 0 : 0) + (f >= 73 && f < 105 && f % 4 == 2) + (f == 1) + (f == 86); }
constexpr int l0_cnt(int f0, int f1) { int n = 0; for (int i = f0; i < f1; i++) n += l0_vm(i); return n; }
constexpr int sp_s(int f) { return f < 14 ? f / 7 : f < 98 ? 2 + ((f - 14) % 28) / 2 : 2 + (f - 98); }
constexpr int sp_j(int f) { return f < 14 ? f % 7 : f < 98 ? (f - 14) / 28 * 2 + (f & 1) : 6; }
#define XA(n) W[(n) / 14][2 + (n) % 14]

__global__ __launch_bounds__(256, 1) void fused_kernel(const char* __restrict__ x0, const char* __restrict__ wqkv, const char* __restrict__ wo,
                                                       const char* __restrict__ win, const char* __restrict__ wsp, const char* __restrict__ bsp,
                                                       const char* __restrict__ wout, char* out, char* ws) {
  const int w_ = __builtin_amdgcn_readfirstlane(threadIdx.x >> 6);
  const int b = blockIdx.x, xcd = b & 7, jb = b >> 3;
  const int pa = xcd * 4 + (jb & 3), ca = jb >> 2, pb = (xcd >> 1) * 8 + (jb & 7), cb = (xcd & 1) * 4 + (jb >> 3);
  const int po_ = MAPO ? pb : pa, co_ = MAPO ? cb : ca, pz_ = MAPZ ? pb : pa, cz_ = MAPZ ? cb : ca;
  const int P_ = MAPQ ? (xcd >> 1) * 4 + (jb & 3) : xcd * 2 + (jb & 1), h_ = MAPQ ? (xcd & 1) * 8 + (jb >> 2) : jb >> 1;
  const rsrc_t rws = mk(ws, WS_USED);
  const v4i dws = mkd(ws, WS_USED);
  const u64 t_start = __builtin_amdgcn_s_memrealtime();
  unsigned tag_lo, tag_hi;
  v4u W[4][16];
#pragma unroll
  for (int r = 0; r < 4; r++)
#pragma unroll
    for (int s = 0; s < 16; s++) asm volatile("" : "=a"(W[r][s]));
  {
    LANE();
    {
      const v4i dq = mkd(wqkv, 640 * 3840);
      const int vq = g * 3840 + c16 * 16 + (c16 < 5 ? 1280 : c16 < 10 ? -80 : 2400), s0 = (P * 40 + w * 12) * 3840 + 80 * h, kill = w == 3 ? OOB : 0;
      glds_in(dq, vq, s0, w * 3072);
      glds_in(dq, vq, (s0 + 4 * 3840) | kill, w * 3072 + 1024);
      glds_in(dq, vq, (s0 + 8 * 3840) | kill, w * 3072 + 2048);
    }
    {
      u64 pc; asm volatile("s_getpc_b64 %0" : "=s"(pc));
      touch(mkd((const void*)(pc & ~127ull), 51200), (((jb * 4 + w) & 7) * 64 + l) * 128, 0);
    }
    const int tau = jb, sl = xcd, stg = 32768 + w * 8192;
    const int ty = tau < 8 ? 0 : tau < 24 ? 1 : 2, ot = tau - ty * 8 - (ty >> 1) * 8, cc = ot & 7;
    const int o_kg = opq_u((ty < 2 ? 10 * sl : sl < 4 ? 14 * sl : 4 + 13 * sl) + w2), o_n = opq_u((ty < 2 ? 10 : sl < 4 ? 14 : 13) - w2);
    {
      const char* src = ty == 0 ? wo : ty == 1 ? win : wout;
      const int ldb = ty == 1 ? 3456 : 1280, lim = ty != 1 ? 9 : cc < 6 ? 13 : 11, chi = kh * 8 + (l & 7);
      const int col = ty != 1 ? ot * 160 : (ot < 8 ? 1728 : 0) + (cc < 6 ? 7 * cc : 6 + 6 * cc) * 32;
      const v4i dsrc = mkd(src, (ty == 2 ? 864 : 640) * ldb);
      const int ov = (l >> 3) * ldb + (chi < lim ? chi : lim) * 16;
      const int o0 = opq_u(o_kg * 8 * ldb + col), od = opq_u(16 * ldb), k5 = opq_u(o_n > 10 ? 0 : OOB), k6 = opq_u(o_n > 12 ? 0 : OOB);
      asm volatile("s_nop 4");
#pragma unroll
      for (int i = 0; i < 7; i++) glds_in(dsrc, ov, (o0 + i * od) | (i == 5 ? k5 : i == 6 ? k6 : 0), stg + i * 1024);
    }
    const v2u ep = ld8<SC1>(rws, 0, 0);
    FENCE();
    const rsrc_t rx = mk(x0, 2048 * 1280);
    const int xo = (128 * P + 32 * w) * 1280, ho = P > 0 ? (128 * P - 16) * 1280 : OOB;
    int vx[4];
#pragma unroll
    for (int i = 0; i < 4; i++) vx[i] = (l >> 2) * 1280 + (((l ^ (0 - g)) & 3) + ((i + w) & 3) * 4) * 16;
    v4u R[45];
    auto ldX = [&](auto ni) AI {
      constexpr int n = ni.v, t = q_step(n), i = (n % 9) & 1;
      R[n] = ld16<0>(rx, vx[t & 3] + (t >> 2) * 256, n % 9 == 0 ? ho : xo + i * 16 * 1280);
    };
    const int ct = 2 * w + (g & 1), pq = l & 3;
    const int cb_ = ct == 2 ? 32 + (pq & 1) * 8 + (pq >> 1) * 80 : ct == 7 ? 192 + (pq & 1) * 8 : (ct < 2 ? ct * 48 : ((ct - 1) >> 1) * 80 + ((ct - 1) & 1) * 48) + pq * 8;
    const int trq = opq_v(((g >> 1) * 8 + (c16 >> 2)) * 256 + cb_);
    int qv[3];
#pragma unroll
    for (int i = 0; i < 3; i++) {
      const int k = 2 * i + (g >> 1), kap = 5 * P + k;
      qv[i] = opq_v((k < 5 && (ct < 7 || c16 < 8)) ? (kap >> 2) * 7680 + ct * 1024 + ((kap & 3) * (ct == 7 ? 8 : 16) + c16) * 16 : OOB);
    }
    FENCE();
    sfor<0, 4>(ldX);
    FENCE();
    WAITV(13);
    STAMP(1);
    BAR0();
    {
      v2u lo[3], hi[3];
#pragma unroll
      for (int i = 0; i < 3; i++) { lo[i] = trd(trq + i * 4096); hi[i] = trd(trq + i * 4096 + 1024); }
      SB();
#pragma unroll
      for (int i = 0; i < 3; i++) st16(v4u{lo[i].x, lo[i].y, hi[i].x, hi[i].y}, rws, qv[i], WQ_OFF + h * WQ_T);
      float cs = 0.f;
#pragma unroll
      for (int i = 0; i < 3; i++) { cs = dot2(lo[i].x, ONE2, cs); cs = dot2(lo[i].y, ONE2, cs); cs = dot2(hi[i].x, ONE2, cs); cs = dot2(hi[i].y, ONE2, cs); }
      const v2u t = sw32(asu(cs), asu(cs));
      __builtin_amdgcn_raw_buffer_store_b32(asu(asf(t.x) + asf(t.y)), rws, seloob(l * 4, M_LO), CSQ_OFF + (h * 16 + P) * 512 + w * 128, SC1);
    }
    FENCE();
    sfor<4, 18>(ldX);
    FENCE();
    {
      const u64 epoch = ((u64)(unsigned)__builtin_amdgcn_readfirstlane(ep.y) << 32) | (unsigned)__builtin_amdgcn_readfirstlane(ep.x);
      const u64 tag = epoch * 0x9E3779B97F4A7C15ull + 0xD1B54A32D192ED03ull;
      tag_lo = opq_s((unsigned)tag); tag_hi = opq_s((unsigned)(tag >> 32));
    }
    WAITV(14);
    BAR0();
    flag(F0C + h * 256 + P * 8);
    STAMP(2);
    float xs[3] = {0.f, 0.f, 0.f}, xq[3] = {0.f, 0.f, 0.f};
    const int xwr = opq_v(16384 + w * 4096 + l16), xrd = opq_v(16384 + w * 4096 + c16 * 64 + ((g ^ (0 - (c16 >> 2))) & 3) * 16);
    auto doX = [&](auto ni) AI {
      constexpr int n = ni.v, i = n % 9 == 0 ? 2 : (n % 9) & 1;
      *lp<v4u>(xwr + (n & 3) * 1024) = R[n];
      ldsa16<(n & 3) * 1024>(XA(n), xrd);
#pragma unroll
      for (int e = 0; e < 4; e++) { xs[i] = dot2(R[n][e], ONE2, xs[i]); xq[i] = dot2(R[n][e], R[n][e], xq[i]); }
      pin(xs[i]); pin(xq[i]);
      SB();
    };
    sfor<18, 45>([&](auto ni) AI { ldX(ni); doX(IC<ni.v - 18>{}); });
    {
      const int tro = stg + (c16 >> 2) * 128 + g * 32 + (l & 3) * 8;
      const int o_v = kh * 4 + g < (ty == 1 ? 7 : 5) ? (kh * 4 + g) * 1024 + c16 * 16 : OOB;
      const int o_dst = ty == 0 ? WO_OFF + ot * WO_T : ty == 1 ? WI_OFF + ot * WI_T : WU_OFF + ot * WU_T, kb = ty < 2 ? 10 : 14, kstr = ty == 1 ? 7168 : 5120;
      float cs = 0.f;
#pragma unroll
      for (int i = 0; i < 7; i++) {
        const v2u lo = trd(tro + i * 1024), hi = trd(tro + i * 1024 + 512);
        if (i < 5) { cs = dot2(lo.x, ONE2, cs); cs = dot2(lo.y, ONE2, cs); cs = dot2(hi.x, ONE2, cs); cs = dot2(hi.y, ONE2, cs); }
        const int kap = o_kg + 2 * i, ks = kap >> 2, pos = ks < kb ? 2 * ks : 2 * (ks - kb) + 1;
        st16(v4u{lo.x, lo.y, hi.x, hi.y}, rws, o_v, opq_u(2 * i < o_n ? o_dst + pos * kstr + (kap & 3) * 256 : OOB));
      }
      __builtin_amdgcn_raw_buffer_store_b32(asu(cs), rws, l * 4, opq_u(ty == 1 ? CSI_OFF + (ot * 16 + sl * 2 + w2) * 512 + kh * 256 : OOB), SC1);
      st16(v4u{0, 0, 0, 0}, rws, seloob(g * ST_LAY + c16 * 16, M_LO), ST_OFF + po * 8192 + w * 2048 + co * 256);
    }
    FENCE();
    sfor<27, 45>(doX);
#pragma unroll
    for (int i = 0; i < 3; i++) { xs[i] += dpp<0xB1>(xs[i]); xq[i] += dpp<0xB1>(xq[i]); xs[i] += dpp<0x4E>(xs[i]); xq[i] += dpp<0x4E>(xq[i]); }
#pragma unroll
    for (int i = 0; i < 2; i++) {
      const f32x2 v = finish_stat(xs[i], xq[i], 1.f / 640.f);
      *lp<float>(L_XST1 + w * 512 + i * 64 + (l & 60)) = v.x; *lp<float>(L_XST1 + w * 512 + 256 + i * 64 + (l & 60)) = v.y;
    }
    *lp<f32x2>(L_HST + w * 128 + (l >> 2) * 8) = f32x2{xs[2], xq[2]};
    STAMP(3);
  }

#pragma unroll 1
  for (int Lr = 0;; Lr = 1) {
    const int L = opq_s(Lr), kill1 = L << 30;
    const int act = ACT_OFF + L * ACT_LAY, fl = FLAY + L * FLAY_SZ;
    const int sb = L * 24 + 5;
    f32x4 z1[7];
    unsigned braw;
    constexpr int RZ = 14;
    v4u Z[RZ];
    { LANE();
      const int qo = qslot_off(w), wq = WQ_OFF + h * WQ_T + qo;
      int vb[4], vh[4];
#pragma unroll
      for (int i = 0; i < 4; i++) { const int k = (i + w) & 3; vb[i] = opq_v(l16 + k * 7680); vh[i] = opq_v((g * 8 + (l & 7)) * 16 + k * 7680); }
      const int h2 = w < 2 ? w + 4 : w;
      const int c4 = opq_v(c16 * 4), g16 = opq_v(g * 16);
      const int wos = opq_s(WO_OFF + co * WO_T), wzs = opq_s(WI_OFF + cz * WI_T);
      f32x4 acc[2][8];
      float mt[3], rt_[3], csn[3], hcn; f32x4 mn[3], rn[3], cst[5], ha[2][4], hcs;
      auto qkv = [&](auto l0) AI {
        constexpr bool L0 = l0.v;
        v4u A[2][20], Hh[5], B[8];
        int va[4], spo, xoff, hoff, fbo, cqo, wv, wr, wk; v4i dw;
        auto ldB = [&](auto fi) AI { constexpr int f = fi.v, t = f >> 3, ct = f & 7; B[f & 7] = lds16((ct < 7 ? vb[t & 3] : vh[t & 3]) + ((t >> 2) & 1) * 30720 + ct * 1024); };
        auto ldA = [&](auto ni) AI {
          constexpr int n = ni.v, t = q_step(n), i = (n % 9) & 1;
          if constexpr (n % 9 == 0) Hh[t >> 2] = ld16<SC1>(rws, va[0], hoff + (t >> 2) * 4096);
          else A[i][t] = ld16<SC1>(rws, va[t & 3], xoff + i * XT + (t >> 2) * 4096);
        };
        if constexpr (L0) {
          dw = mkd(wsp, 2048 * 4096);
          wv = opq_v((l >> 2) * 4096 + ((l ^ (0 - g)) & 3) * 16);
          wr = opq_v(24576 + w * 8192 + c16 * 64 + ((g ^ (0 - (c16 >> 2))) & 3) * 16);
          wk = opq_s(16 * w + ((MAPZ ? pz + 2 * cz + (cz >> 2) : 2 * pz + cz) & 7) * 2);
          fbo = opq_v(seloob(F0B + ((l >> 3) * 8 + (((l >> 3) + 1) & 2 ? cz : co)) * 256 + (l & 7) * 8, M_LO));
          cqo = opq_v((l >> 5) * 4096 + (l & 31) * 4);
          poll(rws, seloob(F0C + h * 256 + c16 * 8, M_G0), l >= 16, tag_lo, tag_hi);
          BAR();
          STAMP(sb - 1);
          cp_run<0, 8>(dws, l16, wq, qo); cp_run<0, 8>(dws, l16, wq + 30720, qo + 30720);
          const f32x2 a = (*lp<f32x2>(L_HST + c16 * 8) + *lp<f32x2>(L_HST + 128 + c16 * 8)) + (*lp<f32x2>(L_HST + 256 + c16 * 8) + *lp<f32x2>(L_HST + 384 + c16 * 8));
          const f32x2 v = finish_stat(a.x, a.y, 1.f / 640.f);
          *lp<float>(L_XST1 + w * 512 + 128 + c4) = v.x; *lp<float>(L_XST1 + w * 512 + 384 + c4) = v.y;
          WAITV(8);
          BAR0();
          sfor<0, 6>([&](auto fi) AI { ldB(IC<(fi.v ^ 1)>{}); SB(); });
        } else {
#pragma unroll
          for (int i = 0; i < 4; i++) va[i] = opq_v(l16 + ((i + w) & 3) * 1024);
          spo = opq_v(g < 2 ? (2 * w + 1 + g) * XT + c16 * 8 : (g == 2 && P > 0) ? c16 * 8 : OOB);
          xoff = opq_s(act + A_XIN + (8 * P + 2 * w) * XT); hoff = opq_s(P > 0 ? act + A_XIN + (8 * P - 1) * XT : OOB);
          sfor<0, 6>([&](auto fi) AI { ldB(IC<(fi.v ^ 1)>{}); SB(); });
          const bool on = l < 8 || (l < 16 && P > 0);
          poll(rws, on ? FLAY + F6 + (l < 8 ? 2 * P + w2 : 2 * P - 1) * 256 + (l & 7) * 8 : OOB, !on, tag_lo, tag_hi);
          STAMP(sb - 1);
          sfor<0, QPRE>(ldA);
          SB();
        }
        f32x4 hac[6];
        v2u sp[8], fb; float ss[8], sq[8]; int xa, xb;
        sfor<0, 160>([&](auto fi) AI {
          constexpr int f = fi.v, t = f >> 3, ct = f & 7, hc = ct < 3 ? ct : ct - 2, xn = (t >> 2) * 9 + 1 + (t & 3) * 2;
          constexpr bool halo = (t & 3) == 0 && (ct < 3 || ct >= 5);
          constexpr int fr = L0 ? -100 : q_free(f);
          constexpr int si = f == 33 ? 0 : f == 37 ? 1 : f == 39 ? 2 : f == 62 ? 3 : f == 65 ? 4 : f == 68 ? 5 : f == 69 ? 6 : f == 71 ? 7 : -1; (void)si;
          const v4u bb = B[f & 7];
          auto mm = [&](f32x4& d, const v4u& x) AI {
            if constexpr (ct < 5) mma<t == 0, L0>(d, bb, x); else if constexpr (L0) mma_aa<t == 0>(d, x, bb); else mma<t == 0, false>(d, x, bb);
            SB();
          };
          if constexpr (L0) mm(acc[0][ct], XA(xn + 1)); else mm(acc[0][ct], A[0][t]);
          if constexpr (L0 && f == 26) WAITV(l0_cnt(0, 26));
          if constexpr (L0 && f == 58) WAITV(l0_cnt(22, 58));
          if constexpr (L0 && f == 90) WAITV(l0_cnt(46, 90));
          if constexpr (f == 26 || (L0 && (f == 58 || f == 67 || f == 90)) || (!L0 && f == 122)) BAR0();
          if constexpr (L0 && f == 122) BAR();
          if constexpr (((f + 6) ^ 1) < 160) ldB(IC<((f + 6) ^ 1)>{});
          if constexpr ((t & 7) >= 4 && t < 16 && ct == 3) vb[t & 3] = gaddi<61440>(vb[t & 3]);
          if constexpr ((t & 7) >= 4 && t < 16 && ct == 5) vh[t & 3] = gaddi<61440>(vh[t & 3]);
          if constexpr (L0 && f % 12 == 0 && f < 72) cp_arm(qo + (2 + f / 24) * 30720 + (f / 12 & 1) * 4096);
          if constexpr (L0 && f >= 72 && f < 104 && f % 4 == 0) cp_arm(24576 + w * 8192 + (f - 72) * 256);
          if constexpr (fr == 0 || fr == 4) cp_arm(qo + 4 * 30720 + fr * 1024);
          if constexpr (f == 124 || f == 140) cp_arm(L_WO + w * 8192 + (f - 124) * 256);
          if constexpr (!L0) {
            if constexpr (f >= 96 && f < 104 && !(f & 1)) ss[(f - 96) / 2] = gadd(asf(sp[(f - 96) / 2].x), asf(sp[(f - 96) / 2 + 4].x));
            if constexpr (f == 104 || f == 106) ss[(f - 104) / 2] = gadd(ss[(f - 104) / 2], ss[(f - 104) / 2 + 2]);
            if constexpr (f == 108) ss[0] = gadd(ss[0], ss[1]);
            if constexpr (f == 110) { ss[0] = ss[0] * (1.f / 640.f); pin(ss[0]); }
            if constexpr (f == 111) ss[1] = gmul(ss[0], ss[0]);
            if constexpr (f == 113) { sq[0] = sq[0] * (1.f / 640.f) - ss[1]; pin(sq[0]); }
            if constexpr (f == 114) { sq[0] = fmaxf(sq[0], 0.f); pin(sq[0]); }
            if constexpr (f == 116) { sq[0] += 1e-5f; pin(sq[0]); }
            if constexpr (f == 118) { sq[0] = __builtin_amdgcn_rsqf(sq[0]); pin(sq[0]); }
            if constexpr (f == 119) xa = opq_v(L_XST1 + w * 512 + l * 4);
            if constexpr (f == 120) *lp<float>(xa) = ss[0];
            if constexpr (f == 121) *lp<float>(xa + 256) = sq[0];
          } else {
            if constexpr (f == 118) {
              const float cs = ((ss[0] + ss[1]) + (ss[2] + ss[3])) + ((ss[4] + ss[5]) + (ss[6] + ss[7]));
              const v2u t2 = sw32(asu(cs), asu(cs));
              *lp<float>(L_CSQ + w * 128 + (l & 31) * 4) = asf(t2.x) + asf(t2.y);
            }
          }
          if constexpr (f == 126) xb = opq_v(L_XST1 + w * 512 + g16);
          if constexpr (f >= 127 && f < 130) mn[f - 127] = ldsf4(xb + (f - 127) * 64);
          if constexpr (f == 130 || f == 131 || f == 133) rn[f == 133 ? 2 : f - 130] = ldsf4(xb + 256 + (f == 133 ? 2 : f - 130) * 64);
          if constexpr (f == 134) xa = opq_v(L_HALO + w * 6144 + l16);
          if constexpr (f == 142) xb = opq_v(L_CSQ + g16);
          if constexpr (f >= 143 && f < 148) cst[f - 143] = ldsf4(xb + (f - 143) * 64);
          if constexpr (f == 148) xb = opq_v(xb + w * 64);
          if constexpr (f == 149) hcs = ldsf4(xb);
          if constexpr (f == 150) xa = opq_v(L_HALO + w * 1024 + l16);
          if constexpr (f == 151) xb = opq_v(L_HALO + h2 * 1024 + l16);
          if constexpr (f >= 152 && f < 156) ha[0][f - 152] = ldsf4(xa + (f - 152) * 6144);
          SB();
          if constexpr (L0) mm(acc[1][ct], XA(xn)); else mm(acc[1][ct], A[1][t]);
          if constexpr (L0) {
            if constexpr (f % 3 == 0 && f < 72) cp_one<f / 3>(dws, l16, wq + (2 + f / 24) * 30720 + (f / 12 & 1) * 4096);
            if constexpr (f == 1) flag(F0B + jb * 256 + xcd * 8);
            if constexpr (f >= 72 && f < 104 && f % 4 == 0) {
              constexpr int n = (f - 72) / 4;
              asm volatile("buffer_load_dwordx4 %0, %1, %2 offen lds" ::"v"(wv), "s"(dw), "s"((64 * pz + 16 * ((w + (n >> 1)) & 3)) * 4096 + (wk + (n & 1)) * 64) : "memory");
            }
            if constexpr (f >= 73 && f < 105 && f % 4 == 2) ss[(f - 74) / 4] = asf(__builtin_amdgcn_raw_buffer_load_b32(rws, cqo + (f - 74) * 128, CSQ_OFF + h * 8192 + w * 128, SC1));
            if constexpr (f == 86) fb = ld8<SC1>(rws, fbo, 0);
            if constexpr (f == 119) { if (!__all(l >= 32 || (fb.x == tag_lo && fb.y == tag_hi))) poll(rws, fbo, l >= 32, tag_lo, tag_hi); }
            if constexpr (f == 120 || f == 121) sfor<0, 4>([&](auto ki) AI { constexpr int n = (f - 120) * 4 + ki.v; ldsa16<n * 1024>(W[n >> 1][n & 1], wr); });
            if constexpr (f >= 130 && f < 160 && f % 4 == 2) {
              constexpr int n = (f - 130) / 4;
              sta16<(n & 1) * 1024>(W[n >> 1][n & 1], dws, l16, WS_OFF + ((4 * pz + ((w + (n >> 1)) & 3)) * 64 + wk) * 1024);
            }
          } else {
            if constexpr (q_cnt(f) > q_cnt(f - 1)) ldA(IC<q_cnt(f - 1)>{});
            if constexpr (fr >= 0 && fr < 8) cp_one<fr & 7>(dws, l16, wq + 4 * 30720 + (fr & 4) * 1024);
            if constexpr (si >= 0) sp[si] = ld8<SC1>(rws, spo + si * 128, act + A_XIN + (8 * P - 1) * XT + 20480);
            if constexpr (f >= 96 && f < 104 && (f & 1)) sq[(f - 96) / 2] = gadd(asf(sp[(f - 96) / 2].y), asf(sp[(f - 96) / 2 + 4].y));
            if constexpr (f == 105 || f == 107) sq[(f - 105) / 2] = gadd(sq[(f - 105) / 2], sq[(f - 105) / 2 + 2]);
            if constexpr (f == 109) sq[0] = gadd(sq[0], sq[1]);
          }
          if constexpr (f == 123) xa = opq_v(L_XST1 + w * 512 + c4);
          if constexpr (!L0 && f == 123) asm volatile("" ::"v"(A[0][19]));
          if constexpr (f >= 124 && f < 156 && f % 4 == 0) cp_one<(f - 124) / 4>(dws, l16, wos + w * 8192 + (f - 124) / 16 * 4096);
          if constexpr (f == 125 || f == 127 || f == 131) { constexpr int i = f == 131 ? 2 : (f - 125) / 2; mt[i] = ldsf(xa + i * 64); rt_[i] = ldsf(xa + 256 + i * 64); }
          if constexpr (f >= 136 && f < 142) *lp<f32x4>(xa + (f - 136) * 1024) = hac[f - 136];
          if constexpr (f == 143) xa = opq_v(L_CSQ + c4);
          if constexpr (f == 144) { csn[0] = ldsf(xa + 320); csn[1] = ldsf(xa + 384); }
          if constexpr (f == 146) { csn[2] = ldsf(xa + 448); xa = opq_v(xa + (h2 + 2) * 64); }
          if constexpr (f == 148) hcn = ldsf(xa);
          if constexpr (f == 150) BAR0();
          if constexpr (f >= 152 && f < 156) ha[1][f - 152] = ldsf4(xb + (f - 152) * 6144);
          if constexpr (halo) { SB(); if constexpr (L0) mm(hac[hc], XA(xn - 1)); else mm(hac[hc], Hh[t >> 2]); }
          SB();
        });
        mma_settle();
#pragma unroll
        for (int j = 0; j < 8; j++) { pin(acc[0][j]); pin(acc[1][j]); }
      };
      if (L == 0) qkv(IC<1>{}); else qkv(IC<0>{});
      STAMP(sb + 0);
      const int nbw = opq_v(L_NB + l * 8), w2s = wos + 32768 + w * 8192, w2d = L_WO + 32768 + w * 8192, w3s = wos + 65536 + w * 9216, w3d = L_WO + 65536 + w * 9216;
      cp_run<0, 3>(dws, l16, w2s, w2d);
      {
        const f32x4 a0 = (ha[0][0] + ha[0][1]) + (ha[0][2] + ha[0][3]), a1 = (ha[1][0] + ha[1][1]) + (ha[1][2] + ha[1][3]);
        *lp<v2u>(nbw + (w < 3 ? w * 512 : 15360)) = pk4((a0 - mt[2] * hcs) * rt_[2]);
        *lp<v2u>(nbw + (w != 2 ? h2 * 512 : 15360)) = pk4((a1 - mn[2] * hcn) * rn[2]);
      }
      v2u tk[2][8];
      auto fix = [&](int i, int ct) AI { tk[i][ct] = ct < 5 ? pk4((acc[i][ct] - mt[i] * cst[ct]) * rt_[i]) : pk4((acc[i][ct] - mn[i] * csn[ct - 5]) * rn[i]); };
      fix(1, 0); fix(1, 1); fix(1, 2); fix(1, 5); fix(1, 6); fix(1, 7);
      const int nbr = opq_v(nbw + w * 3072);
#pragma unroll
      for (int i = 0; i < 6; i++) *lp<v2u>(nbr + 3072 + i * 512) = tk[1][i < 3 ? i : i + 2];
      cp_run<3, 6>(dws, l16, w2s, w2d);
      SB();
      fix(0, 0); fix(0, 1); fix(0, 2); fix(1, 3); fix(1, 4);
      BAR();
      v2u nb[6];
#pragma unroll
      for (int i = 0; i < 6; i++) { nb[i] = lds8(nbr + i * 512); asm volatile("" ::: "memory"); }
      cp_run<6, 8>(dws, l16, w2s, w2d);
      STAMP(sb + 1);
      SB();
      fix(0, 3); fix(0, 4);
      v6u K6[3]; v4u Q[2][2];
      {
        const v2u qx = sw32(tk[0][2].x, tk[1][2].x), qy = sw32(tk[0][2].y, tk[1][2].y);
        const unsigned q1x = sw32(qx.y, qx.y).y, q1y = sw32(qy.y, qy.y).y;
        Q[0][0] = v4u{tk[0][3].x, tk[0][3].y, sel0(qx.y, M_G0), sel0(qy.y, M_G0)};
        Q[0][1] = v4u{sel0(qx.y, M_G1), sel0(qy.y, M_G1), tk[0][4].x, tk[0][4].y};
        Q[1][0] = v4u{tk[1][3].x, tk[1][3].y, sel0(q1x, M_G0), sel0(q1y, M_G0)};
        Q[1][1] = v4u{sel0(q1x, M_G1), sel0(q1y, M_G1), tk[1][4].x, tk[1][4].y};
      }
#pragma unroll
      for (int i = 0; i < 2; i++) {
        K6[i + 1] = v6u{tk[i][0].x, tk[i][0].y, tk[i][2].x, tk[i][2].y, tk[i][1].x, tk[i][1].y};
        pin(Q[i][0]); pin(Q[i][1]); pin(K6[i + 1]);
      }
      K6[0] = v6u{nb[0].x, nb[0].y, nb[2].x, nb[2].y, nb[1].x, nb[1].y};
      pin(K6[0]);
      NOP2();
      f32x4 S[4][2];
#pragma unroll
      for (int u = 3; u >= 0; u--)
#pragma unroll
        for (int k = 1; k >= 0; k--) {
          const v6u kk = K6[(u >> 1) + k];
          mma<true, false>(S[u][k], (u & 1) ? __builtin_shufflevector(kk, kk, 2, 3, 4, 5) : __builtin_shufflevector(kk, kk, 0, 1, 2, 3), Q[u >> 1][u & 1]);
        }
      SB();
      cp_run<0, 3>(dws, l16, w3s, w3d);
      unsigned sel[4];
      {
        const unsigned zz = 0x0c0c0c0cu, a = ::sel(0x0c0c0504u, 0x05040c0cu, M_ODD), d = ::sel(0x0c0c0706u, 0x07060c0cu, M_ODD);
        sel[2] = ::sel(zz, a, le_mask(0)); sel[3] = ::sel(zz, d, le_mask(2));
        sel[0] = ::sel(a, zz, le_mask(0)); sel[1] = ::sel(d, zz, le_mask(2));
      }
      const int ovo = (g & 1) * 768 + (g >> 1) * 256 + c16 * 16, ovt = seloob(g * OT + 512 + c16 * 16, M_LO);
      fix(0, 5); fix(0, 6); fix(0, 7);
      v6u V6[3];
#pragma unroll
      for (int j = 0; j < 3; j++) { V6[j] = v6u{nb[3 + j].x, nb[3 + j].y, tk[0][5 + j].x, tk[0][5 + j].y, tk[1][5 + j].x, tk[1][5 + j].y}; pin(V6[j]); }
      SB();
#pragma unroll
      for (int u = 0; u < 8; u++) pin(S[u >> 1][u & 1]);
      if (P + w == 0) { S[0][0] = f32x4{-1e30f, -1e30f, -1e30f, -1e30f}; pin(S[0][0]); S[1][0] = S[0][0]; }
      float s0[4], s1[4], mx[4];
#pragma unroll
      for (int u = 0; u < 4; u++) {
        const f32x4 a = S[u][0], bq = S[u][1];
        s0[u] = self(self(a[0], a[1], M_ODD), self(bq[0], bq[1], M_ODD), le_mask(0));
        s1[u] = self(self(a[2], a[3], M_ODD), self(bq[2], bq[3], M_ODD), le_mask(2));
        mx[u] = vmax(s0[u], s1[u]);
      }
      auto spread = [&](float t, float (&o)[4]) AI {
        const v2u a = sw16(asu(t), asu(t)), x = sw32(a.x, a.x), y = sw32(a.y, a.y);
        o[0] = asf(x.x); o[1] = asf(x.y); o[2] = asf(y.x); o[3] = asf(y.y);
      };
      const float kk = 0.22360679774997896f * 1.4426950408889634f;
      {
        const v2u a = sw32(asu(mx[0]), asu(mx[1])), bq = sw32(asu(mx[2]), asu(mx[3]));
        const v2u d = sw16(asu(vmax(asf(a.x), asf(a.y))), asu(vmax(asf(bq.x), asf(bq.y))));
        spread(vmax(asf(d.x), asf(d.y)), mx);
      }
      SB();
      cp_run<3, 6>(dws, l16, w3s, w3d);
      float sm4[4];
#pragma unroll
      for (int u = 0; u < 4; u++) {
        const f32x2 e = (f32x2{s0[u], s1[u]} - mx[u]) * kk;
        s0[u] = __builtin_amdgcn_exp2f(e.x); s1[u] = __builtin_amdgcn_exp2f(e.y);
        sm4[u] = s0[u] + s1[u];
      }
      {
        const v2u a = sw32(asu(sm4[0]), asu(sm4[1])), bq = sw32(asu(sm4[2]), asu(sm4[3]));
        const v2u d = sw16(asu(asf(a.x) + asf(a.y)), asu(asf(bq.x) + asf(bq.y)));
        spread(__builtin_amdgcn_rcpf(asf(d.x) + asf(d.y)), sm4);
      }
      v4u pf[4];
#pragma unroll
      for (int u = 0; u < 4; u++) {
        const f32x2 pn = f32x2{s0[u], s1[u]} * sm4[u];
        const unsigned pp = pk(pn.x, pn.y);
        pf[u] = v4u{__builtin_amdgcn_perm(pp, 0u, sel[0]), __builtin_amdgcn_perm(pp, 0u, sel[1]), __builtin_amdgcn_perm(pp, 0u, sel[2]), __builtin_amdgcn_perm(pp, 0u, sel[3])};
        pin(pf[u]);
      }
      NOP2();
      f32x4 Om[4], Ot[4];
#pragma unroll
      for (int u = 0; u < 4; u++) { const v6u v = V6[u & 1]; mma<true, false>(Om[u], (u >> 1) ? __builtin_shufflevector(v, v, 2, 3, 4, 5) : __builtin_shufflevector(v, v, 0, 1, 2, 3), pf[u]); }
#pragma unroll
      for (int u = 0; u < 4; u++) { const v6u v = V6[2]; mma<true, false>(Ot[u], (u >> 1) ? __builtin_shufflevector(v, v, 2, 3, 4, 5) : __builtin_shufflevector(v, v, 0, 1, 2, 3), pf[u]); }
      asm volatile("s_nop 3");
#pragma unroll
      for (int u = 0; u < 4; u++) pin(Om[u]);
      const int so = act + A_O + (8 * P + 2 * w) * OT + 5 * h * 256;
      st16(join16(pk4(Om[0]), pk4(Om[1])), rws, ovo, so);
      st16(join16(pk4(Om[2]), pk4(Om[3])), rws, ovo, so + OT);
      SB();
#pragma unroll
      for (int u = 0; u < 4; u++) pin(Ot[u]);
      {
        const v2u x0_ = pk4(Ot[0]), y0_ = pk4(Ot[1]), x1_ = pk4(Ot[2]), y1_ = pk4(Ot[3]);
        st16(v4u{sw16(x0_.x, x1_.x).x, sw16(x0_.y, x1_.y).x, sw16(y0_.x, y1_.x).y, sw16(y0_.y, y1_.y).y}, rws, ovt, so);
      }
      STAMP(sb + 2);
      cp_run<6, 9>(dws, l16, w3s, w3d);
      cp_run<0, 6>(dws, l16, wzs + w * 6144, w * 6144);
      WAITV(9);
      BAR();
      flag(fl + F1 + P * 256 + h * 8);
      flag((F0S + pz * 256 + cz * 8) | kill1);
      STAMP(sb + 3);
      touch(dws, (g < 2 && c16 < 10) ? g * OT + c16 * 128 : OOB, so);
      cp_run<0, 8>(dws, l16, wzs + 24576 + w * 8192, 24576 + w * 8192);
    }
    { LANE();
      f32x4 acc[2][5];
      v2u rr[5];
      const int tile = opq_s(act + A_X1 + (4 * po + w) * XT), wzs = opq_s(WI_OFF + cz * WI_T);
      if (L == 0) {
        const rsrc_t rx = mk(x0, 2048 * 1280);
#pragma unroll
        for (int j = 0; j < 5; j++) rr[j] = ld8<0>(rx, c16 * 1280 + g * 8 + j * 32, (64 * po + 16 * w) * 1280 + co * 160);
        float cs = 0.f;
#pragma unroll
        for (int i = 0; i < 16; i++) cs += asf(__builtin_amdgcn_raw_buffer_load_b32(rws, l * 4 + (i & 7) * 512, CSI_OFF + (w2 * 8 + cz) * 8192 + kh * 256 + (i >> 3) * 4096, SC1));
        *lp<float>(L_CS2 + (w * 64 + l) * 4) = cs;
      } else {
#pragma unroll
        for (int j = 0; j < 5; j++) rr[j] = ld8<SC1>(rws, (g >> 1) * 256 + c16 * 16 + (g & 1) * 8 + j * 512, tile + (A_XIN - A_X1) + co * 2560);
      }
      const int vo = opq_v((g & 1) * 512 + (g >> 1) * 256 + c16 * 16), vo4 = opq_v(seloob(vo, M_GEVEN)), vs = opq_v(seloob(c16 * 8 + (g >> 1) * 4, M_GEVEN));
      const int xw = opq_v(L_X2 + (w ^ 1) * 5120 + l16), xr = opq_v(L_X2 + w * 5120 + l16);
      const int a0 = opq_s(act + A_O + (4 * po + w) * OT + kh * 10240), a1 = opq_s(act + A_O + (4 * po + (w ^ 1)) * OT + kh * 10240);
      const int b0 = opq_v(L_WO + l16 + kh * 5120), b1 = opq_v(b0 + 51200);
      const bool pon = l < 8 || (l < 16 && L == 0);
      const int pv = opq_v(pon ? (l < 8 ? fl + F1 + (po >> 1) * 256 + kh * 64 : F0S + pz * 256 - 64) + l * 8 : OOB);
      v4u A[2][10];
      gemm_m2k2<10, 5, true, 5, true>([&](auto ti) AI { return ti.v < 5 ? b0 + ti.v * 10240 : b1 + (ti.v - 5) * 10240; }, xw, A, acc,
          [&]() AI {
#pragma unroll
        for (int j = 0; j < 5; j++) { acc[0][j] = unpk4(rr[j]); pin(acc[0][j]); }
        poll(rws, pv, !pon, tag_lo, tag_hi);
        STAMP(sb + 4);
      },
          [&](auto ii, auto ti) AI { return ld16<SC1>(rws, l16 + (ti.v & 3) * 1024, (ii.v ? a1 : a0) + (ti.v >> 2) * 4096); },
          [&](auto fi) AI { if constexpr (fi.v == 13) BAR0(); },
          [&](auto) AI {});
      STAMP(sb + 5);
      BAR();
      xchg_read<5>(xr, acc);
      store_x(acc[0], rws, tile + co * 2560, tile + 20480 + co * 128, vo, vo4, vs);
      cp_run<0, 8>(dws, l16, wzs + 71680 + w * 10240, L_Z2B + 71680 + w * 10240);
      WAITV(8);
      BAR();
      flag(fl + F2 + po * 256 + co * 8);
      STAMP(sb + 6);
      touch(dws, l < 20 ? co * 2560 + l * 128 : l == 20 ? 20480 + co * 128 : OOB, tile);
      cp_run<0, 4>(dws, l16, wzs + 57344 + ((w * 3584) & ~1023), L_Z2B + 57344 + ((w * 3584) & ~1023));
      cp_run<8, 10>(dws, l16, wzs + 71680 + w * 10240, L_Z2B + 71680 + w * 10240);
      cp_run<0, 8>(dws, l16, wzs + 112640 + qslot_off(w), L_Z2B + 112640 + qslot_off(w));
    }
    { LANE(); WSP();
      const int wz1 = opq_s(WI_OFF + (8 + cz) * WI_T);
      const int a0 = opq_s(act + A_X1 + (4 * pz + w) * XT + kh * 10240), a1 = opq_s(act + A_X1 + (4 * pz + (w ^ 1)) * XT + kh * 10240);
      int xw = opq_v(L_X3 + (w ^ 1) * 7168 + l16), xr = opq_v(L_X3 + w * 7168 + l16);
      const int gso = opq_v(seloob(l * 4, 0x1111111111111111ull)), gro = opq_v(((l >> 2) & 3) * 256 + (4 * g + (l & 3)) * 16);
      const int zvo = opq_v((g & 1) * 1024 + (g >> 1) * 256 + c16 * 16), spo = opq_v(g * 256 + c16 * 8);
      const int sta = opq_v(L_XST3 + w * 128 + (g & 1) * 64 + c16 * 4), stb = opq_v(L_XST3 + w * 128 + g * 16), tv = opq_v(l * 128);
      const int b0 = opq_v(l16 + kh * 7168), b1 = opq_v(b0 + L_Z1B + 3 * 14336), b2 = opq_v(b1 + 4 * 14336);
      static_assert(L_Z1B == L_Z2B, "");
      const int sps = opq_s(act + A_X1 + (4 * pz + w) * XT + 20480), za0 = opq_s((w * 10752) & ~1023);
      float cs2[7];
#pragma unroll
      for (int j = 0; j < 7; j++) { cs2[j] = ldsf(L_CS2 + c16 * 4 + j * 64); pin(cs2[j]); }
      braw = __builtin_amdgcn_raw_buffer_load_b16(mk(bsp, 4096), c16 * 2, (64 * pz + 16 * w) * 2, 0);
      v4u A[2][10]; f32x4 acc[2][7];
      v2u sp0, sp1, u_;
      float m1, r1, st_; f32x4 m4, r4;
      gemm_m2k2<10, 7, false, 5, false>([&](auto ti) AI { return ti.v < 4 ? b0 + ti.v * 14336 : ti.v < 7 ? b1 + (ti.v - 3) * 14336 : b2 + (ti.v - 7) * 14336; }, xw, A, acc,
          [&]() AI { poll(rws, seloob(fl + F2 + pz * 256 + l * 8, 0xFFull), l >= 8, tag_lo, tag_hi); pin(braw); STAMP(sb + 7); },
          [&](auto ii, auto ti) AI { return ld16<SC1>(rws, l16 + (ti.v & 3) * 1024, (ii.v ? a1 : a0) + (ti.v >> 2) * 4096); },
          [&](auto fi) AI {
        constexpr int f = fi.v;
        if constexpr (f == 15 || f == 31) BAR0();
        if constexpr (f == 46 || f == 54 || f == 62) cp_arm(za0 + (f - 46) * 512);
        if constexpr (f == 47) { u_ = sw32(asu(m1), asu(r1)); pin(u_); }
        if constexpr (f == 48) { st_ = gadd(asf(u_.x), asf(u_.y)); }
        if constexpr (f == 49) { u_ = sw16(asu(st_), asu(st_)); pin(u_); }
        if constexpr (f == 50) { st_ = gadd(asf(u_.x), asf(u_.y)); }
        if constexpr (f == 51) { u_ = sw32(asu(st_), asu(st_)); pin(u_); }
        if constexpr (f == 52) { m1 = asf(u_.x) * (1.f / 640.f); pin(m1); }
        if constexpr (f == 53) { st_ = gmul(m1, m1); }
        if constexpr (f == 55) { r1 = asf(u_.y) * (1.f / 640.f) - st_; pin(r1); }
        if constexpr (f == 56) { r1 = fmaxf(r1, 0.f); pin(r1); }
        if constexpr (f == 57) { r1 += 1e-5f; pin(r1); }
        if constexpr (f == 58) { r1 = __builtin_amdgcn_rsqf(r1); pin(r1); }
        if constexpr (f == 60) *lp<float>(sta) = self(r1, m1, M_GEVEN);
        if constexpr (f == 63) cp_one<1>(dws, l16, wz1 + za0 + 8192);
        if constexpr (f == 65) { m4 = ldsf4(stb); r4 = ldsf4(stb + 64); }
        if constexpr (f == 67) cp_one<2>(dws, l16, wz1 + za0 + 8192);
      },
          [&](auto fi) AI {
        constexpr int f = fi.v;
        if constexpr (f == 20) sp0 = ld8<SC1>(rws, spo, sps);
        if constexpr (f == 22) sp1 = ld8<SC1>(rws, spo + 128, sps);
        if constexpr (f == 44) { m1 = gadd(asf(sp0.x), asf(sp1.x)); }
        if constexpr (f == 45) { r1 = gadd(asf(sp0.y), asf(sp1.y)); }
        if constexpr (f >= 46 && !(f & 1)) cp_one<(f - 46) / 2>(dws, l16, wz1 + za0 + (f - 46) / 8 * 4096);
      });
      STAMP(sb + 8);
      BAR();
      xchg_read<7>(xr, acc);
      BAR();
      f32x4 z2[7], zs, zq;
      const int gs = wz1 + 43008 + w * 14336, gd = L_Z1B + 43008 + w * 14336;
      auto gel = [&](auto l0) AI {
        sfor<0, 7>([&](auto ji) AI {
          constexpr int j = ji.v;
          cp_run<2 * j, 2 * j + 2>(dws, l16, gs, gd);
          if constexpr (l0.v && j < 6) sfor<8 + 2 * j, 10 + 2 * j>(wsp_ld);
          SB();
          f32x4 v = gelu4((acc[0][j] - m4 * cs2[j]) * r4);
          if constexpr (j == 6) v *= cz < 6 ? 1.f : 0.f;
          z2[j] = v;
          if (j == 0) { zs = v; zq = v * v; } else { zs += v; zq += v * v; }
          pin(zs); pin(zq);
          SB();
        });
      };
      if (L == 0) gel(IC<1>{}); else gel(IC<0>{});
      const int sto = ST_OFF + L * ST_LAY + pz * 8192 + w * 2048;
      {
        const f32x2 t = rowsum8(zs, zq);
        st16(v4u{asu(t.x), GTAG, asu(t.y), GTAG}, rws, gso, sto + cz * 256);
      }
      STAMP(sb + 9);
      if (L == 0) sfor<20, 30>(wsp_ld);
      float ts, tq;
      {
        v4u ga, gb;
        for (unsigned spins = 0; spins < (1u << 18); ++spins) {
          asm volatile("" ::: "memory");
          ga = ld16<SC1>(rws, gro, sto); gb = ld16<SC1>(rws, gro + 1024, sto);
          if (__all(ga.y == GTAG && ga.w == GTAG && gb.y == GTAG && gb.w == GTAG)) break;
        }
        ts = asf(ga.x) + asf(gb.x); tq = asf(ga.z) + asf(gb.z);
        ts += dpp<0x128>(ts); tq += dpp<0x128>(tq); ts += dpp<0x124>(ts); tq += dpp<0x124>(tq);
      }
      STAMP(sb + 10);
      const int zo = act + A_Z + (cz * 64 + 2 * pz) * 7168;
      {
        f32x2 zz = finish_stat(ts, tq, 1.f / 864.f);
        zz.x = -zz.x * zz.y;
        const f32x4 mm = {dpp<0x00>(zz.x), dpp<0x55>(zz.x), dpp<0xAA>(zz.x), dpp<0xFF>(zz.x)};
        const f32x4 rr = {dpp<0x00>(zz.y), dpp<0x55>(zz.y), dpp<0xAA>(zz.y), dpp<0xFF>(zz.y)};
        const int so = zo + w2 * 7168 + kh * 512;
        v2u d[7];
#pragma unroll
        for (int j = 0; j < 7; j++) d[j] = pk4(z2[j] * rr + mm);
        st16(join16(d[0], d[1]), rws, zvo, so);
        st16(join16(d[2], d[3]), rws, zvo + 2048, so);
        st16(join16(d[4], d[5]), rws, zvo, so + 4096);
        st16(join16(d[6], d[6]), rws, seloob(zvo, M_GEVEN) + 2048, so + 4096);
      }
      cp_run<0, 8>(dws, l16, wz1 + 100352 + za0, L_Z1B + 100352 + za0);
      WAITV(8);
      BAR();
      flag(fl + F4 + cz * 256 + pz * 8);
      STAMP(sb + 11);
      touch(dws, seloob(tv, (1ull << 28) - 1), zo + w * 3584);
      cp_run<8, 11>(dws, l16, wz1 + 100352 + za0, L_Z1B + 100352 + za0);
      const int us = opq_s(WU_OFF + co * WU_T), ua = opq_s((w * 12800) & ~1023), ub = opq_s(51200 + w * 10240);
      f32x4 cs1[7];
      int tmp;
      xw += L_X3B - L_X3; xr += L_X3B - L_X3;
      v2u fz;
      const int fzo = opq_v(seloob(fl + F4 + cz * 256 + w * 64 + l * 8, 0xFFull));
      auto z1gemm = [&](auto l0) AI {
        constexpr bool L0 = l0.v;
        gemm_m2k2<10, 7, true, 0, false>([&](auto ti) AI { return ti.v < 3 ? b0 + ti.v * 14336 : ti.v < 7 ? b1 + (ti.v - 3) * 14336 : b2 + (ti.v - 7) * 14336; }, xw, A, acc,
            [&]() AI {}, [&](auto, auto) AI { return v4u{}; },
            [&](auto fi) AI {
          constexpr int f = fi.v;
          if constexpr (f == 22) { WAITV(L0 ? 22 : 0); BAR0(); }
          if constexpr (f == 43 || f == 57) BAR0();
          if constexpr (f >= 23 && f < 49 && (f - 23) % 8 == 0) cp_arm(ua + (f - 23) * 512);
          if constexpr (f >= 49 && f < 59 && (f - 49) % 4 == 0) cp_arm(ub + (f - 49) * 1024);
          if constexpr (f == 3) tmp = opq_v(L_CS1 + g * 16);
          if constexpr (f >= 5 && f < 19 && (f & 1)) cs1[(f - 5) / 2] = ldsf4(tmp + (f - 5) * 32);
          if constexpr (f == 63) fz = ld8<SC1>(rws, fzo, 0);
        },
            [&](auto fi) AI {
          constexpr int f = fi.v;
          if constexpr (L0 && f < 22) wsp_ld(IC<30 + f>{});
          if constexpr (f >= 24 && f < 49 && !(f & 1)) cp_one<(f - 24) / 2>(dws, l16, us + ua + (f - 24) / 8 * 4096);
          if constexpr (L0 && f >= 25 && f < 49 && (f & 1)) wsp_ld(IC<52 + (f - 25) / 2>{});
          if constexpr (f >= 49 && f < 59) cp_one<f - 49>(dws, l16, us + ub + (f - 49) / 4 * 4096);
        });
      };
      if (L == 0) z1gemm(IC<1>{}); else z1gemm(IC<0>{});
      STAMP(sb + 12);
      BAR();
      xchg_read<7>(xr, acc);
      if (!__all(l >= 8 || (fz.x == tag_lo && fz.y == tag_hi))) poll(rws, fzo, l >= 8, tag_lo, tag_hi);
      FENCE();
      STAMP(sb + 13);
      const int z0 = act + A_Z + (cz * 64 + 16 * w + (zr & 7) * 2) * 7168;
      auto ldZ0 = [&](auto fi) AI { Z[fi.v] = ld16<SC1>(rws, l16 + (fi.v & 3) * 1024, z0 + (fi.v >> 2) * 4096); };
      sfor<0, 7>([&](auto ji) AI {
        constexpr int j = ji.v;
        if constexpr (j < 4) sfor<3 * j, 3 * j + 3>(ldZ0);
        if constexpr (j == 4) sfor<12, 14>(ldZ0);
        SB();
        z1[j] = gelu4((acc[0][j] - m1 * cs1[j]) * r1);
        pin(z1[j]);
        SB();
      });
    }
    { LANE();
      const int ct0 = cz < 6 ? 7 * cz : 6 + 6 * cz, zr = MAPZ ? pz + 2 * cz + (cz >> 2) : 2 * pz + cz;
      STAMP(sb + 14);
      f32x4 acc[4][7];
      const int so = act + A_G + (4 * pz + w) * GT + ct0 * 512;
      f32x4 bias = blo(braw);
      pin(bias);
      int zp[8], za, rw[3], rd, vo; unsigned d[4];
      constexpr int RN = RZ + 1;
      v4u Y[RN];
      sfor<0, RZ>([&](auto fi) AI { Y[fi.v] = Z[fi.v]; });
      const int zb = opq_s(act + A_Z + (cz * 64 + 16 * w) * 7168);
      zp[1] = opq_s(zb + ((1 + zr) & 7) * 14336);
      NOP2();
      SB();
      sfor<0, 112>([&](auto fi) AI {
        constexpr int f = fi.v, s = sp_s(f), j = sp_j(f), fn = f + RZ;
        constexpr int q = f < 42 ? -1 : (f - 42) / 28, k = f < 42 ? 0 : (f - 42) % 28, buf = (q & 1) ? L_RED1 - L_RED0 : 0;
        auto fin = [&](auto ui) AI {
          constexpr int u = ui.v;
          if constexpr (q >= 0 && u >= 0 && u < 36) {
            constexpr int jj = 2 * q + (u < 32 ? (u >> 2) & 1 : (u - 32) >> 1), e = u & 3, st = u >> 3;
            if constexpr (st == 0) acc[0][jj][e] = gadd(acc[0][jj][e], acc[1][jj][e]);
            if constexpr (st == 1) acc[2][jj][e] = gadd(acc[2][jj][e], acc[3][jj][e]);
            if constexpr (st == 2) acc[0][jj][e] = gadd(acc[0][jj][e], acc[2][jj][e]);
            if constexpr (st == 3) acc[0][jj][e] = gmul(acc[0][jj][e], z1[jj][e]);
            if constexpr (st == 4) d[u - 32] = gcvt(acc[0][jj][(u & 1) * 2], acc[0][jj][(u & 1) * 2 + 1]);
          }
        };
        constexpr int per = q == 2 ? 7 : 4, u0 = (k - (q == 2 ? 9 : 10)) * per;
        const v4u z = Y[f % RN];
        mma<s == 0, true>(acc[1][j], z, W[1][s]);
        SB();
        if constexpr (fn < 112 && (fn >= 98 || !(fn & 1))) za = opq_s(zp[sp_s(fn) / 2] + ((sp_s(fn) & 1) * 7 + (sp_j(fn) & 6)) * 1024);
        if constexpr (q >= 0 && k >= 1 && k < 4) *lp<f32x4>(rw[k - 1] + buf) = acc[k][2 * q];
        if constexpr (q >= 0 && k >= 6 && k < 9) acc[k - 5][2 * q] = ldsf4(rd + buf + (k - 6) * 2048);
        fin(IC<u0>{});
        if constexpr (q == 2) fin(IC<u0 + 1>{});
        SB();
        mma<s == 0, true>(acc[2][j], z, W[2][s]);
        SB();
        if constexpr (fn < 112) Y[fn % RN] = ld16<SC1>(rws, l16 + (sp_j(fn) & 1) * 1024, za);
        if constexpr (q == 2) { fin(IC<u0 + 2>{}); fin(IC<u0 + 3>{}); }
        SB();
        mma<s == 0, true>(acc[3][j], z, W[3][s]);
        SB();
        if constexpr (q >= 0 && k >= 2 && k < 5) *lp<f32x4>(rw[k - 2] + buf + 1024) = acc[k - 1][2 * q + 1];
        if constexpr (q >= 0 && k >= 6 && k < 9) acc[k - 5][2 * q + 1] = ldsf4(rd + buf + (k - 6) * 2048 + 1024);
        fin(IC<u0 + per - 3>{}); fin(IC<u0 + per - 2>{});
        if constexpr (f % 4 == 1 && f / 4 + 2 < 8) zp[f / 4 + 2] = opq_s(zb + ((f / 4 + 2 + zr) & 7) * 14336);
        if constexpr (f >= 28 && f < 31) rw[f - 28] = opq_v(L_RED0 + ((((w + f - 27) & 3) * 3 + f - 28) * 2) * 1024 + l16);
        if constexpr (f == 31) rd = opq_v(L_RED0 + w * 6144 + l16);
        if constexpr (f == 32) vo = opq_v((g & 1) * 512 + c16 * 16);
        if constexpr (f == 34) vo = opq_v(vo + (g >> 1) * 256);
        if constexpr (q >= 0 && q < 2 && k == 20) { const v2u a = sw16(d[0], d[2]), e = sw16(d[1], d[3]); d[0] = a.x; d[2] = a.y; d[1] = e.x; d[3] = e.y; pin(d[0]); pin(d[1]); }
        if constexpr (q >= 0 && q < 2 && k == 21) st16(v4u{d[0], d[1], d[2], d[3]}, rws, vo + q * 1024, so);
        if constexpr (f == 38) BAR0();
        if constexpr (q >= 0 && k == 5) BAR();
        SB();
        if constexpr (s == 0) mma_c(acc[0][j], z, W[0][s], bias); else mma<false, true>(acc[0][j], z, W[0][s]);
        SB();
        fin(IC<u0 + per - 1>{});
        SB();
      });
      d[3] = gcvt(acc[0][5][2], acc[0][5][3]);
      const int rw2 = opq_v(L_RED2 + l16);
      asm volatile("s_nop 2" ::: "memory");
#pragma unroll
      for (int i = 1; i < 4; i++) pin(acc[i][6]);
      STAMP(sb + 15);
#pragma unroll
      for (int r = 1; r < 4; r++) *lp<f32x4>(rw2 + (((w + r) & 3) * 3 + r - 1) * 1024) = acc[r][6];
      st16(join16(v2u{d[0], d[1]}, v2u{d[2], d[3]}), rws, vo + 2048, so);
      BAR();
#pragma unroll
      for (int r = 1; r < 4; r++) acc[r][6] = ldsf4(rw2 + (w * 3 + r - 1) * 1024);
      pin(acc[0][6]);
      {
        const v2u dd = pk4(((acc[0][6] + acc[1][6]) + (acc[2][6] + acc[3][6])) * z1[6]);
        st16(join16(dd, dd), rws, seloob(vo, M_GEVEN) + 3072, cz < 6 ? so : OOB);
      }
      const int uc = 92160 + w * 11264;
      cp_run<0, 8>(dws, l16, WU_OFF + co * WU_T + uc, uc);
      WAITV(8);
      BAR();
      flag(fl + F5 + pz * 256 + cz * 8);
      STAMP(sb + 16);
      touch(dws, l < (cz < 6 ? 28 : 24) ? l * 128 : OOB, so);
      cp_run<8, 12>(dws, l16, WU_OFF + co * WU_T + uc, uc);
    }
    { LANE();
      f32x4 acc[2][5];
      v2u rr[5];
#pragma unroll
      for (int j = 0; j < 5; j++) rr[j] = ld8<SC1>(rws, (g >> 1) * 256 + c16 * 16 + (g & 1) * 8 + j * 512, act + A_X1 + (4 * po + w) * XT + co * 2560);
      const int vo = opq_v(L == 0 ? (g & 1) * 512 + (g >> 1) * 256 + c16 * 16 : c16 * 1280 + (g & 1) * 32 + (g >> 1) * 16);
      const int vo4 = opq_v(seloob(vo, M_GEVEN)), vs = opq_v(seloob(c16 * 8 + (g >> 1) * 4, M_GEVEN));
      const int xw = opq_v(L_X5 + (w ^ 1) * 5120 + l16), xr = opq_v(L_X5 + w * 5120 + l16);
      const int a0 = opq_s(act + A_G + (4 * po + w) * GT + kh * 14336), a1 = opq_s(act + A_G + (4 * po + (w ^ 1)) * GT + kh * 14336);
      const int k0 = opq_s(kh ? OOB : a0 + 12288), k1 = opq_s(kh ? OOB : a1 + 12288);
      const int b0 = opq_v(l16 + kh * 5120), b1 = opq_v(b0 + 61440), b2 = opq_v(b0 + 122880), b3 = opq_v(l16 + 133120);
      const int qo = opq_s(qslot_off(w)), wq = opq_s((WQ_OFF + h * WQ_T + qo) | kill1);
      const int tile = opq_s(ACT_OFF + ACT_LAY + A_XIN + (4 * po + w) * XT);
      const int pv = opq_v(seloob(fl + F5 + po * 256 + kh * 32 + l * 8, 0xFull));
      v4u A[2][14];
      gemm_m2k2<14, 5, true, 5, true>([&](auto ti) AI { return ti.v < 6 ? b0 + ti.v * 10240 : ti.v < 12 ? b1 + (ti.v - 6) * 10240 : ti.v == 12 ? b2 : b3; }, xw, A, acc,
          [&]() AI {
#pragma unroll
        for (int j = 0; j < 5; j++) { acc[0][j] = unpk4(rr[j]); pin(acc[0][j]); }
        poll(rws, pv, l >= 4, tag_lo, tag_hi);
        STAMP(sb + 17);
      },
          [&](auto ii, auto ti) AI { return ld16<SC1>(rws, l16 + (ti.v & 3) * 1024, ti.v == 13 ? (ii.v ? k1 : k0) : (ii.v ? a1 : a0) + (ti.v >> 2) * 4096); },
          [&](auto fi) AI {
        constexpr int f = fi.v;
        if constexpr (f == 21) BAR0();
        if constexpr (f == 53) { asm volatile("" ::"v"(A[0][13])); cp_arm(qo); }
        if constexpr (f == 61) cp_arm(qo + 4096);
        if constexpr (f == 66 || f == 68) cp_one<(f - 54) / 2>(dws, l16, wq + 4096);
      },
          [&](auto fi) AI {
        constexpr int f = fi.v;
        if constexpr (f >= 54 && !(f & 1)) cp_one<(f - 54) / 2>(dws, l16, wq + (f - 54) / 8 * 4096);
      });
      STAMP(sb + 18);
      BAR();
      xchg_read<5>(xr, acc);
      if (L == 1) {
        const rsrc_t ro_ = mk(out, 2048 * 1280);
        v2u d[5];
#pragma unroll
        for (int j = 0; j < 5; j++) d[j] = pk4(acc[0][j]);
        const int so = (64 * po + 16 * w) * 1280 + co * 160;
        __builtin_amdgcn_raw_buffer_store_b128(join16(d[0], d[1]), ro_, vo, so, 0);
        __builtin_amdgcn_raw_buffer_store_b128(join16(d[2], d[3]), ro_, vo + 64, so, 0);
        __builtin_amdgcn_raw_buffer_store_b128(join16(d[4], d[4]), ro_, vo4 + 128, so, 0);
        flag(b == 0 ? 0 : OOB);
        STAMP(48);
        __builtin_amdgcn_endpgm();
      }
      store_x(acc[0], rws, tile + co * 2560, tile + 20480 + co * 128, vo, vo4, vs);
      cp_run<0, 8>(dws, l16, wq + 30720, qo + 30720);
      WAITV(8);
      BAR();
      flag(fl + F6 + po * 256 + co * 8);
      STAMP(sb + 19);
      touch(dws, l < 20 ? co * 2560 + l * 128 : l == 20 ? 20480 + co * 128 : OOB, tile);
      cp_run<0, 8>(dws, l16, wq + 2 * 30720, qo + 2 * 30720);
      cp_run<0, 8>(dws, l16, wq + 3 * 30720, qo + 3 * 30720);
    }
  }
}
}

extern "C" void kernel_launch(void* const* d_in, const int* in_sizes, int n_in, void* d_out, int out_size, void* d_ws, size_t ws_size, hipStream_t stream) {
  (void)hipFuncSetAttribute((const void*)fused_kernel, hipFuncAttributeMaxDynamicSharedMemorySize, LDS_BYTES);
  fused_kernel<<<256, 256, LDS_BYTES, stream>>>((const char*)d_in[0], (const char*)d_in[1], (const char*)d_in[2], (const char*)d_in[4],
                                                (const char*)d_in[5], (const char*)d_in[6], (const char*)d_in[7], (char*)d_out, (char*)d_ws);
}
```

```cpp
#define DEADLINE_ID 99
#include <hip/hip_runtime.h>
#include <hip/hip_bf16.h>
#include <stdint.h>

#define AI __attribute__((always_inline))
#define LAS __attribute__((address_space(3)))
#define GAS __attribute__((address_space(1)))
typedef unsigned v6u __attribute__((ext_vector_type(6)));
typedef unsigned v4u __attribute__((ext_vector_type(4)));
typedef unsigned v2u __attribute__((ext_vector_type(2)));
typedef int v4i __attribute__((ext_vector_type(4)));
typedef float f32x4 __attribute__((ext_vector_type(4)));
typedef float f32x2 __attribute__((ext_vector_type(2)));
typedef __bf16 bf2 __attribute__((ext_vector_type(2)));
typedef short s4 __attribute__((ext_vector_type(4)));
typedef unsigned long long u64;
typedef __amdgpu_buffer_rsrc_t rsrc_t;

#define PROF 0

namespace {
constexpr int SC1 = 16;
constexpr int F0C = 4096;
constexpr int F0B = F0C + 16 * 256;
constexpr int F0S = F0B + 32 * 256;
constexpr int FLAY = F0S + 32 * 256;
constexpr int F1 = 0, F2 = F1 + 16 * 256, F4 = F2 + 32 * 256, F5 = F4 + 8 * 256, F6 = F5 + 32 * 256, FLAY_SZ = F6 + 32 * 256;
constexpr int ST_OFF = 1 << 17;
constexpr int ST_LAY = 32 * 8192;
constexpr int CSQ_OFF = ST_OFF + 2 * ST_LAY;
constexpr int CSI_OFF = CSQ_OFF + 16 * 16 * 512;
constexpr int WQ_OFF = 1 << 20, WQ_T = 153600;
constexpr int WO_OFF = WQ_OFF + 16 * WQ_T, WO_T = 102400;
constexpr int WI_OFF = WO_OFF + 8 * WO_T, WI_T = 143360;
constexpr int WU_OFF = WI_OFF + 16 * WI_T, WU_T = 138240;
constexpr int WS_OFF = WU_OFF + 8 * WU_T;
constexpr int ACT_OFF = WS_OFF + 128 * 65536;
constexpr int XT = 21504, OT = 20480, GT = 27648;
constexpr int XSZ = 128 * XT, OSZ = 128 * OT, ZSZ = 8 * 64 * 7168, GSZ = 128 * GT;
constexpr int A_XIN = 0, A_O = A_XIN + XSZ, A_X1 = A_O + OSZ, A_Z = A_X1 + XSZ, A_G = A_Z + ZSZ, ACT_LAY = A_G + GSZ;
constexpr int PROF_OFF = ACT_OFF + 2 * ACT_LAY;
constexpr int WS_USED = PROF_OFF + 256 * 256 * 4;
static_assert(FLAY + 2 * FLAY_SZ <= ST_OFF, "");
static_assert(CSI_OFF + 16 * 16 * 512 <= WQ_OFF, "");
constexpr int OOB = 0x40000000;
constexpr int LDS_BYTES = 163840;
constexpr int L_TOP = 159744;
constexpr int L_CSQ = L_TOP, L_CS2 = L_CSQ + 512, L_CS1 = L_CS2 + 512, L_XST3 = L_CS1 + 512, L_JUNK = L_XST3 + 512, L_PROF = L_JUNK + 256;
constexpr int L_XST1 = 153600, L_HST = L_XST1 + 2048;
constexpr int L_HALO = 0, L_NB = 24576;
constexpr int L_WO = 57344, L_X2 = L_WO;
constexpr int L_Z2B = 73728 - 4 * 14336;
constexpr int L_Z1B = 59392 - 3 * 14336;
constexpr int L_X3 = 43008, L_X3B = 102400, L_X5 = 139264;
constexpr int L_RED0 = 98304, L_RED1 = L_RED0 + 24576, L_RED2 = L_RED1 + 24576;
static_assert(L_RED2 >= 138240 && L_RED2 + 12288 <= L_TOP, "");
#define MAPQ 1
#define MAPO 1
#define MAPZ 1
constexpr int SHQ = MAPQ ? 4 : 2, SHO = MAPO ? 8 : 4, SHZ = MAPZ ? 8 : 4;
constexpr unsigned GTAG = 0x5A5A5A5Bu;
constexpr unsigned ONE2 = 0x3f803f80u;

template <int I> struct IC { static constexpr int v = I; };
template <int I, int N, class F> __device__ __forceinline__ void sfor(F&& f) {
  if constexpr (I < N) { f(IC<I>{}); sfor<I + 1, N>(f); }
}
__device__ __forceinline__ int opq_s(int x) { asm volatile("" : "+s"(x)); return x; }
__device__ __forceinline__ int opq_u(int x) { return opq_s(__builtin_amdgcn_readfirstlane(x)); }
__device__ __forceinline__ int opq_v(int x) { asm volatile("" : "+v"(x)); return x; }
template <class T> __device__ __forceinline__ void pin(T& c) { asm volatile("" : "+v"(c)); }

template <bool FIRST> __device__ __forceinline__ void mma_aa(f32x4& c, v4u a, v4u b) {
  if constexpr (FIRST) asm volatile("v_mfma_f32_16x16x32_bf16 %0, %1, %2, 0" : "=&v"(c) : "a"(a), "v"(b));
  else asm volatile("v_mfma_f32_16x16x32_bf16 %0, %1, %2, %0" : "+v"(c) : "a"(a), "v"(b));
}
template <bool FIRST, bool BA> __device__ __forceinline__ void mma(f32x4& c, v4u a, v4u b) {
  if constexpr (FIRST) {
    if constexpr (BA) asm volatile("v_mfma_f32_16x16x32_bf16 %0, %1, %2, 0" : "=&v"(c) : "v"(a), "a"(b));
    else asm volatile("v_mfma_f32_16x16x32_bf16 %0, %1, %2, 0" : "=&v"(c) : "v"(a), "v"(b));
  } else {
    if constexpr (BA) asm volatile("v_mfma_f32_16x16x32_bf16 %0, %1, %2, %0" : "+v"(c) : "v"(a), "a"(b));
    else asm volatile("v_mfma_f32_16x16x32_bf16 %0, %1, %2, %0" : "+v"(c) : "v"(a), "v"(b));
  }
}
__device__ __forceinline__ void mma_c(f32x4& d, v4u a, v4u b, f32x4 c) { asm volatile("v_mfma_f32_16x16x32_bf16 %0, %1, %2, %3" : "=&v"(d) : "v"(a), "a"(b), "v"(c)); }
#define NOP2() asm volatile("s_nop 1")
__device__ __forceinline__ void mma_settle() { asm volatile("s_nop 7" ::: "memory"); }
__device__ __forceinline__ float dot2(unsigned a, unsigned b, float c) {
  return __builtin_amdgcn_fdot2_f32_bf16(__builtin_bit_cast(bf2, a), __builtin_bit_cast(bf2, b), c, false);
}
__device__ __forceinline__ unsigned pk(float lo, float hi) {
  f32x2 v = {lo, hi};
  return __builtin_bit_cast(unsigned, __builtin_convertvector(v, bf2));
}
__device__ __forceinline__ v2u pk4(f32x4 v) { return v2u{pk(v[0], v[1]), pk(v[2], v[3])}; }
__device__ __forceinline__ float blo(unsigned u) { return __builtin_bit_cast(float, u << 16); }
__device__ __forceinline__ float bhi(unsigned u) { return __builtin_bit_cast(float, u & 0xffff0000u); }
__device__ __forceinline__ f32x4 unpk4(v2u r) { return f32x4{blo(r.x), bhi(r.x), blo(r.y), bhi(r.y)}; }
__device__ __forceinline__ float asf(unsigned u) { return __builtin_bit_cast(float, u); }
__device__ __forceinline__ unsigned asu(float f) { return __builtin_bit_cast(unsigned, f); }
__device__ __forceinline__ unsigned sel(unsigned a, unsigned b, u64 m) { unsigned r; asm("v_cndmask_b32_e64 %0, %1, %2, %3" : "=v"(r) : "v"(a), "v"(b), "s"(m)); return r; }
__device__ __forceinline__ float self(float a, float b, u64 m) { return asf(sel(asu(a), asu(b), m)); }
__device__ __forceinline__ unsigned sel0(unsigned b, u64 m) { unsigned r; asm("v_cndmask_b32_e64 %0, 0, %1, %2" : "=v"(r) : "v"(b), "s"(m)); return r; }
__device__ __forceinline__ int seloob(int b, u64 m) { int r; asm("v_cndmask_b32_e64 %0, 2.0, %1, %2" : "=v"(r) : "v"(b), "s"(m)); return r; }
__device__ __forceinline__ float vmax(float a, float b) { return __builtin_elementwise_maximum(a, b); }
__device__ __forceinline__ float gadd(float a, float b) { float r; asm volatile("v_add_f32 %0, %1, %2" : "=v"(r) : "v"(a), "v"(b)); return r; }
__device__ __forceinline__ float gmul(float a, float b) { float r; asm volatile("v_mul_f32 %0, %1, %2" : "=v"(r) : "v"(a), "v"(b)); return r; }
__device__ __forceinline__ unsigned gcvt(float a, float b) { unsigned r; asm volatile("v_cvt_pk_bf16_f32 %0, %1, %2" : "=v"(r) : "v"(a), "v"(b)); return r; }
template <int C> __device__ __forceinline__ int gaddi(int a) { int r; asm volatile("v_add_u32 %0, %1, %2" : "=v"(r) : "n"(C), "v"(a)); return r; }
constexpr u64 M_ODD = 0xAAAAAAAAAAAAAAAAull, M_G0 = 0xFFFFull, M_G1 = 0xFFFF0000ull, M_GEVEN = 0x0000FFFF0000FFFFull, M_LO = 0xFFFFFFFFull;
constexpr u64 le_mask(int add) { u64 m = 0; for (int l = 0; l < 64; l++) if (4 * (l >> 4) + (l & 1) + add <= (l & 15)) m |= 1ull << l; return m; }
__device__ __forceinline__ rsrc_t mk(const void* p, int bytes) { return __builtin_amdgcn_make_buffer_rsrc((void*)p, 0, bytes, 0x00020000); }
__device__ __forceinline__ v4i mkd(const void* p, int bytes) {
  u64 a = (u64)p; v4i r; r.x = (int)(unsigned)a; r.y = (int)((unsigned)(a >> 32) & 0xffffu); r.z = bytes; r.w = 0x00020000; return r;
}
template <int AUX> __device__ __forceinline__ v4u ld16(rsrc_t r, int v, int s) { return __builtin_amdgcn_raw_buffer_load_b128(r, v, s, AUX); }
template <int AUX> __device__ __forceinline__ v2u ld8(rsrc_t r, int v, int s) { return __builtin_amdgcn_raw_buffer_load_b64(r, v, s, AUX); }
__device__ __forceinline__ void st16(v4u d, rsrc_t r, int v, int s) { __builtin_amdgcn_raw_buffer_store_b128(d, r, v, s, SC1); }
#define BAR() asm volatile("s_waitcnt lgkmcnt(0)\n\ts_barrier" ::: "memory")
#define BAR0() asm volatile("s_barrier" ::: "memory")
#define DRAIN() asm volatile("s_waitcnt vmcnt(0)" ::: "memory")
#define WAITV(n) asm volatile("s_waitcnt vmcnt(%0)" ::"n"(n) : "memory")
#define SB() __builtin_amdgcn_sched_barrier(0)
#define FENCE() do { asm volatile("" ::: "memory"); __builtin_amdgcn_sched_barrier(0); } while (0)
#define GL(o) "\n\tbuffer_load_dwordx4 %1, %2, %3 offen offset:" #o " sc1 lds"
#define GLARGS ::"s"(lds), "v"(voff), "s"(rs), "s"(soff)
__device__ __forceinline__ void glds_in(v4i rs, int voff, int soff, int lds) {
  asm volatile("s_mov_b32 m0, %0\n\ts_nop 0\n\tbuffer_load_dwordx4 %1, %2, %3 offen lds" GLARGS : "memory");
}
template <int N> __device__ __forceinline__ void gldsn(v4i rs, int voff, int soff, int lds) {
  static_assert(N >= 1 && N <= 4, "");
  if constexpr (N == 1) asm volatile("s_mov_b32 m0, %0\n\ts_nop 0" GL(0) GLARGS : "memory");
  if constexpr (N == 2) asm volatile("s_mov_b32 m0, %0\n\ts_nop 0" GL(0) GL(1024) GLARGS : "memory");
  if constexpr (N == 3) asm volatile("s_mov_b32 m0, %0\n\ts_nop 0" GL(0) GL(1024) GL(2048) GLARGS : "memory");
  if constexpr (N == 4) asm volatile("s_mov_b32 m0, %0\n\ts_nop 0" GL(0) GL(1024) GL(2048) GL(3072) GLARGS : "memory");
}
template <int N0, int N1> __device__ __forceinline__ void cp_run(v4i rs, int voff, int src, int dst) {
  if constexpr (N0 < N1) {
    constexpr int n = N1 - N0 < 4 ? N1 - N0 : 4;
    gldsn<n>(rs, voff, src + N0 * 1024, dst + N0 * 1024);
    cp_run<N0 + n, N1>(rs, voff, src, dst);
  }
}
__device__ __forceinline__ void cp_arm(int lds) { asm volatile("s_mov_b32 m0, %0" ::"s"(lds) : "memory"); }
template <int U> __device__ __forceinline__ void cp_one(v4i rs, int voff, int soff) {
  asm volatile("buffer_load_dwordx4 %0, %1, %2 offen offset:%3 sc1 lds" ::"v"(voff), "s"(rs), "s"(soff), "n"((U & 3) * 1024) : "memory");
}
__device__ __forceinline__ void touch(v4i rs, int voff, int soff) {
  asm volatile("s_mov_b32 m0, %0\n\ts_nop 0\n\tbuffer_load_dword %1, %2, %3 offen sc1 lds" ::"s"(L_JUNK), "v"(voff), "s"(rs), "s"(soff) : "memory");
}
template <int OFF, bool WS> __device__ __forceinline__ void lda16(v4u& d, v4i rs, int voff, int soff) {
  if constexpr (WS) asm volatile("buffer_load_dwordx4 %0, %1, %2, %3 offen offset:%4 sc1" : "+a"(d) : "v"(voff), "s"(rs), "s"(soff), "n"(OFF) : "memory");
  else asm volatile("buffer_load_dwordx4 %0, %1, %2, %3 offen offset:%4" : "+a"(d) : "v"(voff), "s"(rs), "s"(soff), "n"(OFF) : "memory");
}
template <int OFF> __device__ __forceinline__ void sta16(const v4u& d, v4i rs, int voff, int soff) {
  asm volatile("buffer_store_dwordx4 %0, %1, %2, %3 offen offset:%4 sc1" ::"a"(d), "v"(voff), "s"(rs), "s"(soff), "n"(OFF) : "memory");
}
template <int OFF> __device__ __forceinline__ void ldsa16(v4u& d, int addr) { asm volatile("ds_read_b128 %0, %1 offset:%2" : "+a"(d) : "v"(addr), "n"(OFF) : "memory"); }
__device__ __forceinline__ void park(v4u& d, v4u v) { asm volatile("" : "=a"(d) : "0"(v)); }
__device__ __forceinline__ void tch(v4i rs, int voff, int soff) {
  asm volatile("buffer_load_dword %0, %1, %2 offen sc1 lds" ::"v"(voff), "s"(rs), "s"(soff) : "memory");
}
__device__ __forceinline__ int qslot_off(int w) { return (w * 7680) & ~1023; }
template <class T> __device__ __forceinline__ LAS T* lp(int off) { return (LAS T*)(size_t)(unsigned)off; }
__device__ __forceinline__ v4u lds16(int off) { return *lp<v4u>(off); }
__device__ __forceinline__ v2u lds8(int off) { return *lp<v2u>(off); }
__device__ __forceinline__ f32x4 ldsf4(int off) { return *lp<f32x4>(off); }
__device__ __forceinline__ float ldsf(int off) { return *lp<float>(off); }
__device__ __forceinline__ void poll(rsrc_t rws, int voff, bool off, unsigned lo, unsigned hi) {
  for (unsigned spins = 0; spins < (1u << 18); ++spins) {
    asm volatile("" ::: "memory");
    const v2u v = ld8<SC1>(rws, voff, 0);
    if (__all(off || (v.x == lo && v.y == hi))) break;
  }
  asm volatile("" ::: "memory");
}
__device__ __forceinline__ v2u sw16(unsigned a, unsigned b) { auto r = __builtin_amdgcn_permlane16_swap(a, b, false, false); return v2u{r[0], r[1]}; }
__device__ __forceinline__ v2u sw32(unsigned a, unsigned b) { auto r = __builtin_amdgcn_permlane32_swap(a, b, false, false); return v2u{r[0], r[1]}; }
__device__ __forceinline__ v4u join16(v2u x, v2u y) { const v2u a = sw16(x.x, y.x), b = sw16(x.y, y.y); return v4u{a.x, b.x, a.y, b.y}; }
template <int CTRL> __device__ __forceinline__ float dpp(float v) {
  return __builtin_bit_cast(float, __builtin_amdgcn_update_dpp(0, __builtin_bit_cast(int, v), CTRL, 0xf, 0xf, true));
}
__device__ __forceinline__ float xsum2(float s, float q) {
  const v2u a = sw32(asu(s), asu(q)); const float t = asf(a.x) + asf(a.y);
  const v2u b = sw16(asu(t), asu(t)); return asf(b.x) + asf(b.y);
}
__device__ __forceinline__ f32x2 finish_stat(float s, float q, float inv_n) {
  const float m = s * inv_n;
  return f32x2{m, __builtin_amdgcn_rsqf(fmaxf(q * inv_n - m * m, 0.f) + 1e-5f)};
}
__device__ __forceinline__ f32x4 gelu4(f32x4 v) {
  const float k1 = -2.0f * 0.7978845608028654f * 1.4426950408889634f, k2 = k1 * 0.044715f;
  f32x4 e = v * ((v * v) * k2 + k1);
  asm("" : "+v"(e));
#pragma unroll
  for (int i = 0; i < 4; i++) e[i] = __builtin_amdgcn_exp2f(e[i]);
  e += 1.0f;
#pragma unroll
  for (int i = 0; i < 4; i++) e[i] = __builtin_amdgcn_rcpf(e[i]);
  return v * e;
}
__device__ __forceinline__ f32x2 rowsum8(f32x4 s, f32x4 q) {
  float a, b, c, d;
  asm volatile("s_nop 1\n\t"
      "v_add_f32_dpp %0, %4, %4 row_ror:8 row_mask:0xf bank_mask:0x3\n\t"
      "v_add_f32_dpp %1, %5, %5 row_ror:8 row_mask:0xf bank_mask:0x3\n\t"
      "v_add_f32_dpp %2, %8, %8 row_ror:8 row_mask:0xf bank_mask:0x3\n\t"
      "v_add_f32_dpp %3, %9, %9 row_ror:8 row_mask:0xf bank_mask:0x3\n\t"
      "v_add_f32_dpp %0, %6, %6 row_ror:8 row_mask:0xf bank_mask:0xc\n\t"
      "v_add_f32_dpp %1, %7, %7 row_ror:8 row_mask:0xf bank_mask:0xc\n\t"
      "v_add_f32_dpp %2, %10, %10 row_ror:8 row_mask:0xf bank_mask:0xc\n\t"
      "v_add_f32_dpp %3, %11, %11 row_ror:8 row_mask:0xf bank_mask:0xc\n\t"
      "v_add_f32_dpp %0, %0, %0 row_half_mirror row_mask:0xf bank_mask:0x5\n\t"
      "v_add_f32_dpp %0, %1, %1 row_half_mirror row_mask:0xf bank_mask:0xa\n\t"
      "v_add_f32_dpp %2, %2, %2 row_half_mirror row_mask:0xf bank_mask:0x5\n\t"
      "v_add_f32_dpp %2, %3, %3 row_half_mirror row_mask:0xf bank_mask:0xa\n\t"
      "v_add_f32_dpp %0, %0, %0 quad_perm:[1,0,3,2] row_mask:0xf bank_mask:0xf\n\t"
      "s_nop 0\n\t"
      "v_add_f32_dpp %2, %2, %2 quad_perm:[1,0,3,2] row_mask:0xf bank_mask:0xf\n\t"
      "v_add_f32_dpp %0, %0, %0 quad_perm:[2,3,0,1] row_mask:0xf bank_mask:0xf\n\t"
      "s_nop 0\n\t"
      "v_add_f32_dpp %2, %2, %2 quad_perm:[2,3,0,1] row_mask:0xf bank_mask:0xf"
      : "=&v"(a), "=&v"(b), "=&v"(c), "=&v"(d)
      : "v"(s[0]), "v"(s[1]), "v"(s[2]), "v"(s[3]), "v"(q[0]), "v"(q[1]), "v"(q[2]), "v"(q[3]));
  return f32x2{a, c};
}
__device__ __forceinline__ v4u bperm4(int idx, v4u v) {
  v4u r;
  r.x = __builtin_amdgcn_ds_bpermute(idx, v.x); r.y = __builtin_amdgcn_ds_bpermute(idx, v.y);
  r.z = __builtin_amdgcn_ds_bpermute(idx, v.z); r.w = __builtin_amdgcn_ds_bpermute(idx, v.w);
  return r;
}
__device__ __forceinline__ v2u trd(int off) { return __builtin_bit_cast(v2u, __builtin_amdgcn_ds_read_tr16_b64_v4i16(lp<s4>(off))); }
#define STAMP(i) do { if ((i) == DEADLINE_ID) { while (__builtin_amdgcn_s_memrealtime() - t_start < 10000ull) __builtin_amdgcn_s_sleep(2); } } while (0)
#define CSTAMP(i) do {} while (0)
#define LANE() const int l = opq_v((int)threadIdx.x) & 63, l16 = l * 16, g = l >> 4, c16 = l & 15; (void)g; (void)c16; (void)l16; \
  const int w = opq_s(w_), po = opq_s(po_), co = opq_s(co_), pz = opq_s(pz_), cz = opq_s(cz_), P = opq_s(P_), h = opq_s(h_); \
  (void)w; (void)po; (void)co; (void)pz; (void)cz; (void)P; (void)h; \
  const int kh = w & 1, w2 = w >> 1; (void)kh; (void)w2; \
  auto flag = [&](int off) AI { __builtin_amdgcn_raw_buffer_store_b64(v2u{tag_lo, tag_hi}, rws, seloob(0, 1ull), w == 0 ? off : OOB, SC1); }; (void)flag;
#define WSP() int wsb[4]; \
  _Pragma("unroll") for (int r = 0; r < 4; r++) wsb[r] = opq_s(WS_OFF + (pz * 256 + ((w + r) & 3) * 64 + 16 * w) * 1024); \
  const int zr = opq_s(MAPZ ? pz + 2 * cz + (cz >> 2) : 2 * pz + cz); \
  auto wsp_ld = [&](auto ni) AI { constexpr int n = ni.v, e = n >> 3, r = (n >> 1) & 3, s_ = n & 1; \
    lda16<s_ * 1024, true>(W[r][2 * e + s_], dws, l16, wsb[r] + ((e + zr) & 7) * 2048); }; (void)wsp_ld;

template <int NS, int NCT, bool TR, int WIN, bool INIT, class BA, class GO, class LD, class HA, class HB>
__device__ __forceinline__ void gemm_m2k2(BA&& badr, int xw, v4u (&A)[2][NS], f32x4 (&acc)[2][NCT], GO&& go, LD&& lda, HA&& ha, HB&& hb) {
  constexpr int NF = NS * NCT, NL = NF - NCT;
  static_assert(NF % 2 == 0, "");
  v4u B[NF];
  auto ldB = [&](auto fi) AI { constexpr int f = fi.v; B[f] = lds16(badr(IC<f / NCT>{}) + (f % NCT) * 1024); };
  auto mm = [&](auto ii, auto fi) AI {
    constexpr int i = ii.v, f = fi.v, t = f / NCT, ct = f % NCT;
    if (TR) mma<t == 0 && !(INIT && i == 0), false>(acc[i][ct], B[f], A[i][t]); else mma<t == 0 && !(INIT && i == 0), false>(acc[i][ct], A[i][t], B[f]);
    SB();
  };
  sfor<0, 6>([&](auto fi) AI { ldB(IC<(fi.v ^ 1)>{}); SB(); });
  go();
  if constexpr (WIN > 0) sfor<0, WIN>([&](auto ti) AI { A[1][ti.v] = lda(IC<1>{}, ti); A[0][ti.v] = lda(IC<0>{}, ti); });
  SB();
  sfor<0, NL>([&](auto fi) AI {
    constexpr int f = fi.v;
    mm(IC<0>{}, fi);
    if constexpr (((f + 6) ^ 1) < NF) ldB(IC<((f + 6) ^ 1)>{});
    ha(fi);
    SB();
    mm(IC<1>{}, fi);
    if constexpr (WIN > 0) {
      constexpr int n = 2 * WIN + f / 2;
      if constexpr (f % 2 == 0 && n < 2 * NS) A[(n & 1) ^ 1][n >> 1] = lda(IC<((n & 1) ^ 1)>{}, IC<(n >> 1)>{});
    }
    if constexpr (f == NL - 1) sfor<NL, NF>([&](auto gi) AI { if constexpr (((gi.v + 6) ^ 1) < NF) ldB(IC<((gi.v + 6) ^ 1)>{}); });
    hb(fi);
    SB();
  });
  sfor<NL, NF>([&](auto fi) AI { mm(IC<1>{}, fi); ha(fi); SB(); });
  sfor<NL, NF>([&](auto fi) AI {
    constexpr int j = fi.v - NL, D = NCT == 5 ? 3 : 1;
    mm(IC<0>{}, fi);
    if constexpr (j >= D) { pin(acc[1][j - D]); *lp<f32x4>(xw + (j - D) * 1024) = acc[1][j - D]; }
    if constexpr (j == NCT - 1) sfor<NCT - D, NCT>([&](auto ki) AI { pin(acc[1][ki.v]); *lp<f32x4>(xw + ki.v * 1024) = acc[1][ki.v]; });
    SB();
  });
}
template <int NCT> __device__ __forceinline__ void xchg_read(int src, f32x4 (&acc)[2][NCT]) {
#pragma unroll
  for (int j = 0; j < NCT; j++) acc[1][j] = ldsf4(src + j * 1024);
  SB();
#pragma unroll
  for (int j = 0; j < NCT; j++) { pin(acc[0][j]); acc[0][j] += acc[1][j]; }
}
__device__ __forceinline__ void store_x(const f32x4 (&x)[5], rsrc_t rws, int so, int ss, int vo, int vo4, int vs) {
  v2u d[5];
#pragma unroll
  for (int j = 0; j < 5; j++) d[j] = pk4(x[j]);
  float s0 = 0.f, q0 = 0.f;
#pragma unroll
  for (int j = 0; j < 5; j++) { s0 = dot2(d[j].x, ONE2, s0); q0 = dot2(d[j].x, d[j].x, q0); s0 = dot2(d[j].y, ONE2, s0); q0 = dot2(d[j].y, d[j].y, q0); }
  st16(join16(d[0], d[1]), rws, vo, so);
  st16(join16(d[2], d[3]), rws, vo + 1024, so);
  st16(join16(d[4], d[4]), rws, vo4 + 2048, so);
  __builtin_amdgcn_raw_buffer_store_b32(asu(xsum2(s0, q0)), rws, vs, ss, SC1);
}
constexpr int QPRE = 9;
constexpr int q_step(int n) { return n % 9 == 0 ? n / 9 * 4 : n / 9 * 4 + (n % 9 - 1) / 2; }
constexpr int q_cnt(int f) { int n = QPRE; for (int i = 0; i <= f; i++) if (i % 2 == 0 && n < 45 && q_step(n) <= i / 8 + 6) n++; return n; }
constexpr int q_free(int f) { int n = 0; for (int i = 0; i < f; i += 2) if (q_cnt(i) == q_cnt(i - 1)) n++; return (f % 2 == 0 && q_cnt(f) == q_cnt(f - 1)) ? n : -100; }
constexpr int l0_vm(int f) { return (f < 72 ? f % 3 == 0 : f < 104 ? f % 4 == 0 : 0) + (f >= 73 && f < 105 && f % 4 == 2) + (f == 1) + (f == 86); }
constexpr int l0_cnt(int f0, int f1) { int n = 0; for (int i = f0; i < f1; i++) n += l0_vm(i); return n; }
constexpr int sp_s(int f) { return f < 14 ? f / 7 : f < 98 ? 2 + ((f - 14) % 28) / 2 : 2 + (f - 98); }
constexpr int sp_j(int f) { return f < 14 ? f % 7 : f < 98 ? (f - 14) / 28 * 2 + (f & 1) : 6; }
#define XA(n) W[(n) / 14][2 + (n) % 14]

__global__ __launch_bounds__(256, 1) void fused_kernel(const char* __restrict__ x0, const char* __restrict__ wqkv, const char* __restrict__ wo,
                                                       const char* __restrict__ win, const char* __restrict__ wsp, const char* __restrict__ bsp,
                                                       const char* __restrict__ wout, char* out, char* ws) {
  const int w_ = __builtin_amdgcn_readfirstlane(threadIdx.x >> 6);
  const int b = blockIdx.x, xcd = b & 7, jb = b >> 3;
  const int pa = xcd * 4 + (jb & 3), ca = jb >> 2, pb = (xcd >> 1) * 8 + (jb & 7), cb = (xcd & 1) * 4 + (jb >> 3);
  const int po_ = MAPO ? pb : pa, co_ = MAPO ? cb : ca, pz_ = MAPZ ? pb : pa, cz_ = MAPZ ? cb : ca;
  const int P_ = MAPQ ? (xcd >> 1) * 4 + (jb & 3) : xcd * 2 + (jb & 1), h_ = MAPQ ? (xcd & 1) * 8 + (jb >> 2) : jb >> 1;
  const rsrc_t rws = mk(ws, WS_USED);
  const v4i dws = mkd(ws, WS_USED);
  u64 t_start = __builtin_amdgcn_s_memrealtime();
  if (threadIdx.x == 0) __hip_atomic_store((GAS u64*)(ws + PROF_OFF) + b, t_start, __ATOMIC_RELAXED, __HIP_MEMORY_SCOPE_AGENT);
  unsigned tag_lo, tag_hi;
  v4u W[4][16];
#pragma unroll
  for (int r = 0; r < 4; r++)
#pragma unroll
    for (int s = 0; s < 16; s++) asm volatile("" : "=a"(W[r][s]));
  {
    LANE();
    {
      const v4i dq = mkd(wqkv, 640 * 3840);
      const int vq = g * 3840 + c16 * 16 + (c16 < 5 ? 1280 : c16 < 10 ? -80 : 2400), s0 = (P * 40 + w * 12) * 3840 + 80 * h, kill = w == 3 ? OOB : 0;
      glds_in(dq, vq, s0, w * 3072);
      glds_in(dq, vq, (s0 + 4 * 3840) | kill, w * 3072 + 1024);
      glds_in(dq, vq, (s0 + 8 * 3840) | kill, w * 3072 + 2048);
    }
    {
      u64 pc; asm volatile("s_getpc_b64 %0" : "=s"(pc));
      touch(mkd((const void*)(pc & ~127ull), 51200), (((jb * 4 + w) & 7) * 64 + l) * 128, 0);
    }
    const int tau = jb, sl = xcd, stg = 32768 + w * 8192;
    const int ty = tau < 8 ? 0 : tau < 24 ? 1 : 2, ot = tau - ty * 8 - (ty >> 1) * 8, cc = ot & 7;
    const int o_kg = opq_u((ty < 2 ? 10 * sl : sl < 4 ? 14 * sl : 4 + 13 * sl) + w2), o_n = opq_u((ty < 2 ? 10 : sl < 4 ? 14 : 13) - w2);
    {
      const char* src = ty == 0 ? wo : ty == 1 ? win : wout;
      const int ldb = ty == 1 ? 3456 : 1280, lim = ty != 1 ? 9 : cc < 6 ? 13 : 11, chi = kh * 8 + (l & 7);
      const int col = ty != 1 ? ot * 160 : (ot < 8 ? 1728 : 0) + (cc < 6 ? 7 * cc : 6 + 6 * cc) * 32;
      const v4i dsrc = mkd(src, (ty == 2 ? 864 : 640) * ldb);
      const int ov = (l >> 3) * ldb + (chi < lim ? chi : lim) * 16;
      const int o0 = opq_u(o_kg * 8 * ldb + col), od = opq_u(16 * ldb), k5 = opq_u(o_n > 10 ? 0 : OOB), k6 = opq_u(o_n > 12 ? 0 : OOB);
      asm volatile("s_nop 4");
#pragma unroll
      for (int i = 0; i < 7; i++) glds_in(dsrc, ov, (o0 + i * od) | (i == 5 ? k5 : i == 6 ? k6 : 0), stg + i * 1024);
    }
    const v2u ep = ld8<SC1>(rws, 0, 0);
    FENCE();
    const rsrc_t rx = mk(x0, 2048 * 1280);
    const int xo = (128 * P + 32 * w) * 1280, ho = P > 0 ? (128 * P - 16) * 1280 : OOB;
    int vx[4];
#pragma unroll
    for (int i = 0; i < 4; i++) vx[i] = (l >> 2) * 1280 + (((l ^ (0 - g)) & 3) + ((i + w) & 3) * 4) * 16;
    v4u R[45];
    auto ldX = [&](auto ni) AI {
      constexpr int n = ni.v, t = q_step(n), i = (n % 9) & 1;
      R[n] = ld16<0>(rx, vx[t & 3] + (t >> 2) * 256, n % 9 == 0 ? ho : xo + i * 16 * 1280);
    };
    const int ct = 2 * w + (g & 1), pq = l & 3;
    const int cb_ = ct == 2 ? 32 + (pq & 1) * 8 + (pq >> 1) * 80 : ct == 7 ? 192 + (pq & 1) * 8 : (ct < 2 ? ct * 48 : ((ct - 1) >> 1) * 80 + ((ct - 1) & 1) * 48) + pq * 8;
    const int trq = opq_v(((g >> 1) * 8 + (c16 >> 2)) * 256 + cb_);
    int qv[3];
#pragma unroll
    for (int i = 0; i < 3; i++) {
      const int k = 2 * i + (g >> 1), kap = 5 * P + k;
      qv[i] = opq_v((k < 5 && (ct < 7 || c16 < 8)) ? (kap >> 2) * 7680 + ct * 1024 + ((kap & 3) * (ct == 7 ? 8 : 16) + c16) * 16 : OOB);
    }
    FENCE();
    sfor<0, 4>(ldX);
    FENCE();
    WAITV(13);
    STAMP(1);
    BAR0();
    {
      v2u lo[3], hi[3];
#pragma unroll
      for (int i = 0; i < 3; i++) { lo[i] = trd(trq + i * 4096); hi[i] = trd(trq + i * 4096 + 1024); }
      SB();
#pragma unroll
      for (int i = 0; i < 3; i++) st16(v4u{lo[i].x, lo[i].y, hi[i].x, hi[i].y}, rws, qv[i], WQ_OFF + h * WQ_T);
      float cs = 0.f;
#pragma unroll
      for (int i = 0; i < 3; i++) { cs = dot2(lo[i].x, ONE2, cs); cs = dot2(lo[i].y, ONE2, cs); cs = dot2(hi[i].x, ONE2, cs); cs = dot2(hi[i].y, ONE2, cs); }
      const v2u t = sw32(asu(cs), asu(cs));
      __builtin_amdgcn_raw_buffer_store_b32(asu(asf(t.x) + asf(t.y)), rws, seloob(l * 4, M_LO), CSQ_OFF + (h * 16 + P) * 512 + w * 128, SC1);
    }
    FENCE();
    sfor<4, 18>(ldX);
    FENCE();
    {
      const u64 epoch = ((u64)(unsigned)__builtin_amdgcn_readfirstlane(ep.y) << 32) | (unsigned)__builtin_amdgcn_readfirstlane(ep.x);
      const u64 tag = epoch * 0x9E3779B97F4A7C15ull + 0xD1B54A32D192ED03ull;
      tag_lo = opq_s((unsigned)tag); tag_hi = opq_s((unsigned)(tag >> 32));
    }
    WAITV(14);
    BAR0();
    flag(F0C + h * 256 + P * 8);
    STAMP(2);
    float xs[3] = {0.f, 0.f, 0.f}, xq[3] = {0.f, 0.f, 0.f};
    const int xwr = opq_v(16384 + w * 4096 + l16), xrd = opq_v(16384 + w * 4096 + c16 * 64 + ((g ^ (0 - (c16 >> 2))) & 3) * 16);
    auto doX = [&](auto ni) AI {
      constexpr int n = ni.v, i = n % 9 == 0 ? 2 : (n % 9) & 1;
      *lp<v4u>(xwr + (n & 3) * 1024) = R[n];
      ldsa16<(n & 3) * 1024>(XA(n), xrd);
#pragma unroll
      for (int e = 0; e < 4; e++) { xs[i] = dot2(R[n][e], ONE2, xs[i]); xq[i] = dot2(R[n][e], R[n][e], xq[i]); }
      pin(xs[i]); pin(xq[i]);
      SB();
    };
    sfor<18, 45>([&](auto ni) AI { ldX(ni); doX(IC<ni.v - 18>{}); });
    {
      const int tro = stg + (c16 >> 2) * 128 + g * 32 + (l & 3) * 8;
      const int o_v = kh * 4 + g < (ty == 1 ? 7 : 5) ? (kh * 4 + g) * 1024 + c16 * 16 : OOB;
      const int o_dst = ty == 0 ? WO_OFF + ot * WO_T : ty == 1 ? WI_OFF + ot * WI_T : WU_OFF + ot * WU_T, kb = ty < 2 ? 10 : 14, kstr = ty == 1 ? 7168 : 5120;
      float cs = 0.f;
#pragma unroll
      for (int i = 0; i < 7; i++) {
        const v2u lo = trd(tro + i * 1024), hi = trd(tro + i * 1024 + 512);
        if (i < 5) { cs = dot2(lo.x, ONE2, cs); cs = dot2(lo.y, ONE2, cs); cs = dot2(hi.x, ONE2, cs); cs = dot2(hi.y, ONE2, cs); }
        const int kap = o_kg + 2 * i, ks = kap >> 2, pos = ks < kb ? 2 * ks : 2 * (ks - kb) + 1;
        st16(v4u{lo.x, lo.y, hi.x, hi.y}, rws, o_v, opq_u(2 * i < o_n ? o_dst + pos * kstr + (kap & 3) * 256 : OOB));
      }
      __builtin_amdgcn_raw_buffer_store_b32(asu(cs), rws, l * 4, opq_u(ty == 1 ? CSI_OFF + (ot * 16 + sl * 2 + w2) * 512 + kh * 256 : OOB), SC1);
      st16(v4u{0, 0, 0, 0}, rws, seloob(g * ST_LAY + c16 * 16, M_LO), ST_OFF + po * 8192 + w * 2048 + co * 256);
    }
    FENCE();
    sfor<27, 45>(doX);
#pragma unroll
    for (int i = 0; i < 3; i++) { xs[i] += dpp<0xB1>(xs[i]); xq[i] += dpp<0xB1>(xq[i]); xs[i] += dpp<0x4E>(xs[i]); xq[i] += dpp<0x4E>(xq[i]); }
#pragma unroll
    for (int i = 0; i < 2; i++) {
      const f32x2 v = finish_stat(xs[i], xq[i], 1.f / 640.f);
      *lp<float>(L_XST1 + w * 512 + i * 64 + (l & 60)) = v.x; *lp<float>(L_XST1 + w * 512 + 256 + i * 64 + (l & 60)) = v.y;
    }
    *lp<f32x2>(L_HST + w * 128 + (l >> 2) * 8) = f32x2{xs[2], xq[2]};
    STAMP(3);
  }

#pragma unroll 1
  for (int Lr = 0;; Lr = 1) {
    const int L = opq_s(Lr), kill1 = L << 30;
    const int act = ACT_OFF + L * ACT_LAY, fl = FLAY + L * FLAY_SZ;
    const int sb = L * 24 + 5;
    f32x4 z1[7];
    unsigned braw;
    constexpr int RZ = 14;
    v4u Z[RZ];
    { LANE();
      const int qo = qslot_off(w), wq = WQ_OFF + h * WQ_T + qo;
      int vb[4], vh[4];
#pragma unroll
      for (int i = 0; i < 4; i++) { const int k = (i + w) & 3; vb[i] = opq_v(l16 + k * 7680); vh[i] = opq_v((g * 8 + (l & 7)) * 16 + k * 7680); }
      const int h2 = w < 2 ? w + 4 : w;
      const int c4 = opq_v(c16 * 4), g16 = opq_v(g * 16);
      const int wos = opq_s(WO_OFF + co * WO_T), wzs = opq_s(WI_OFF + cz * WI_T);
      f32x4 acc[2][8];
      float mt[3], rt_[3], csn[3], hcn; f32x4 mn[3], rn[3], cst[5], ha[2][4], hcs;
      auto qkv = [&](auto l0) AI {
        constexpr bool L0 = l0.v;
        v4u A[2][20], Hh[5], B[8];
        int va[4], spo, xoff, hoff, fbo, cqo, wv, wr, wk; v4i dw;
        auto ldB = [&](auto fi) AI { constexpr int f = fi.v, t = f >> 3, ct = f & 7; B[f & 7] = lds16((ct < 7 ? vb[t & 3] : vh[t & 3]) + ((t >> 2) & 1) * 30720 + ct * 1024); };
        auto ldA = [&](auto ni) AI {
          constexpr int n = ni.v, t = q_step(n), i = (n % 9) & 1;
          if constexpr (n % 9 == 0) Hh[t >> 2] = ld16<SC1>(rws, va[0], hoff + (t >> 2) * 4096);
          else A[i][t] = ld16<SC1>(rws, va[t & 3], xoff + i * XT + (t >> 2) * 4096);
        };
        if constexpr (L0) {
          dw = mkd(wsp, 2048 * 4096);
          wv = opq_v((l >> 2) * 4096 + ((l ^ (0 - g)) & 3) * 16);
          wr = opq_v(24576 + w * 8192 + c16 * 64 + ((g ^ (0 - (c16 >> 2))) & 3) * 16);
          wk = opq_s(16 * w + ((MAPZ ? pz + 2 * cz + (cz >> 2) : 2 * pz + cz) & 7) * 2);
          fbo = opq_v(seloob(F0B + ((l >> 3) * 8 + (((l >> 3) + 1) & 2 ? cz : co)) * 256 + (l & 7) * 8, M_LO));
          cqo = opq_v((l >> 5) * 4096 + (l & 31) * 4);
          poll(rws, seloob(F0C + h * 256 + c16 * 8, M_G0), l >= 16, tag_lo, tag_hi);
          BAR();
          STAMP(sb - 1);
          cp_run<0, 8>(dws, l16, wq, qo); cp_run<0, 8>(dws, l16, wq + 30720, qo + 30720);
          const f32x2 a = (*lp<f32x2>(L_HST + c16 * 8) + *lp<f32x2>(L_HST + 128 + c16 * 8)) + (*lp<f32x2>(L_HST + 256 + c16 * 8) + *lp<f32x2>(L_HST + 384 + c16 * 8));
          const f32x2 v = finish_stat(a.x, a.y, 1.f / 640.f);
          *lp<float>(L_XST1 + w * 512 + 128 + c4) = v.x; *lp<float>(L_XST1 + w * 512 + 384 + c4) = v.y;
          WAITV(8);
          BAR0();
          sfor<0, 6>([&](auto fi) AI { ldB(IC<(fi.v ^ 1)>{}); SB(); });
        } else {
#pragma unroll
          for (int i = 0; i < 4; i++) va[i] = opq_v(l16 + ((i + w) & 3) * 1024);
          spo = opq_v(g < 2 ? (2 * w + 1 + g) * XT + c16 * 8 : (g == 2 && P > 0) ? c16 * 8 : OOB);
          xoff = opq_s(act + A_XIN + (8 * P + 2 * w) * XT); hoff = opq_s(P > 0 ? act + A_XIN + (8 * P - 1) * XT : OOB);
          sfor<0, 6>([&](auto fi) AI { ldB(IC<(fi.v ^ 1)>{}); SB(); });
          const bool on = l < 8 || (l < 16 && P > 0);
          poll(rws, on ? FLAY + F6 + (l < 8 ? 2 * P + w2 : 2 * P - 1) * 256 + (l & 7) * 8 : OOB, !on, tag_lo, tag_hi);
          STAMP(sb - 1);
          sfor<0, QPRE>(ldA);
          SB();
        }
        f32x4 hac[6];
        v2u sp[8], fb; float ss[8], sq[8]; int xa, xb;
        sfor<0, 160>([&](auto fi) AI {
          constexpr int f = fi.v, t = f >> 3, ct = f & 7, hc = ct < 3 ? ct : ct - 2, xn = (t >> 2) * 9 + 1 + (t & 3) * 2;
          constexpr bool halo = (t & 3) == 0 && (ct < 3 || ct >= 5);
          constexpr int fr = L0 ? -100 : q_free(f);
          constexpr int si = f == 33 ? 0 : f == 37 ? 1 : f == 39 ? 2 : f == 62 ? 3 : f == 65 ? 4 : f == 68 ? 5 : f == 69 ? 6 : f == 71 ? 7 : -1; (void)si;
          const v4u bb = B[f & 7];
          auto mm = [&](f32x4& d, const v4u& x) AI {
            if constexpr (ct < 5) mma<t == 0, L0>(d, bb, x); else if constexpr (L0) mma_aa<t == 0>(d, x, bb); else mma<t == 0, false>(d, x, bb);
            SB();
          };
          if constexpr (L0) mm(acc[0][ct], XA(xn + 1)); else mm(acc[0][ct], A[0][t]);
          if constexpr (L0 && f == 26) WAITV(l0_cnt(0, 26));
          if constexpr (L0 && f == 58) WAITV(l0_cnt(22, 58));
          if constexpr (L0 && f == 90) WAITV(l0_cnt(46, 90));
          if constexpr (f == 26 || (L0 && (f == 58 || f == 67 || f == 90)) || (!L0 && f == 122)) BAR0();
          if constexpr (L0 && f == 122) BAR();
          if constexpr (((f + 6) ^ 1) < 160) ldB(IC<((f + 6) ^ 1)>{});
          if constexpr ((t & 7) >= 4 && t < 16 && ct == 3) vb[t & 3] = gaddi<61440>(vb[t & 3]);
          if constexpr ((t & 7) >= 4 && t < 16 && ct == 5) vh[t & 3] = gaddi<61440>(vh[t & 3]);
          if constexpr (L0 && f % 12 == 0 && f < 72) cp_arm(qo + (2 + f / 24) * 30720 + (f / 12 & 1) * 4096);
          if constexpr (L0 && f >= 72 && f < 104 && f % 4 == 0) cp_arm(24576 + w * 8192 + (f - 72) * 256);
          if constexpr (fr == 0 || fr == 4) cp_arm(qo + 4 * 30720 + fr * 1024);
          if constexpr (f == 124 || f == 140) cp_arm(L_WO + w * 8192 + (f - 124) * 256);
          if constexpr (!L0) {
            if constexpr (f >= 96 && f < 104 && !(f & 1)) ss[(f - 96) / 2] = gadd(asf(sp[(f - 96) / 2].x), asf(sp[(f - 96) / 2 + 4].x));
            if constexpr (f == 104 || f == 106) ss[(f - 104) / 2] = gadd(ss[(f - 104) / 2], ss[(f - 104) / 2 + 2]);
            if constexpr (f == 108) ss[0] = gadd(ss[0], ss[1]);
            if constexpr (f == 110) { ss[0] = ss[0] * (1.f / 640.f); pin(ss[0]); }
            if constexpr (f == 111) ss[1] = gmul(ss[0], ss[0]);
            if constexpr (f == 113) { sq[0] = sq[0] * (1.f / 640.f) - ss[1]; pin(sq[0]); }
            if constexpr (f == 114) { sq[0] = fmaxf(sq[0], 0.f); pin(sq[0]); }
            if constexpr (f == 116) { sq[0] += 1e-5f; pin(sq[0]); }
            if constexpr (f == 118) { sq[0] = __builtin_amdgcn_rsqf(sq[0]); pin(sq[0]); }
            if constexpr (f == 119) xa = opq_v(L_XST1 + w * 512 + l * 4);
            if constexpr (f == 120) *lp<float>(xa) = ss[0];
            if constexpr (f == 121) *lp<float>(xa + 256) = sq[0];
          } else {
            if constexpr (f == 118) {
              const float cs = ((ss[0] + ss[1]) + (ss[2] + ss[3])) + ((ss[4] + ss[5]) + (ss[6] + ss[7]));
              const v2u t2 = sw32(asu(cs), asu(cs));
              *lp<float>(L_CSQ + w * 128 + (l & 31) * 4) = asf(t2.x) + asf(t2.y);
            }
          }
          if constexpr (f == 126) xb = opq_v(L_XST1 + w * 512 + g16);
          if constexpr (f >= 127 && f < 130) mn[f - 127] = ldsf4(xb + (f - 127) * 64);
          if constexpr (f == 130 || f == 131 || f == 133) rn[f == 133 ? 2 : f - 130] = ldsf4(xb + 256 + (f == 133 ? 2 : f - 130) * 64);
          if constexpr (f == 134) xa = opq_v(L_HALO + w * 6144 + l16);
          if constexpr (f == 142) xb = opq_v(L_CSQ + g16);
          if constexpr (f >= 143 && f < 148) cst[f - 143] = ldsf4(xb + (f - 143) * 64);
          if constexpr (f == 148) xb = opq_v(xb + w * 64);
          if constexpr (f == 149) hcs = ldsf4(xb);
          if constexpr (f == 150) xa = opq_v(L_HALO + w * 1024 + l16);
          if constexpr (f == 151) xb = opq_v(L_HALO + h2 * 1024 + l16);
          if constexpr (f >= 152 && f < 156) ha[0][f - 152] = ldsf4(xa + (f - 152) * 6144);
          SB();
          if constexpr (L0) mm(acc[1][ct], XA(xn)); else mm(acc[1][ct], A[1][t]);
          if constexpr (L0) {
            if constexpr (f % 3 == 0 && f < 72) cp_one<f / 3>(dws, l16, wq + (2 + f / 24) * 30720 + (f / 12 & 1) * 4096);
            if constexpr (f == 1) flag(F0B + jb * 256 + xcd * 8);
            if constexpr (f >= 72 && f < 104 && f % 4 == 0) {
              constexpr int n = (f - 72) / 4;
              asm volatile("buffer_load_dwordx4 %0, %1, %2 offen lds" ::"v"(wv), "s"(dw), "s"((64 * pz + 16 * ((w + (n >> 1)) & 3)) * 4096 + (wk + (n & 1)) * 64) : "memory");
            }
            if constexpr (f >= 73 && f < 105 && f % 4 == 2) ss[(f - 74) / 4] = asf(__builtin_amdgcn_raw_buffer_load_b32(rws, cqo + (f - 74) * 128, CSQ_OFF + h * 8192 + w * 128, SC1));
            if constexpr (f == 86) fb = ld8<SC1>(rws, fbo, 0);
            if constexpr (f == 119) { if (!__all(l >= 32 || (fb.x == tag_lo && fb.y == tag_hi))) poll(rws, fbo, l >= 32, tag_lo, tag_hi); }
            if constexpr (f == 120 || f == 121) sfor<0, 4>([&](auto ki) AI { constexpr int n = (f - 120) * 4 + ki.v; ldsa16<n * 1024>(W[n >> 1][n & 1], wr); });
            if constexpr (f >= 130 && f < 160 && f % 4 == 2) {
              constexpr int n = (f - 130) / 4;
              sta16<(n & 1) * 1024>(W[n >> 1][n & 1], dws, l16, WS_OFF + ((4 * pz + ((w + (n >> 1)) & 3)) * 64 + wk) * 1024);
            }
          } else {
            if constexpr (q_cnt(f) > q_cnt(f - 1)) ldA(IC<q_cnt(f - 1)>{});
            if constexpr (fr >= 0 && fr < 8) cp_one<fr & 7>(dws, l16, wq + 4 * 30720 + (fr & 4) * 1024);
            if constexpr (si >= 0) sp[si] = ld8<SC1>(rws, spo + si * 128, act + A_XIN + (8 * P - 1) * XT + 20480);
            if constexpr (f >= 96 && f < 104 && (f & 1)) sq[(f - 96) / 2] = gadd(asf(sp[(f - 96) / 2].y), asf(sp[(f - 96) / 2 + 4].y));
            if constexpr (f == 105 || f == 107) sq[(f - 105) / 2] = gadd(sq[(f - 105) / 2], sq[(f - 105) / 2 + 2]);
            if constexpr (f == 109) sq[0] = gadd(sq[0], sq[1]);
          }
          if constexpr (f == 123) xa = opq_v(L_XST1 + w * 512 + c4);
          if constexpr (!L0 && f == 123) asm volatile("" ::"v"(A[0][19]));
          if constexpr (f >= 124 && f < 156 && f % 4 == 0) cp_one<(f - 124) / 4>(dws, l16, wos + w * 8192 + (f - 124) / 16 * 4096);
          if constexpr (f == 125 || f == 127 || f == 131) { constexpr int i = f == 131 ? 2 : (f - 125) / 2; mt[i] = ldsf(xa + i * 64); rt_[i] = ldsf(xa + 256 + i * 64); }
          if constexpr (f >= 136 && f < 142) *lp<f32x4>(xa + (f - 136) * 1024) = hac[f - 136];
          if constexpr (f == 143) xa = opq_v(L_CSQ + c4);
          if constexpr (f == 144) { csn[0] = ldsf(xa + 320); csn[1] = ldsf(xa + 384); }
          if constexpr (f == 146) { csn[2] = ldsf(xa + 448); xa = opq_v(xa + (h2 + 2) * 64); }
          if constexpr (f == 148) hcn = ldsf(xa);
          if constexpr (f == 150) BAR0();
          if constexpr (f >= 152 && f < 156) ha[1][f - 152] = ldsf4(xb + (f - 152) * 6144);
          if constexpr (halo) { SB(); if constexpr (L0) mm(hac[hc], XA(xn - 1)); else mm(hac[hc], Hh[t >> 2]); }
          SB();
        });
        mma_settle();
#pragma unroll
        for (int j = 0; j < 8; j++) { pin(acc[0][j]); pin(acc[1][j]); }
      };
      if (L == 0) qkv(IC<1>{}); else qkv(IC<0>{});
      STAMP(sb + 0);
      const int nbw = opq_v(L_NB + l * 8), w2s = wos + 32768 + w * 8192, w2d = L_WO + 32768 + w * 8192, w3s = wos + 65536 + w * 9216, w3d = L_WO + 65536 + w * 9216;
      cp_run<0, 3>(dws, l16, w2s, w2d);
      {
        const f32x4 a0 = (ha[0][0] + ha[0][1]) + (ha[0][2] + ha[0][3]), a1 = (ha[1][0] + ha[1][1]) + (ha[1][2] + ha[1][3]);
        *lp<v2u>(nbw + (w < 3 ? w * 512 : 15360)) = pk4((a0 - mt[2] * hcs) * rt_[2]);
        *lp<v2u>(nbw + (w != 2 ? h2 * 512 : 15360)) = pk4((a1 - mn[2] * hcn) * rn[2]);
      }
      v2u tk[2][8];
      auto fix = [&](int i, int ct) AI { tk[i][ct] = ct < 5 ? pk4((acc[i][ct] - mt[i] * cst[ct]) * rt_[i]) : pk4((acc[i][ct] - mn[i] * csn[ct - 5]) * rn[i]); };
      fix(1, 0); fix(1, 1); fix(1, 2); fix(1, 5); fix(1, 6); fix(1, 7);
      const int nbr = opq_v(nbw + w * 3072);
#pragma unroll
      for (int i = 0; i < 6; i++) *lp<v2u>(nbr + 3072 + i * 512) = tk[1][i < 3 ? i : i + 2];
      cp_run<3, 6>(dws, l16, w2s, w2d);
      SB();
      fix(0, 0); fix(0, 1); fix(0, 2); fix(1, 3); fix(1, 4);
      BAR();
      v2u nb[6];
#pragma unroll
      for (int i = 0; i < 6; i++) { nb[i] = lds8(nbr + i * 512); asm volatile("" ::: "memory"); }
      cp_run<6, 8>(dws, l16, w2s, w2d);
      STAMP(sb + 1);
      SB();
      fix(0, 3); fix(0, 4);
      v6u K6[3]; v4u Q[2][2];
      {
        const v2u qx = sw32(tk[0][2].x, tk[1][2].x), qy = sw32(tk[0][2].y, tk[1][2].y);
        const unsigned q1x = sw32(qx.y, qx.y).y, q1y = sw32(qy.y, qy.y).y;
        Q[0][0] = v4u{tk[0][3].x, tk[0][3].y, sel0(qx.y, M_G0), sel0(qy.y, M_G0)};
        Q[0][1] = v4u{sel0(qx.y, M_G1), sel0(qy.y, M_G1), tk[0][4].x, tk[0][4].y};
        Q[1][0] = v4u{tk[1][3].x, tk[1][3].y, sel0(q1x, M_G0), sel0(q1y, M_G0)};
        Q[1][1] = v4u{sel0(q1x, M_G1), sel0(q1y, M_G1), tk[1][4].x, tk[1][4].y};
      }
#pragma unroll
      for (int i = 0; i < 2; i++) {
        K6[i + 1] = v6u{tk[i][0].x, tk[i][0].y, tk[i][2].x, tk[i][2].y, tk[i][1].x, tk[i][1].y};
        pin(Q[i][0]); pin(Q[i][1]); pin(K6[i + 1]);
      }
      K6[0] = v6u{nb[0].x, nb[0].y, nb[2].x, nb[2].y, nb[1].x, nb[1].y};
      pin(K6[0]);
      NOP2();
      f32x4 S[4][2];
#pragma unroll
      for (int u = 3; u >= 0; u--)
#pragma unroll
        for (int k = 1; k >= 0; k--) {
          const v6u kk = K6[(u >> 1) + k];
          mma<true, false>(S[u][k], (u & 1) ? __builtin_shufflevector(kk, kk, 2, 3, 4, 5) : __builtin_shufflevector(kk, kk, 0, 1, 2, 3), Q[u >> 1][u & 1]);
        }
      SB();
      cp_run<0, 3>(dws, l16, w3s, w3d);
      unsigned sel[4];
      {
        const unsigned zz = 0x0c0c0c0cu, a = ::sel(0x0c0c0504u, 0x05040c0cu, M_ODD), d = ::sel(0x0c0c0706u, 0x07060c0cu, M_ODD);
        sel[2] = ::sel(zz, a, le_mask(0)); sel[3] = ::sel(zz, d, le_mask(2));
        sel[0] = ::sel(a, zz, le_mask(0)); sel[1] = ::sel(d, zz, le_mask(2));
      }
      const int ovo = (g & 1) * 768 + (g >> 1) * 256 + c16 * 16, ovt = seloob(g * OT + 512 + c16 * 16, M_LO);
      fix(0, 5); fix(0, 6); fix(0, 7);
      v6u V6[3];
#pragma unroll
      for (int j = 0; j < 3; j++) { V6[j] = v6u{nb[3 + j].x, nb[3 + j].y, tk[0][5 + j].x, tk[0][5 + j].y, tk[1][5 + j].x, tk[1][5 + j].y}; pin(V6[j]); }
      SB();
#pragma unroll
      for (int u = 0; u < 8; u++) pin(S[u >> 1][u & 1]);
      if (P + w == 0) { S[0][0] = f32x4{-1e30f, -1e30f, -1e30f, -1e30f}; pin(S[0][0]); S[1][0] = S[0][0]; }
      float s0[4], s1[4], mx[4];
#pragma unroll
      for (int u = 0; u < 4; u++) {
        const f32x4 a = S[u][0], bq = S[u][1];
        s0[u] = self(self(a[0], a[1], M_ODD), self(bq[0], bq[1], M_ODD), le_mask(0));
        s1[u] = self(self(a[2], a[3], M_ODD), self(bq[2], bq[3], M_ODD), le_mask(2));
        mx[u] = vmax(s0[u], s1[u]);
      }
      auto spread = [&](float t, float (&o)[4]) AI {
        const v2u a = sw16(asu(t), asu(t)), x = sw32(a.x, a.x), y = sw32(a.y, a.y);
        o[0] = asf(x.x); o[1] = asf(x.y); o[2] = asf(y.x); o[3] = asf(y.y);
      };
      const float kk = 0.22360679774997896f * 1.4426950408889634f;
      {
        const v2u a = sw32(asu(mx[0]), asu(mx[1])), bq = sw32(asu(mx[2]), asu(mx[3]));
        const v2u d = sw16(asu(vmax(asf(a.x), asf(a.y))), asu(vmax(asf(bq.x), asf(bq.y))));
        spread(vmax(asf(d.x), asf(d.y)), mx);
      }
      SB();
      cp_run<3, 6>(dws, l16, w3s, w3d);
      float sm4[4];
#pragma unroll
      for (int u = 0; u < 4; u++) {
        const f32x2 e = (f32x2{s0[u], s1[u]} - mx[u]) * kk;
        s0[u] = __builtin_amdgcn_exp2f(e.x); s1[u] = __builtin_amdgcn_exp2f(e.y);
        sm4[u] = s0[u] + s1[u];
      }
      {
        const v2u a = sw32(asu(sm4[0]), asu(sm4[1])), bq = sw32(asu(sm4[2]), asu(sm4[3]));
        const v2u d = sw16(asu(asf(a.x) + asf(a.y)), asu(asf(bq.x) + asf(bq.y)));
        spread(__builtin_amdgcn_rcpf(asf(d.x) + asf(d.y)), sm4);
      }
      v4u pf[4];
#pragma unroll
      for (int u = 0; u < 4; u++) {
        const f32x2 pn = f32x2{s0[u], s1[u]} * sm4[u];
        const unsigned pp = pk(pn.x, pn.y);
        pf[u] = v4u{__builtin_amdgcn_perm(pp, 0u, sel[0]), __builtin_amdgcn_perm(pp, 0u, sel[1]), __builtin_amdgcn_perm(pp, 0u, sel[2]), __builtin_amdgcn_perm(pp, 0u, sel[3])};
        pin(pf[u]);
      }
      NOP2();
      f32x4 Om[4], Ot[4];
#pragma unroll
      for (int u = 0; u < 4; u++) { const v6u v = V6[u & 1]; mma<true, false>(Om[u], (u >> 1) ? __builtin_shufflevector(v, v, 2, 3, 4, 5) : __builtin_shufflevector(v, v, 0, 1, 2, 3), pf[u]); }
#pragma unroll
      for (int u = 0; u < 4; u++) { const v6u v = V6[2]; mma<true, false>(Ot[u], (u >> 1) ? __builtin_shufflevector(v, v, 2, 3, 4, 5) : __builtin_shufflevector(v, v, 0, 1, 2, 3), pf[u]); }
      asm volatile("s_nop 3");
#pragma unroll
      for (int u = 0; u < 4; u++) pin(Om[u]);
      const int so = act + A_O + (8 * P + 2 * w) * OT + 5 * h * 256;
      st16(join16(pk4(Om[0]), pk4(Om[1])), rws, ovo, so);
      st16(join16(pk4(Om[2]), pk4(Om[3])), rws, ovo, so + OT);
      SB();
#pragma unroll
      for (int u = 0; u < 4; u++) pin(Ot[u]);
      {
        const v2u x0_ = pk4(Ot[0]), y0_ = pk4(Ot[1]), x1_ = pk4(Ot[2]), y1_ = pk4(Ot[3]);
        st16(v4u{sw16(x0_.x, x1_.x).x, sw16(x0_.y, x1_.y).x, sw16(y0_.x, y1_.x).y, sw16(y0_.y, y1_.y).y}, rws, ovt, so);
      }
      STAMP(sb + 2);
      cp_run<6, 9>(dws, l16, w3s, w3d);
      cp_run<0, 6>(dws, l16, wzs + w * 6144, w * 6144);
      WAITV(9);
      BAR();
      flag(fl + F1 + P * 256 + h * 8);
      flag((F0S + pz * 256 + cz * 8) | kill1);
      STAMP(sb + 3);
      touch(dws, (g < 2 && c16 < 10) ? g * OT + c16 * 128 : OOB, so);
      cp_run<0, 8>(dws, l16, wzs + 24576 + w * 8192, 24576 + w * 8192);
    }
    { LANE();
      f32x4 acc[2][5];
      v2u rr[5];
      const int tile = opq_s(act + A_X1 + (4 * po + w) * XT), wzs = opq_s(WI_OFF + cz * WI_T);
      if (L == 0) {
        const rsrc_t rx = mk(x0, 2048 * 1280);
#pragma unroll
        for (int j = 0; j < 5; j++) rr[j] = ld8<0>(rx, c16 * 1280 + g * 8 + j * 32, (64 * po + 16 * w) * 1280 + co * 160);
        float cs = 0.f;
#pragma unroll
        for (int i = 0; i < 16; i++) cs += asf(__builtin_amdgcn_raw_buffer_load_b32(rws, l * 4 + (i & 7) * 512, CSI_OFF + (w2 * 8 + cz) * 8192 + kh * 256 + (i >> 3) * 4096, SC1));
        *lp<float>(L_CS2 + (w * 64 + l) * 4) = cs;
      } else {
#pragma unroll
        for (int j = 0; j < 5; j++) rr[j] = ld8<SC1>(rws, (g >> 1) * 256 + c16 * 16 + (g & 1) * 8 + j * 512, tile + (A_XIN - A_X1) + co * 2560);
      }
      const int vo = opq_v((g & 1) * 512 + (g >> 1) * 256 + c16 * 16), vo4 = opq_v(seloob(vo, M_GEVEN)), vs = opq_v(seloob(c16 * 8 + (g >> 1) * 4, M_GEVEN));
      const int xw = opq_v(L_X2 + (w ^ 1) * 5120 + l16), xr = opq_v(L_X2 + w * 5120 + l16);
      const int a0 = opq_s(act + A_O + (4 * po + w) * OT + kh * 10240), a1 = opq_s(act + A_O + (4 * po + (w ^ 1)) * OT + kh * 10240);
      const int b0 = opq_v(L_WO + l16 + kh * 5120), b1 = opq_v(b0 + 51200);
      const bool pon = l < 8 || (l < 16 && L == 0);
      const int pv = opq_v(pon ? (l < 8 ? fl + F1 + (po >> 1) * 256 + kh * 64 : F0S + pz * 256 - 64) + l * 8 : OOB);
      v4u A[2][10];
      gemm_m2k2<10, 5, true, 5, true>([&](auto ti) AI { return ti.v < 5 ? b0 + ti.v * 10240 : b1 + (ti.v - 5) * 10240; }, xw, A, acc,
          [&]() AI {
#pragma unroll
        for (int j = 0; j < 5; j++) { acc[0][j] = unpk4(rr[j]); pin(acc[0][j]); }
        poll(rws, pv, !pon, tag_lo, tag_hi);
        STAMP(sb + 4);
      },
          [&](auto ii, auto ti) AI { return ld16<SC1>(rws, l16 + (ti.v & 3) * 1024, (ii.v ? a1 : a0) + (ti.v >> 2) * 4096); },
          [&](auto fi) AI { if constexpr (fi.v == 13) BAR0(); },
          [&](auto) AI {});
      STAMP(sb + 5);
      BAR();
      xchg_read<5>(xr, acc);
      store_x(acc[0], rws, tile + co * 2560, tile + 20480 + co * 128, vo, vo4, vs);
      cp_run<0, 8>(dws, l16, wzs + 71680 + w * 10240, L_Z2B + 71680 + w * 10240);
      WAITV(8);
      BAR();
      flag(fl + F2 + po * 256 + co * 8);
      STAMP(sb + 6);
      touch(dws, l < 20 ? co * 2560 + l * 128 : l == 20 ? 20480 + co * 128 : OOB, tile);
      cp_run<0, 4>(dws, l16, wzs + 57344 + ((w * 3584) & ~1023), L_Z2B + 57344 + ((w * 3584) & ~1023));
      cp_run<8, 10>(dws, l16, wzs + 71680 + w * 10240, L_Z2B + 71680 + w * 10240);
      cp_run<0, 8>(dws, l16, wzs + 112640 + qslot_off(w), L_Z2B + 112640 + qslot_off(w));
    }
    { LANE(); WSP();
      const int wz1 = opq_s(WI_OFF + (8 + cz) * WI_T);
      const int a0 = opq_s(act + A_X1 + (4 * pz + w) * XT + kh * 10240), a1 = opq_s(act + A_X1 + (4 * pz + (w ^ 1)) * XT + kh * 10240);
      int xw = opq_v(L_X3 + (w ^ 1) * 7168 + l16), xr = opq_v(L_X3 + w * 7168 + l16);
      const int gso = opq_v(seloob(l * 4, 0x1111111111111111ull)), gro = opq_v(((l >> 2) & 3) * 256 + (4 * g + (l & 3)) * 16);
      const int zvo = opq_v((g & 1) * 1024 + (g >> 1) * 256 + c16 * 16), spo = opq_v(g * 256 + c16 * 8);
      const int sta = opq_v(L_XST3 + w * 128 + (g & 1) * 64 + c16 * 4), stb = opq_v(L_XST3 + w * 128 + g * 16), tv = opq_v(l * 128);
      const int b0 = opq_v(l16 + kh * 7168), b1 = opq_v(b0 + L_Z1B + 3 * 14336), b2 = opq_v(b1 + 4 * 14336);
      static_assert(L_Z1B == L_Z2B, "");
      const int sps = opq_s(act + A_X1 + (4 * pz + w) * XT + 20480), za0 = opq_s((w * 10752) & ~1023);
      float cs2[7];
#pragma unroll
      for (int j = 0; j < 7; j++) { cs2[j] = ldsf(L_CS2 + c16 * 4 + j * 64); pin(cs2[j]); }
      braw = __builtin_amdgcn_raw_buffer_load_b16(mk(bsp, 4096), c16 * 2, (64 * pz + 16 * w) * 2, 0);
      v4u A[2][10]; f32x4 acc[2][7];
      v2u sp0, sp1, u_;
      float m1, r1, st_; f32x4 m4, r4;
      gemm_m2k2<10, 7, false, 5, false>([&](auto ti) AI { return ti.v < 4 ? b0 + ti.v * 14336 : ti.v < 7 ? b1 + (ti.v - 3) * 14336 : b2 + (ti.v - 7) * 14336; }, xw, A, acc,
          [&]() AI { poll(rws, seloob(fl + F2 + pz * 256 + l * 8, 0xFFull), l >= 8, tag_lo, tag_hi); pin(braw); STAMP(sb + 7); },
          [&](auto ii, auto ti) AI { return ld16<SC1>(rws, l16 + (ti.v & 3) * 1024, (ii.v ? a1 : a0) + (ti.v >> 2) * 4096); },
          [&](auto fi) AI {
        constexpr int f = fi.v;
        if constexpr (f == 15 || f == 31) BAR0();
        if constexpr (f == 46 || f == 54 || f == 62) cp_arm(za0 + (f - 46) * 512);
        if constexpr (f == 47) { u_ = sw32(asu(m1), asu(r1)); pin(u_); }
        if constexpr (f == 48) { st_ = gadd(asf(u_.x), asf(u_.y)); }
        if constexpr (f == 49) { u_ = sw16(asu(st_), asu(st_)); pin(u_); }
        if constexpr (f == 50) { st_ = gadd(asf(u_.x), asf(u_.y)); }
        if constexpr (f == 51) { u_ = sw32(asu(st_), asu(st_)); pin(u_); }
        if constexpr (f == 52) { m1 = asf(u_.x) * (1.f / 640.f); pin(m1); }
        if constexpr (f == 53) { st_ = gmul(m1, m1); }
        if constexpr (f == 55) { r1 = asf(u_.y) * (1.f / 640.f) - st_; pin(r1); }
        if constexpr (f == 56) { r1 = fmaxf(r1, 0.f); pin(r1); }
        if constexpr (f == 57) { r1 += 1e-5f; pin(r1); }
        if constexpr (f == 58) { r1 = __builtin_amdgcn_rsqf(r1); pin(r1); }
        if constexpr (f == 60) *lp<float>(sta) = self(r1, m1, M_GEVEN);
        if constexpr (f == 63) cp_one<1>(dws, l16, wz1 + za0 + 8192);
        if constexpr (f == 65) { m4 = ldsf4(stb); r4 = ldsf4(stb + 64); }
        if constexpr (f == 67) cp_one<2>(dws, l16, wz1 + za0 + 8192);
      },
          [&](auto fi) AI {
        constexpr int f = fi.v;
        if constexpr (f == 20) sp0 = ld8<SC1>(rws, spo, sps);
        if constexpr (f == 22) sp1 = ld8<SC1>(rws, spo + 128, sps);
        if constexpr (f == 44) { m1 = gadd(asf(sp0.x), asf(sp1.x)); }
        if constexpr (f == 45) { r1 = gadd(asf(sp0.y), asf(sp1.y)); }
        if constexpr (f >= 46 && !(f & 1)) cp_one<(f - 46) / 2>(dws, l16, wz1 + za0 + (f - 46) / 8 * 4096);
      });
      STAMP(sb + 8);
      BAR();
      xchg_read<7>(xr, acc);
      BAR();
      f32x4 z2[7], zs, zq;
      const int gs = wz1 + 43008 + w * 14336, gd = L_Z1B + 43008 + w * 14336;
      auto gel = [&](auto l0) AI {
        sfor<0, 7>([&](auto ji) AI {
          constexpr int j = ji.v;
          cp_run<2 * j, 2 * j + 2>(dws, l16, gs, gd);
          if constexpr (l0.v && j < 6) sfor<8 + 2 * j, 10 + 2 * j>(wsp_ld);
          SB();
          f32x4 v = gelu4((acc[0][j] - m4 * cs2[j]) * r4);
          if constexpr (j == 6) v *= cz < 6 ? 1.f : 0.f;
          z2[j] = v;
          if (j == 0) { zs = v; zq = v * v; } else { zs += v; zq += v * v; }
          pin(zs); pin(zq);
          SB();
        });
      };
      if (L == 0) gel(IC<1>{}); else gel(IC<0>{});
      const int sto = ST_OFF + L * ST_LAY + pz * 8192 + w * 2048;
      {
        const f32x2 t = rowsum8(zs, zq);
        st16(v4u{asu(t.x), GTAG, asu(t.y), GTAG}, rws, gso, sto + cz * 256);
      }
      STAMP(sb + 9);
      if (L == 0) sfor<20, 30>(wsp_ld);
      float ts, tq;
      {
        v4u ga, gb;
        for (unsigned spins = 0; spins < (1u << 18); ++spins) {
          asm volatile("" ::: "memory");
          ga = ld16<SC1>(rws, gro, sto); gb = ld16<SC1>(rws, gro + 1024, sto);
          if (__all(ga.y == GTAG && ga.w == GTAG && gb.y == GTAG && gb.w == GTAG)) break;
        }
        ts = asf(ga.x) + asf(gb.x); tq = asf(ga.z) + asf(gb.z);
        ts += dpp<0x128>(ts); tq += dpp<0x128>(tq); ts += dpp<0x124>(ts); tq += dpp<0x124>(tq);
      }
      STAMP(sb + 10);
      const int zo = act + A_Z + (cz * 64 + 2 * pz) * 7168;
      {
        f32x2 zz = finish_stat(ts, tq, 1.f / 864.f);
        zz.x = -zz.x * zz.y;
        const f32x4 mm = {dpp<0x00>(zz.x), dpp<0x55>(zz.x), dpp<0xAA>(zz.x), dpp<0xFF>(zz.x)};
        const f32x4 rr = {dpp<0x00>(zz.y), dpp<0x55>(zz.y), dpp<0xAA>(zz.y), dpp<0xFF>(zz.y)};
        const int so = zo + w2 * 7168 + kh * 512;
        v2u d[7];
#pragma unroll
        for (int j = 0; j < 7; j++) d[j] = pk4(z2[j] * rr + mm);
        st16(join16(d[0], d[1]), rws, zvo, so);
        st16(join16(d[2], d[3]), rws, zvo + 2048, so);
        st16(join16(d[4], d[5]), rws, zvo, so + 4096);
        st16(join16(d[6], d[6]), rws, seloob(zvo, M_GEVEN) + 2048, so + 4096);
      }
      cp_run<0, 8>(dws, l16, wz1 + 100352 + za0, L_Z1B + 100352 + za0);
      WAITV(8);
      BAR();
      flag(fl + F4 + cz * 256 + pz * 8);
      STAMP(sb + 11);
      touch(dws, seloob(tv, (1ull << 28) - 1), zo + w * 3584);
      cp_run<8, 11>(dws, l16, wz1 + 100352 + za0, L_Z1B + 100352 + za0);
      const int us = opq_s(WU_OFF + co * WU_T), ua = opq_s((w * 12800) & ~1023), ub = opq_s(51200 + w * 10240);
      f32x4 cs1[7];
      int tmp;
      xw += L_X3B - L_X3; xr += L_X3B - L_X3;
      v2u fz;
      const int fzo = opq_v(seloob(fl + F4 + cz * 256 + w * 64 + l * 8, 0xFFull));
      auto z1gemm = [&](auto l0) AI {
        constexpr bool L0 = l0.v;
        gemm_m2k2<10, 7, true, 0, false>([&](auto ti) AI { return ti.v < 3 ? b0 + ti.v * 14336 : ti.v < 7 ? b1 + (ti.v - 3) * 14336 : b2 + (ti.v - 7) * 14336; }, xw, A, acc,
            [&]() AI {}, [&](auto, auto) AI { return v4u{}; },
            [&](auto fi) AI {
          constexpr int f = fi.v;
          if constexpr (f == 22) { WAITV(L0 ? 22 : 0); BAR0(); }
          if constexpr (f == 43 || f == 57) BAR0();
          if constexpr (f >= 23 && f < 49 && (f - 23) % 8 == 0) cp_arm(ua + (f - 23) * 512);
          if constexpr (f >= 49 && f < 59 && (f - 49) % 4 == 0) cp_arm(ub + (f - 49) * 1024);
          if constexpr (f == 3) tmp = opq_v(L_CS1 + g * 16);
          if constexpr (f >= 5 && f < 19 && (f & 1)) cs1[(f - 5) / 2] = ldsf4(tmp + (f - 5) * 32);
          if constexpr (f == 63) fz = ld8<SC1>(rws, fzo, 0);
        },
            [&](auto fi) AI {
          constexpr int f = fi.v;
          if constexpr (L0 && f < 22) wsp_ld(IC<30 + f>{});
          if constexpr (f >= 24 && f < 49 && !(f & 1)) cp_one<(f - 24) / 2>(dws, l16, us + ua + (f - 24) / 8 * 4096);
          if constexpr (L0 && f >= 25 && f < 49 && (f & 1)) wsp_ld(IC<52 + (f - 25) / 2>{});
          if constexpr (f >= 49 && f < 59) cp_one<f - 49>(dws, l16, us + ub + (f - 49) / 4 * 4096);
        });
      };
      if (L == 0) z1gemm(IC<1>{}); else z1gemm(IC<0>{});
      STAMP(sb + 12);
      BAR();
      xchg_read<7>(xr, acc);
      if (!__all(l >= 8 || (fz.x == tag_lo && fz.y == tag_hi))) poll(rws, fzo, l >= 8, tag_lo, tag_hi);
      FENCE();
      STAMP(sb + 13);
      const int z0 = act + A_Z + (cz * 64 + 16 * w + (zr & 7) * 2) * 7168;
      auto ldZ0 = [&](auto fi) AI { Z[fi.v] = ld16<SC1>(rws, l16 + (fi.v & 3) * 1024, z0 + (fi.v >> 2) * 4096); };
      sfor<0, 7>([&](auto ji) AI {
        constexpr int j = ji.v;
        if constexpr (j < 4) sfor<3 * j, 3 * j + 3>(ldZ0);
        if constexpr (j == 4) sfor<12, 14>(ldZ0);
        SB();
        z1[j] = gelu4((acc[0][j] - m1 * cs1[j]) * r1);
        pin(z1[j]);
        SB();
      });
    }
    { LANE();
      const int ct0 = cz < 6 ? 7 * cz : 6 + 6 * cz, zr = MAPZ ? pz + 2 * cz + (cz >> 2) : 2 * pz + cz;
      STAMP(sb + 14);
      f32x4 acc[4][7];
      const int so = act + A_G + (4 * pz + w) * GT + ct0 * 512;
      f32x4 bias = blo(braw);
      pin(bias);
      int zp[8], za, rw[3], rd, vo; unsigned d[4];
      constexpr int RN = RZ + 1;
      v4u Y[RN];
      sfor<0, RZ>([&](auto fi) AI { Y[fi.v] = Z[fi.v]; });
      const int zb = opq_s(act + A_Z + (cz * 64 + 16 * w) * 7168);
      zp[1] = opq_s(zb + ((1 + zr) & 7) * 14336);
      NOP2();
      SB();
      sfor<0, 112>([&](auto fi) AI {
        constexpr int f = fi.v, s = sp_s(f), j = sp_j(f), fn = f + RZ;
        constexpr int q = f < 42 ? -1 : (f - 42) / 28, k = f < 42 ? 0 : (f - 42) % 28, buf = (q & 1) ? L_RED1 - L_RED0 : 0;
        auto fin = [&](auto ui) AI {
          constexpr int u = ui.v;
          if constexpr (q >= 0 && u >= 0 && u < 36) {
            constexpr int jj = 2 * q + (u < 32 ? (u >> 2) & 1 : (u - 32) >> 1), e = u & 3, st = u >> 3;
            if constexpr (st == 0) acc[0][jj][e] = gadd(acc[0][jj][e], acc[1][jj][e]);
            if constexpr (st == 1) acc[2][jj][e] = gadd(acc[2][jj][e], acc[3][jj][e]);
            if constexpr (st == 2) acc[0][jj][e] = gadd(acc[0][jj][e], acc[2][jj][e]);
            if constexpr (st == 3) acc[0][jj][e] = gmul(acc[0][jj][e], z1[jj][e]);
            if constexpr (st == 4) d[u - 32] = gcvt(acc[0][jj][(u & 1) * 2], acc[0][jj][(u & 1) * 2 + 1]);
          }
        };
        constexpr int per = q == 2 ? 7 : 4, u0 = (k - (q == 2 ? 9 : 10)) * per;
        const v4u z = Y[f % RN];
        mma<s == 0, true>(acc[1][j], z, W[1][s]);
        SB();
        if constexpr (fn < 112 && (fn >= 98 || !(fn & 1))) za = opq_s(zp[sp_s(fn) / 2] + ((sp_s(fn) & 1) * 7 + (sp_j(fn) & 6)) * 1024);
        if constexpr (q >= 0 && k >= 1 && k < 4) *lp<f32x4>(rw[k - 1] + buf) = acc[k][2 * q];
        if constexpr (q >= 0 && k >= 6 && k < 9) acc[k - 5][2 * q] = ldsf4(rd + buf + (k - 6) * 2048);
        fin(IC<u0>{});
        if constexpr (q == 2) fin(IC<u0 + 1>{});
        SB();
        mma<s == 0, true>(acc[2][j], z, W[2][s]);
        SB();
        if constexpr (fn < 112) Y[fn % RN] = ld16<SC1>(rws, l16 + (sp_j(fn) & 1) * 1024, za);
        if constexpr (q == 2) { fin(IC<u0 + 2>{}); fin(IC<u0 + 3>{}); }
        SB();
        mma<s == 0, true>(acc[3][j], z, W[3][s]);
        SB();
        if constexpr (q >= 0 && k >= 2 && k < 5) *lp<f32x4>(rw[k - 2] + buf + 1024) = acc[k - 1][2 * q + 1];
        if constexpr (q >= 0 && k >= 6 && k < 9) acc[k - 5][2 * q + 1] = ldsf4(rd + buf + (k - 6) * 2048 + 1024);
        fin(IC<u0 + per - 3>{}); fin(IC<u0 + per - 2>{});
        if constexpr (f % 4 == 1 && f / 4 + 2 < 8) zp[f / 4 + 2] = opq_s(zb + ((f / 4 + 2 + zr) & 7) * 14336);
        if constexpr (f >= 28 && f < 31) rw[f - 28] = opq_v(L_RED0 + ((((w + f - 27) & 3) * 3 + f - 28) * 2) * 1024 + l16);
        if constexpr (f == 31) rd = opq_v(L_RED0 + w * 6144 + l16);
        if constexpr (f == 32) vo = opq_v((g & 1) * 512 + c16 * 16);
        if constexpr (f == 34) vo = opq_v(vo + (g >> 1) * 256);
        if constexpr (q >= 0 && q < 2 && k == 20) { const v2u a = sw16(d[0], d[2]), e = sw16(d[1], d[3]); d[0] = a.x; d[2] = a.y; d[1] = e.x; d[3] = e.y; pin(d[0]); pin(d[1]); }
        if constexpr (q >= 0 && q < 2 && k == 21) st16(v4u{d[0], d[1], d[2], d[3]}, rws, vo + q * 1024, so);
        if constexpr (f == 38) BAR0();
        if constexpr (q >= 0 && k == 5) BAR();
        SB();
        if constexpr (s == 0) mma_c(acc[0][j], z, W[0][s], bias); else mma<false, true>(acc[0][j], z, W[0][s]);
        SB();
        fin(IC<u0 + per - 1>{});
        SB();
      });
      d[3] = gcvt(acc[0][5][2], acc[0][5][3]);
      const int rw2 = opq_v(L_RED2 + l16);
      asm volatile("s_nop 2" ::: "memory");
#pragma unroll
      for (int i = 1; i < 4; i++) pin(acc[i][6]);
      STAMP(sb + 15);
#pragma unroll
      for (int r = 1; r < 4; r++) *lp<f32x4>(rw2 + (((w + r) & 3) * 3 + r - 1) * 1024) = acc[r][6];
      st16(join16(v2u{d[0], d[1]}, v2u{d[2], d[3]}), rws, vo + 2048, so);
      BAR();
#pragma unroll
      for (int r = 1; r < 4; r++) acc[r][6] = ldsf4(rw2 + (w * 3 + r - 1) * 1024);
      pin(acc[0][6]);
      {
        const v2u dd = pk4(((acc[0][6] + acc[1][6]) + (acc[2][6] + acc[3][6])) * z1[6]);
        st16(join16(dd, dd), rws, seloob(vo, M_GEVEN) + 3072, cz < 6 ? so : OOB);
      }
      const int uc = 92160 + w * 11264;
      cp_run<0, 8>(dws, l16, WU_OFF + co * WU_T + uc, uc);
      WAITV(8);
      BAR();
      flag(fl + F5 + pz * 256 + cz * 8);
      STAMP(sb + 16);
      touch(dws, l < (cz < 6 ? 28 : 24) ? l * 128 : OOB, so);
      cp_run<8, 12>(dws, l16, WU_OFF + co * WU_T + uc, uc);
    }
    { LANE();
      f32x4 acc[2][5];
      v2u rr[5];
#pragma unroll
      for (int j = 0; j < 5; j++) rr[j] = ld8<SC1>(rws, (g >> 1) * 256 + c16 * 16 + (g & 1) * 8 + j * 512, act + A_X1 + (4 * po + w) * XT + co * 2560);
      const int vo = opq_v(L == 0 ? (g & 1) * 512 + (g >> 1) * 256 + c16 * 16 : c16 * 1280 + (g & 1) * 32 + (g >> 1) * 16);
      const int vo4 = opq_v(seloob(vo, M_GEVEN)), vs = opq_v(seloob(c16 * 8 + (g >> 1) * 4, M_GEVEN));
      const int xw = opq_v(L_X5 + (w ^ 1) * 5120 + l16), xr = opq_v(L_X5 + w * 5120 + l16);
      const int a0 = opq_s(act + A_G + (4 * po + w) * GT + kh * 14336), a1 = opq_s(act + A_G + (4 * po + (w ^ 1)) * GT + kh * 14336);
      const int k0 = opq_s(kh ? OOB : a0 + 12288), k1 = opq_s(kh ? OOB : a1 + 12288);
      const int b0 = opq_v(l16 + kh * 5120), b1 = opq_v(b0 + 61440), b2 = opq_v(b0 + 122880), b3 = opq_v(l16 + 133120);
      const int qo = opq_s(qslot_off(w)), wq = opq_s((WQ_OFF + h * WQ_T + qo) | kill1);
      const int tile = opq_s(ACT_OFF + ACT_LAY + A_XIN + (4 * po + w) * XT);
      const int pv = opq_v(seloob(fl + F5 + po * 256 + kh * 32 + l * 8, 0xFull));
      v4u A[2][14];
      gemm_m2k2<14, 5, true, 5, true>([&](auto ti) AI { return ti.v < 6 ? b0 + ti.v * 10240 : ti.v < 12 ? b1 + (ti.v - 6) * 10240 : ti.v == 12 ? b2 : b3; }, xw, A, acc,
          [&]() AI {
#pragma unroll
        for (int j = 0; j < 5; j++) { acc[0][j] = unpk4(rr[j]); pin(acc[0][j]); }
        poll(rws, pv, l >= 4, tag_lo, tag_hi);
        STAMP(sb + 17);
      },
          [&](auto ii, auto ti) AI { return ld16<SC1>(rws, l16 + (ti.v & 3) * 1024, ti.v == 13 ? (ii.v ? k1 : k0) : (ii.v ? a1 : a0) + (ti.v >> 2) * 4096); },
          [&](auto fi) AI {
        constexpr int f = fi.v;
        if constexpr (f == 21) BAR0();
        if constexpr (f == 53) { asm volatile("" ::"v"(A[0][13])); cp_arm(qo); }
        if constexpr (f == 61) cp_arm(qo + 4096);
        if constexpr (f == 66 || f == 68) cp_one<(f - 54) / 2>(dws, l16, wq + 4096);
      },
          [&](auto fi) AI {
        constexpr int f = fi.v;
        if constexpr (f >= 54 && !(f & 1)) cp_one<(f - 54) / 2>(dws, l16, wq + (f - 54) / 8 * 4096);
      });
      STAMP(sb + 18);
      BAR();
      xchg_read<5>(xr, acc);
      if (L == 1) {
        const rsrc_t ro_ = mk(out, 2048 * 1280);
        v2u d[5];
#pragma unroll
        for (int j = 0; j < 5; j++) d[j] = pk4(acc[0][j]);
        const int so = (64 * po + 16 * w) * 1280 + co * 160;
        __builtin_amdgcn_raw_buffer_store_b128(join16(d[0], d[1]), ro_, vo, so, 0);
        __builtin_amdgcn_raw_buffer_store_b128(join16(d[2], d[3]), ro_, vo + 64, so, 0);
        __builtin_amdgcn_raw_buffer_store_b128(join16(d[4], d[4]), ro_, vo4 + 128, so, 0);
        flag(b == 0 ? 0 : OOB);
        {
          unsigned m = 0;
          for (int i = 0; i < 4; i++) { const u64 t = __hip_atomic_load((GAS u64*)(ws + PROF_OFF) + i * 64 + l, __ATOMIC_RELAXED, __HIP_MEMORY_SCOPE_AGENT); const int d = (int)(t_start - t); m = d > (int)m ? d : m; }
          for (int o = 1; o < 64; o <<= 1) { const unsigned x = __shfl_xor(m, o); m = x > m ? x : m; }
          const u64 t0 = t_start - (unsigned)__builtin_amdgcn_readfirstlane(m & 0xfff);
          while (__builtin_amdgcn_s_memrealtime() - t0 < 10000ull) __builtin_amdgcn_s_sleep(2);
        }
        __builtin_amdgcn_endpgm();
      }
      store_x(acc[0], rws, tile + co * 2560, tile + 20480 + co * 128, vo, vo4, vs);
      cp_run<0, 8>(dws, l16, wq + 30720, qo + 30720);
      WAITV(8);
      BAR();
      flag(fl + F6 + po * 256 + co * 8);
      STAMP(sb + 19);
      touch(dws, l < 20 ? co * 2560 + l * 128 : l == 20 ? 20480 + co * 128 : OOB, tile);
      cp_run<0, 8>(dws, l16, wq + 2 * 30720, qo + 2 * 30720);
      cp_run<0, 8>(dws, l16, wq + 3 * 30720, qo + 3 * 30720);
    }
  }
}
}

extern "C" void kernel_launch(void* const* d_in, const int* in_sizes, int n_in, void* d_out, int out_size, void* d_ws, size_t ws_size, hipStream_t stream) {
  (void)hipFuncSetAttribute((const void*)fused_kernel, hipFuncAttributeMaxDynamicSharedMemorySize, LDS_BYTES);
  fused_kernel<<<256, 256, LDS_BYTES, stream>>>((const char*)d_in[0], (const char*)d_in[1], (const char*)d_in[2], (const char*)d_in[4],
                                                (const char*)d_in[5], (const char*)d_in[6], (const char*)d_in[7], (char*)d_out, (char*)d_ws);
}
```

```cpp
#define NTN 0
#include <hip/hip_runtime.h>
#include <hip/hip_bf16.h>
#include <stdint.h>
#include <utility>

#define AI __attribute__((always_inline))
#define LAS __attribute__((address_space(3)))
#define GAS __attribute__((address_space(1)))
typedef unsigned v6u __attribute__((ext_vector_type(6)));
typedef unsigned v4u __attribute__((ext_vector_type(4)));
typedef unsigned v2u __attribute__((ext_vector_type(2)));
typedef int v4i __attribute__((ext_vector_type(4)));
typedef float f32x4 __attribute__((ext_vector_type(4)));
typedef float f32x2 __attribute__((ext_vector_type(2)));
typedef __bf16 bf2 __attribute__((ext_vector_type(2)));
typedef short s4 __attribute__((ext_vector_type(4)));
typedef unsigned long long u64;
typedef __amdgpu_buffer_rsrc_t rsrc_t;

#define PROF 0

namespace {
constexpr int SC1 = 16;
#define FS 128
constexpr int FARR = 256 * FS;
constexpr int F0R = 4096, F0B = F0R + FARR, FLAY = F0B + FARR;
constexpr int F1 = 0, F2 = FARR, F5 = 2 * FARR, F6 = 3 * FARR, F4 = 5 * FARR, FLAY_SZ = 6 * FARR;
constexpr int ST_OFF = 1 << 20;
constexpr int ST_LAY = 32 * 8192;
constexpr int CSQ_OFF = ST_OFF + 2 * ST_LAY;
constexpr int CSI_OFF = CSQ_OFF + 16 * 16 * 512;
constexpr int WQ_OFF = 2 << 20, WQ_T = 153600;
constexpr int WO_OFF = WQ_OFF + 16 * WQ_T, WO_T = 102400;
constexpr int WI_OFF = WO_OFF + 8 * WO_T, WI_T = 143360;
constexpr int WU_OFF = WI_OFF + 16 * WI_T, WU_T = 138240;
constexpr int ACT_OFF = WU_OFF + 8 * WU_T;
constexpr int XT = 21504, OT = 20480, GT = 27648;
constexpr int XSZ = 128 * XT, OSZ = 128 * OT, ZSZ = 8 * 64 * 7168, GSZ = 128 * GT;
constexpr int A_XIN = 0, A_O = A_XIN + XSZ, A_X1 = A_O + OSZ, A_Z = A_X1 + XSZ, A_G = A_Z + ZSZ, ACT_LAY = A_G + GSZ;
constexpr int PROF_OFF = ACT_OFF + 2 * ACT_LAY;
constexpr int WS_USED = PROF_OFF + 256 * 256 * 4;
static_assert(FLAY + 2 * FLAY_SZ <= ST_OFF, "");
static_assert(CSI_OFF + 16 * 16 * 512 <= WQ_OFF, "");
constexpr int OOB = 0x40000000;
constexpr int LDS_BYTES = 163840;
constexpr int L_TOP = 159744;
constexpr int L_CSQ = L_TOP, L_CS2 = L_CSQ + 512, L_CS1 = L_CS2 + 512, L_XST3 = L_CS1 + 512, L_JUNK = L_XST3 + 512, L_PROF = L_JUNK + 256;
constexpr int L_XST1 = 153600, L_HST = L_XST1 + 2048;
constexpr int L_HALO = 0, L_NB = 24576;
constexpr int L_WO = 57344, L_X2 = L_WO;
constexpr int L_Z2B = 73728 - 4 * 14336;
constexpr int L_Z1B = 59392 - 3 * 14336;
constexpr int L_X3 = 43008, L_X3B = 73728, L_X5 = 139264;
constexpr int L_RED0 = 98304, L_RED1 = L_RED0 + 24576, L_RED2 = L_RED1 + 24576;
static_assert(L_RED2 >= 138240 && L_RED2 + 12288 <= L_TOP, "");
#define MAPQ 1
#define MAPO 1
#define MAPZ 1
constexpr int SHQ = MAPQ ? 4 : 2, SHO = MAPO ? 8 : 4, SHZ = MAPZ ? 8 : 4;
#define LOCF 1
#define WPRE 8
#define PSLP 0
#define PDLY 0
#define FTCH 1
#define PAUX 16
#define FAUX 16
#define NCHK 5
#define GLD 24
#define PUBF 62
#define ZPAD 1
#define WHIT 0
#define TCH 0
#define WINA 3
constexpr int CODE_BYTES = 41984;
constexpr unsigned GTAG = 0x5A5A5A5Bu;
constexpr unsigned ONE2 = 0x3f803f80u;

template <int I> struct IC { static constexpr int v = I; };
template <int B, class F, int... I> __device__ __forceinline__ void sfor_(F& f, std::integer_sequence<int, I...>) { (f(IC<B + I>{}), ...); }
template <int I, int N, class F> __device__ __forceinline__ void sfor(F&& f) {
  if constexpr (I < N) sfor_<I>(f, std::make_integer_sequence<int, N - I>{});
}
__device__ __forceinline__ int opq_s(int x) { asm volatile("" : "+s"(x)); return x; }
__device__ __forceinline__ int opq_u(int x) { return opq_s(__builtin_amdgcn_readfirstlane(x)); }
__device__ __forceinline__ int opq_v(int x) { asm volatile("" : "+v"(x)); return x; }
template <class T> __device__ __forceinline__ void keep(const T& c) { asm volatile("" ::"v"(c)); }
template <class T> __device__ __forceinline__ void pin(T& c) { asm volatile("" : "+v"(c)); }

template <bool FIRST> __device__ __forceinline__ void mma_aa(f32x4& c, v4u a, v4u b) {
  if constexpr (FIRST) asm volatile("v_mfma_f32_16x16x32_bf16 %0, %1, %2, 0" : "=&v"(c) : "a"(a), "v"(b));
  else asm volatile("v_mfma_f32_16x16x32_bf16 %0, %1, %2, %0" : "+v"(c) : "a"(a), "v"(b));
}
template <bool FIRST, bool BA> __device__ __forceinline__ void mma(f32x4& c, v4u a, v4u b) {
  if constexpr (FIRST) {
    if constexpr (BA) asm volatile("v_mfma_f32_16x16x32_bf16 %0, %1, %2, 0" : "=&v"(c) : "v"(a), "a"(b));
    else asm volatile("v_mfma_f32_16x16x32_bf16 %0, %1, %2, 0" : "=&v"(c) : "v"(a), "v"(b));
  } else {
    if constexpr (BA) asm volatile("v_mfma_f32_16x16x32_bf16 %0, %1, %2, %0" : "+v"(c) : "v"(a), "a"(b));
    else asm volatile("v_mfma_f32_16x16x32_bf16 %0, %1, %2, %0" : "+v"(c) : "v"(a), "v"(b));
  }
}
__device__ __forceinline__ void mma_c(f32x4& d, v4u a, v4u b, f32x4 c) { asm volatile("v_mfma_f32_16x16x32_bf16 %0, %1, %2, %3" : "=&v"(d) : "v"(a), "a"(b), "v"(c)); }
#define NOP2() asm volatile("s_nop 1")
__device__ __forceinline__ void mma_settle() { asm volatile("s_nop 7" ::: "memory"); }
__device__ __forceinline__ float dot2(unsigned a, unsigned b, float c) {
  return __builtin_amdgcn_fdot2_f32_bf16(__builtin_bit_cast(bf2, a), __builtin_bit_cast(bf2, b), c, false);
}
__device__ __forceinline__ unsigned pk(float lo, float hi) {
  f32x2 v = {lo, hi};
  return __builtin_bit_cast(unsigned, __builtin_convertvector(v, bf2));
}
__device__ __forceinline__ v2u pk4(f32x4 v) { return v2u{pk(v[0], v[1]), pk(v[2], v[3])}; }
__device__ __forceinline__ float blo(unsigned u) { return __builtin_bit_cast(float, u << 16); }
__device__ __forceinline__ float bhi(unsigned u) { return __builtin_bit_cast(float, u & 0xffff0000u); }
__device__ __forceinline__ f32x4 unpk4(v2u r) { return f32x4{blo(r.x), bhi(r.x), blo(r.y), bhi(r.y)}; }
__device__ __forceinline__ float asf(unsigned u) { return __builtin_bit_cast(float, u); }
__device__ __forceinline__ unsigned asu(float f) { return __builtin_bit_cast(unsigned, f); }
__device__ __forceinline__ unsigned sel(unsigned a, unsigned b, u64 m) { unsigned r; asm("v_cndmask_b32_e64 %0, %1, %2, %3" : "=v"(r) : "v"(a), "v"(b), "s"(m)); return r; }
__device__ __forceinline__ float self(float a, float b, u64 m) { return asf(sel(asu(a), asu(b), m)); }
__device__ __forceinline__ unsigned sel0(unsigned b, u64 m) { unsigned r; asm("v_cndmask_b32_e64 %0, 0, %1, %2" : "=v"(r) : "v"(b), "s"(m)); return r; }
__device__ __forceinline__ int seloob(int b, u64 m) { int r; asm("v_cndmask_b32_e64 %0, 2.0, %1, %2" : "=v"(r) : "v"(b), "s"(m)); return r; }
__device__ __forceinline__ float vmax(float a, float b) { return __builtin_elementwise_maximum(a, b); }
__device__ __forceinline__ float gadd(float a, float b) { float r; asm volatile("v_add_f32 %0, %1, %2" : "=v"(r) : "v"(a), "v"(b)); return r; }
__device__ __forceinline__ float gmul(float a, float b) { float r; asm volatile("v_mul_f32 %0, %1, %2" : "=v"(r) : "v"(a), "v"(b)); return r; }
__device__ __forceinline__ unsigned gcvt(float a, float b) { unsigned r; asm volatile("v_cvt_pk_bf16_f32 %0, %1, %2" : "=v"(r) : "v"(a), "v"(b)); return r; }
template <int C> __device__ __forceinline__ int gaddi(int a) { int r; asm volatile("v_add_u32 %0, %1, %2" : "=v"(r) : "n"(C), "v"(a)); return r; }
constexpr u64 M_ODD = 0xAAAAAAAAAAAAAAAAull, M_G0 = 0xFFFFull, M_G1 = 0xFFFF0000ull, M_GEVEN = 0x0000FFFF0000FFFFull, M_LO = 0xFFFFFFFFull;
constexpr u64 le_mask(int add) { u64 m = 0; for (int l = 0; l < 64; l++) if (4 * (l >> 4) + (l & 1) + add <= (l & 15)) m |= 1ull << l; return m; }
__device__ __forceinline__ rsrc_t mk(const void* p, int bytes) { return __builtin_amdgcn_make_buffer_rsrc((void*)p, 0, bytes, 0x00020000); }
__device__ __forceinline__ v4i mkd(const void* p, int bytes) {
  u64 a = (u64)p; v4i r; r.x = (int)(unsigned)a; r.y = (int)((unsigned)(a >> 32) & 0xffffu); r.z = bytes; r.w = 0x00020000; return r;
}
template <int AUX> __device__ __forceinline__ v4u ld16(rsrc_t r, int v, int s) { return __builtin_amdgcn_raw_buffer_load_b128(r, v, s, AUX); }
template <int AUX> __device__ __forceinline__ v2u ld8(rsrc_t r, int v, int s) { return __builtin_amdgcn_raw_buffer_load_b64(r, v, s, AUX); }
__device__ __forceinline__ void st16(v4u d, rsrc_t r, int v, int s) { __builtin_amdgcn_raw_buffer_store_b128(d, r, v, s, SC1); }
#define BAR() asm volatile("s_waitcnt lgkmcnt(0)\n\ts_barrier" ::: "memory")
#define BAR0() asm volatile("s_barrier" ::: "memory")
#define DRAIN() asm volatile("s_waitcnt vmcnt(0)" ::: "memory")
#define WAITV(n) asm volatile("s_waitcnt vmcnt(%0)" ::"n"(n) : "memory")
#define SB() __builtin_amdgcn_sched_barrier(0)
#define FENCE() do { asm volatile("" ::: "memory"); __builtin_amdgcn_sched_barrier(0); } while (0)
#define GL(o) "\n\tbuffer_load_dwordx4 %1, %2, %3 offen offset:" #o " sc1 lds"
#define GLARGS ::"s"(lds), "v"(voff), "s"(rs), "s"(soff)
#define NTW " nt"
#define NTX ""
__device__ __forceinline__ void glds_in(v4i rs, int voff, int soff, int lds) {
  asm volatile("s_mov_b32 m0, %0\n\ts_nop 0\n\tbuffer_load_dwordx4 %1, %2, %3 offen" NTW " lds" GLARGS : "memory");
}
#define NTS ""
__device__ __forceinline__ void glds_w(v4i rs, int voff, int soff, int lds) {
  asm volatile("s_mov_b32 m0, %0\n\ts_nop 0\n\tbuffer_load_dwordx4 %1, %2, %3 offen" NTS " lds" GLARGS : "memory");
}
__device__ __forceinline__ void touch_in(v4i rs, int voff, int soff, int lds) {
  asm volatile("s_mov_b32 m0, %0\n\ts_nop 0\n\tbuffer_load_dword %1, %2, %3 offen" NTX " lds" GLARGS : "memory");
}
template <bool FIRST> __device__ __forceinline__ void mma_gram(f32x4& c, v4u a) {
  if constexpr (FIRST) asm volatile("v_mfma_f32_16x16x32_bf16 %0, %1, %1, 0" : "=&v"(c) : "a"(a));
  else asm volatile("v_mfma_f32_16x16x32_bf16 %0, %1, %1, %0" : "+v"(c) : "a"(a));
}
template <int N> __device__ __forceinline__ void gldsn(v4i rs, int voff, int soff, int lds) {
  static_assert(N >= 1 && N <= 4, "");
  if constexpr (N == 1) asm volatile("s_mov_b32 m0, %0\n\ts_nop 0" GL(0) GLARGS : "memory");
  if constexpr (N == 2) asm volatile("s_mov_b32 m0, %0\n\ts_nop 0" GL(0) GL(1024) GLARGS : "memory");
  if constexpr (N == 3) asm volatile("s_mov_b32 m0, %0\n\ts_nop 0" GL(0) GL(1024) GL(2048) GLARGS : "memory");
  if constexpr (N == 4) asm volatile("s_mov_b32 m0, %0\n\ts_nop 0" GL(0) GL(1024) GL(2048) GL(3072) GLARGS : "memory");
}
template <int N0, int N1> __device__ __forceinline__ void cp_run(v4i rs, int voff, int src, int dst) {
  if constexpr (N0 < N1) {
    constexpr int n = N1 - N0 < 4 ? N1 - N0 : 4;
    gldsn<n>(rs, voff, src + N0 * 1024, dst + N0 * 1024);
    cp_run<N0 + n, N1>(rs, voff, src, dst);
  }
}
__device__ __forceinline__ void cp_arm(int lds) { asm volatile("s_mov_b32 m0, %0" ::"s"(lds) : "memory"); }
template <int U> __device__ __forceinline__ void cp_one(v4i rs, int voff, int soff) {
  asm volatile("buffer_load_dwordx4 %0, %1, %2 offen offset:%3 sc1 lds" ::"v"(voff), "s"(rs), "s"(soff), "n"((U & 3) * 1024) : "memory");
}
__device__ __forceinline__ void touch(v4i rs, int voff, int soff) {
  asm volatile("s_mov_b32 m0, %0\n\ts_nop 0\n\tbuffer_load_dword %1, %2, %3 offen sc1 lds" ::"s"(L_JUNK), "v"(voff), "s"(rs), "s"(soff) : "memory");
}
template <int OFF, bool WS> __device__ __forceinline__ void lda16(v4u& d, v4i rs, int voff, int soff) {
  if constexpr (WS) asm volatile("buffer_load_dwordx4 %0, %1, %2, %3 offen offset:%4 sc1" : "+a"(d) : "v"(voff), "s"(rs), "s"(soff), "n"(OFF) : "memory");
  else asm volatile("buffer_load_dwordx4 %0, %1, %2, %3 offen offset:%4" : "+a"(d) : "v"(voff), "s"(rs), "s"(soff), "n"(OFF) : "memory");
}
template <int OFF> __device__ __forceinline__ void sta16(const v4u& d, v4i rs, int voff, int soff) {
  asm volatile("buffer_store_dwordx4 %0, %1, %2, %3 offen offset:%4 sc1" ::"a"(d), "v"(voff), "s"(rs), "s"(soff), "n"(OFF) : "memory");
}
template <int OFF> __device__ __forceinline__ void ldsa16(v4u& d, int addr) { asm volatile("ds_read_b128 %0, %1 offset:%2" : "+a"(d) : "v"(addr), "n"(OFF) : "memory"); }
__device__ __forceinline__ void park(v4u& d, v4u v) { asm volatile("" : "=a"(d) : "0"(v)); }
__device__ __forceinline__ void tch(v4i rs, int voff, int soff) {
  asm volatile("buffer_load_dword %0, %1, %2 offen sc1 lds" ::"v"(voff), "s"(rs), "s"(soff) : "memory");
}
template <int OFF> __device__ __forceinline__ v4u ld16h(v4i rs, int voff, int soff) {
  v4u r; asm volatile("buffer_load_dwordx4 %0, %1, %2, %3 offen offset:%4 sc1" : "=v"(r) : "v"(voff), "s"(rs), "s"(soff), "n"(OFF) : "memory"); return r;
}
template <int OFF> __device__ __forceinline__ v2u ld8h(v4i rs, int voff, int soff) {
  v2u r; asm volatile("buffer_load_dwordx2 %0, %1, %2, %3 offen offset:%4 sc1" : "=v"(r) : "v"(voff), "s"(rs), "s"(soff), "n"(OFF) : "memory"); return r;
}
template <int OFF> __device__ __forceinline__ float ld4h(v4i rs, int voff, int soff) {
  float r; asm volatile("buffer_load_dword %0, %1, %2, %3 offen offset:%4 sc1" : "=v"(r) : "v"(voff), "s"(rs), "s"(soff), "n"(OFF) : "memory"); return r;
}
__device__ __forceinline__ int qslot_off(int w) { return (w * 7680) & ~1023; }
template <class T> __device__ __forceinline__ LAS T* lp(int off) { return (LAS T*)(size_t)(unsigned)off; }
__device__ __forceinline__ v4u lds16(int off) { return *lp<v4u>(off); }
__device__ __forceinline__ v2u lds8(int off) { return *lp<v2u>(off); }
__device__ __forceinline__ f32x4 ldsf4(int off) { return *lp<f32x4>(off); }
__device__ __forceinline__ float ldsf(int off) { return *lp<float>(off); }
template <int DLY = 0> __device__ __forceinline__ void poll(rsrc_t rws, int voff, bool off, unsigned lo, unsigned hi) {
  if (DLY) __builtin_amdgcn_s_sleep(DLY);
  for (unsigned spins = 0; spins < (1u << 18); ++spins) {
    asm volatile("" ::: "memory");
    const v2u v = ld8<PAUX>(rws, voff, 0);
    if (__all(off || (v.x == lo && v.y == hi))) break;
    if (PSLP) __builtin_amdgcn_s_sleep(PSLP);
  }
  asm volatile("" ::: "memory");
}
__device__ __forceinline__ v2u sw16(unsigned a, unsigned b) { auto r = __builtin_amdgcn_permlane16_swap(a, b, false, false); return v2u{r[0], r[1]}; }
__device__ __forceinline__ v2u sw32(unsigned a, unsigned b) { auto r = __builtin_amdgcn_permlane32_swap(a, b, false, false); return v2u{r[0], r[1]}; }
__device__ __forceinline__ v4u join16(v2u x, v2u y) { const v2u a = sw16(x.x, y.x), b = sw16(x.y, y.y); return v4u{a.x, b.x, a.y, b.y}; }
template <int CTRL> __device__ __forceinline__ float dpp(float v) {
  return __builtin_bit_cast(float, __builtin_amdgcn_update_dpp(0, __builtin_bit_cast(int, v), CTRL, 0xf, 0xf, true));
}
__device__ __forceinline__ float xsum2(float s, float q) {
  const v2u a = sw32(asu(s), asu(q)); const float t = asf(a.x) + asf(a.y);
  const v2u b = sw16(asu(t), asu(t)); return asf(b.x) + asf(b.y);
}
__device__ __forceinline__ f32x2 finish_stat(float s, float q, float inv_n) {
  const float m = s * inv_n;
  return f32x2{m, __builtin_amdgcn_rsqf(fmaxf(q * inv_n - m * m, 0.f) + 1e-5f)};
}
__device__ __forceinline__ f32x4 gelu4(f32x4 v) {
  const float k1 = -2.0f * 0.7978845608028654f * 1.4426950408889634f, k2 = k1 * 0.044715f;
  f32x4 e = v * ((v * v) * k2 + k1);
  asm("" : "+v"(e));
#pragma unroll
  for (int i = 0; i < 4; i++) e[i] = __builtin_amdgcn_exp2f(e[i]);
  e += 1.0f;
#pragma unroll
  for (int i = 0; i < 4; i++) e[i] = __builtin_amdgcn_rcpf(e[i]);
  return v * e;
}
__device__ __forceinline__ f32x2 rowsum8(f32x4 s, f32x4 q) {
  float a, b, c, d;
  asm volatile("s_nop 1\n\t"
      "v_add_f32_dpp %0, %4, %4 row_ror:8 row_mask:0xf bank_mask:0x3\n\t"
      "v_add_f32_dpp %1, %5, %5 row_ror:8 row_mask:0xf bank_mask:0x3\n\t"
      "v_add_f32_dpp %2, %8, %8 row_ror:8 row_mask:0xf bank_mask:0x3\n\t"
      "v_add_f32_dpp %3, %9, %9 row_ror:8 row_mask:0xf bank_mask:0x3\n\t"
      "v_add_f32_dpp %0, %6, %6 row_ror:8 row_mask:0xf bank_mask:0xc\n\t"
      "v_add_f32_dpp %1, %7, %7 row_ror:8 row_mask:0xf bank_mask:0xc\n\t"
      "v_add_f32_dpp %2, %10, %10 row_ror:8 row_mask:0xf bank_mask:0xc\n\t"
      "v_add_f32_dpp %3, %11, %11 row_ror:8 row_mask:0xf bank_mask:0xc\n\t"
      "v_add_f32_dpp %0, %0, %0 row_half_mirror row_mask:0xf bank_mask:0x5\n\t"
      "v_add_f32_dpp %0, %1, %1 row_half_mirror row_mask:0xf bank_mask:0xa\n\t"
      "v_add_f32_dpp %2, %2, %2 row_half_mirror row_mask:0xf bank_mask:0x5\n\t"
      "v_add_f32_dpp %2, %3, %3 row_half_mirror row_mask:0xf bank_mask:0xa\n\t"
      "v_add_f32_dpp %0, %0, %0 quad_perm:[1,0,3,2] row_mask:0xf bank_mask:0xf\n\t"
      "s_nop 0\n\t"
      "v_add_f32_dpp %2, %2, %2 quad_perm:[1,0,3,2] row_mask:0xf bank_mask:0xf\n\t"
      "v_add_f32_dpp %0, %0, %0 quad_perm:[2,3,0,1] row_mask:0xf bank_mask:0xf\n\t"
      "s_nop 0\n\t"
      "v_add_f32_dpp %2, %2, %2 quad_perm:[2,3,0,1] row_mask:0xf bank_mask:0xf"
      : "=&v"(a), "=&v"(b), "=&v"(c), "=&v"(d)
      : "v"(s[0]), "v"(s[1]), "v"(s[2]), "v"(s[3]), "v"(q[0]), "v"(q[1]), "v"(q[2]), "v"(q[3]));
  return f32x2{a, c};
}
__device__ __forceinline__ v4u bperm4(int idx, v4u v) {
  v4u r;
  r.x = __builtin_amdgcn_ds_bpermute(idx, v.x); r.y = __builtin_amdgcn_ds_bpermute(idx, v.y);
  r.z = __builtin_amdgcn_ds_bpermute(idx, v.z); r.w = __builtin_amdgcn_ds_bpermute(idx, v.w);
  return r;
}
__device__ __forceinline__ v2u trd(int off) { return __builtin_bit_cast(v2u, __builtin_amdgcn_ds_read_tr16_b64_v4i16(lp<s4>(off))); }
#define STAMP(i) do {} while (0)
#define CSTAMP(i) do {} while (0)
#define LANE() const int l = opq_v((int)threadIdx.x) & 63, l16 = l * 16, g = l >> 4, c16 = l & 15; (void)g; (void)c16; (void)l16; \
  const int w = opq_s(w_), po = opq_s(po_), co = opq_s(co_), pz = opq_s(pz_), cz = opq_s(cz_), P = opq_s(P_), h = opq_s(h_); \
  (void)w; (void)po; (void)co; (void)pz; (void)cz; (void)P; (void)h; \
  const int kh = w & 1, w2 = w >> 1; (void)kh; (void)w2; \
  auto flag = [&](int off) AI { __builtin_amdgcn_raw_buffer_store_b64(v2u{tag_lo, tag_hi}, rws, seloob(0, 1ull), w == 0 ? off : OOB, FAUX); }; (void)flag; \
  auto push = [&](int voff, int off) AI { __builtin_amdgcn_raw_buffer_store_b64(v2u{tag_lo, tag_hi}, rws, voff, w == 0 ? off : OOB, SC1); }; (void)push;
#define QROT (2 * cz)
#define KQ ((w + QROT) & 3)
#define RROT 3
__device__ __forceinline__ int pan(int e, int pz, int kq) {
  int r = e - 2 + RROT * (pz & 1); r = r >= 6 ? r - 6 : r;
  const int u = e < 2 ? e : r + 2;
  return (((pz >> 3) + (u >> 1)) & 3) * 8 + 2 * kq + (u & 1);
}

#define DIAG 0
__device__ __forceinline__ u64 tick() { u64 t; asm volatile("s_memtime %0" : "=s"(t)); return t; }
#define DUMP() do { asm volatile("s_waitcnt lgkmcnt(0)" ::: "memory"); _Pragma("unroll") for (int i = 0; i < 12; i++) { asm volatile("" : "+s"(dg[i])); *lp<unsigned>(L_PROF + w * 256 + (50 + i) * 4) = (unsigned)dg[i]; } } while (0)
template <int NS, int NCT, int WIN, int (*NV)(int)> constexpr int younger(int t, int fe) {
  const int n = 2 * t + 1, f0 = n < 2 * WIN ? -1 : (n - 2 * WIN) * 2;
  int c = n < 2 * WIN ? 2 * WIN - 1 - n : 0;
  for (int f = f0 + 1; f < fe; f++) c += NV(f) + (f % 2 == 0 && 2 * WIN + f / 2 < 2 * NS);
  return c;
}
template <int NS, int NCT, int TL, bool TR, int WIN, bool INIT, int (*NV)(int), class BA, class GO, class LD, class HA, class HB>
__device__ __forceinline__ void gemm_m2k2(BA&& badr, int xw, v4u (&A)[2][NS], f32x4 (&acc)[2][NCT], GO&& go, LD&& lda, HA&& ha, HB&& hb) {
  constexpr int NF = NS * NCT, NT = TL * NCT, NL = NF - NT;
  static_assert(NF % 2 == 0 && (WIN == 0 || 4 * (NS - WIN) <= NL + 1), "");
  v4u B[NF];
  auto ldB = [&](auto fi) AI { constexpr int f = fi.v; if constexpr (f < NF) B[f] = lds16(badr(IC<f / NCT>{}) + (f % NCT) * 1024); };
  auto mm = [&](auto ii, auto fi) AI {
    constexpr int i = ii.v, f = fi.v, t = f / NCT, ct = f % NCT;
    if (TR) mma<t == 0 && !(INIT && i == 0), false>(acc[i][ct], B[f], A[i][t]); else mma<t == 0 && !(INIT && i == 0), false>(acc[i][ct], A[i][t], B[f]);
    SB();
  };
  sfor<0, 6>([&](auto fi) AI { ldB(IC<(fi.v ^ 1)>{}); SB(); });
  go();
  if constexpr (WIN > 0) sfor<0, WIN>([&](auto ti) AI { A[1][ti.v] = lda(IC<1>{}, ti); A[0][ti.v] = lda(IC<0>{}, ti); });
  SB();
  sfor<0, NL>([&](auto fi) AI {
    constexpr int f = fi.v;
    if constexpr (WIN > 0 && f % NCT == 0) WAITV((younger<NS, NCT, WIN, NV>(f / NCT, f)));
    mm(IC<0>{}, fi);
    ldB(IC<((f + 6) ^ 1)>{});
    ha(fi);
    SB();
    mm(IC<1>{}, fi);
    if constexpr (WIN > 0) {
      constexpr int n = 2 * WIN + f / 2;
      if constexpr (f % 2 == 0 && n < 2 * NS) A[(n & 1) ^ 1][n >> 1] = lda(IC<((n & 1) ^ 1)>{}, IC<(n >> 1)>{});
    }
    if constexpr (f >= NL - 4) ldB(IC<((f + 10) ^ 1)>{});
    hb(fi);
    SB();
  });
  sfor<NL, NF>([&](auto fi) AI {
    constexpr int f = fi.v;
    if constexpr (WIN > 0 && f % NCT == 0) WAITV((younger<NS, NCT, WIN, NV>(f / NCT, NL)));
    mm(IC<1>{}, fi);
    ldB(IC<((f + 10) ^ 1)>{});
    SB();
  });
  sfor<NL, NF>([&](auto fi) AI {
    constexpr int k = fi.v - NL, D = NCT == 5 ? 3 : 1, SP = TL == 1 ? 1 : 2;
    mm(IC<0>{}, fi);
    if constexpr (k >= D && (k - D) % SP == 0 && (k - D) / SP < NCT) { pin(acc[1][(k - D) / SP]); *lp<f32x4>(xw + (k - D) / SP * 1024) = acc[1][(k - D) / SP]; }
    else ha(fi);
    if constexpr (k == NT - 1) sfor<(NT - 1 - D) / SP + 1, NCT>([&](auto ki) AI { pin(acc[1][ki.v]); *lp<f32x4>(xw + ki.v * 1024) = acc[1][ki.v]; });
    SB();
  });
}
template <int NCT> __device__ __forceinline__ void xchg_read(int src, f32x4 (&acc)[2][NCT]) {
#pragma unroll
  for (int j = 0; j < NCT; j++) acc[1][j] = ldsf4(src + j * 1024);
  SB();
#pragma unroll
  for (int j = 0; j < NCT; j++) { pin(acc[0][j]); acc[0][j] += acc[1][j]; }
}
__device__ __forceinline__ void store_x(const f32x4 (&x)[5], rsrc_t rws, int so, int ss, int vo, int vo4, int vs) {
  v2u d[5];
#pragma unroll
  for (int j = 0; j < 5; j++) d[j] = pk4(x[j]);
  float s0 = 0.f, q0 = 0.f;
#pragma unroll
  for (int j = 0; j < 5; j++) { s0 = dot2(d[j].x, ONE2, s0); q0 = dot2(d[j].x, d[j].x, q0); s0 = dot2(d[j].y, ONE2, s0); q0 = dot2(d[j].y, d[j].y, q0); }
  st16(join16(d[0], d[1]), rws, vo, so);
  st16(join16(d[2], d[3]), rws, vo + 1024, so);
  st16(join16(d[4], d[4]), rws, vo4 + 2048, so);
  __builtin_amdgcn_raw_buffer_store_b32(asu(xsum2(s0, q0)), rws, vs, ss, SC1);
}
constexpr int nv_none(int) { return 0; }
constexpr int nv_o(int f) { return (f >= 17 && f < 25 && (f & 1)) + (f >= 32 && f < 35); }
constexpr int nv_z(int f) { return (f == 1 || f == 3) + (f >= 32 && f % 3 == 2); }
constexpr int nv_u(int f) { return f >= 44 && !(f & 1); }
#define QPRE 7
#define QWSP 2
#define QAHEAD 6
constexpr int q_step(int n) { return n % 9 == 0 ? n / 9 * 4 : n / 9 * 4 + (n % 9 - 1) / 2; }
constexpr int q_si(int f) { return f >= 57 && f < 89 && f % 4 == 1 ? (f - 57) / 4 : -1; }
struct QTab { int ld[160], wu[160], ou[160], cum[161], wt[20], ws[3]; };
constexpr QTab q_tab() {
  QTab t{}; int n = QPRE, u = 0, o = 0, lastw[3] = {0, 0, 0};
  for (int i = 0; i < 160; i++) {
    t.ld[i] = t.wu[i] = t.ou[i] = -1;
    if (i % 2 == 0) {
      const int da = n < 45 ? 8 * q_step(n) : 1000, dw = u < 24 ? 58 + u / 8 * 32 - 2 * (7 - u % 8) * QWSP : 1000;
      if (da <= dw && n < 45 && q_step(n) <= i / 8 + QAHEAD) t.ld[i] = n++;
      else if (u < 24) { lastw[u / 8] = i; t.wu[i] = u++; }
      else if (i >= 124 && o < 8 && i % 4 == 0) t.ou[i] = o++;
    }
    t.cum[i + 1] = t.cum[i] + (t.ld[i] >= 0) + (t.wu[i] >= 0) + (t.ou[i] >= 0) + (q_si(i) >= 0) + (i >= 110 && i < 130 && i % 4 == 3) + (i == 45 || i == 49 || i == 53);
  }
  for (int s = 0; s < 20; s++) {
    const int last = s / 4 * 9 + 2 + (s & 3) * 2;
    int f0 = -1;
    for (int i = 0; i < 160; i++) if (t.ld[i] == last) f0 = i;
    t.wt[s] = (last < QPRE ? QPRE - 1 - last : 0) + t.cum[8 * s] - t.cum[f0 + 1];
  }
  for (int k = 0; k < 3; k++) t.ws[k] = t.cum[58 + 32 * k] - t.cum[lastw[k] + 1];
  return t;
}
constexpr QTab QT = q_tab();
constexpr int s_vm(int f) { return (f == 73 || f == 97) + (f == 4) + (f >= 40 && f < 50 && !(f & 1)) + (f >= 98 && f < 108); }
constexpr int s_wait(int f) { int c = f < 14 ? 13 - f : 0; for (int i = f < 14 ? 0 : f - 13; i < f; i++) c += (i < 98) + s_vm(i); return c; }
constexpr int sp_s(int f) { return f < 28 ? f / 7 : f < 100 ? 4 + ((f - 28) % 24) / 2 : 4 + (f - 100); }
constexpr int sp_j(int f) { return f < 28 ? f % 7 : f < 100 ? (f - 28) / 24 * 2 + (f & 1) : 6; }

__global__ __launch_bounds__(256, 1) void fused_kernel(const char* __restrict__ x0, const char* __restrict__ wqkv, const char* __restrict__ wo,
                                                       const char* __restrict__ win, const char* __restrict__ wsp, const char* __restrict__ bsp,
                                                       const char* __restrict__ wout, char* out, char* ws) {
  {
    u64 pc; asm volatile("s_getpc_b64 %0" : "=s"(pc));
    const int i = (blockIdx.x >> 3) * 16 + (threadIdx.x >> 4);
    touch_in(mkd((const void*)(pc & ~127ull), CODE_BYTES), (threadIdx.x & 15) == 0 ? i * 128 : OOB, 0, L_JUNK);
  }
  const int w_ = __builtin_amdgcn_readfirstlane(threadIdx.x >> 6);
  const int b = blockIdx.x, xcd = b & 7, jb = b >> 3;
  const int pa = xcd * 4 + (jb & 3), ca = jb >> 2, pb = (xcd >> 1) * 8 + (jb & 7), cb = (xcd & 1) * 4 + (jb >> 3);
  const int po_ = MAPO ? pb : pa, co_ = MAPO ? cb : ca, pz_ = MAPZ ? pb : pa, cz_ = MAPZ ? cb : ca;
  const int P_ = MAPQ ? (xcd >> 1) * 4 + (jb & 3) : xcd * 2 + (jb & 1), h_ = MAPQ ? (xcd & 1) * 8 + (jb >> 2) : jb >> 1;
  const rsrc_t rws = mk(ws, WS_USED);
  const v4i dws = mkd(ws, WS_USED);
  STAMP(0); CSTAMP(62);
  unsigned tag_lo, tag_hi;
  v4u W[4][16];
#pragma unroll
  for (int r = 0; r < 4; r++)
#pragma unroll
    for (int s = 0; s < 16; s++) asm volatile("" : "=a"(W[r][s]));
  {
    LANE();
    {
      const v4i dq = mkd(wqkv, 640 * 3840);
      const int vq = g * 3840 + c16 * 16 + (c16 < 5 ? 1280 : c16 < 10 ? -80 : 2400), s0 = (P * 40 + w * 12) * 3840 + 80 * h, kill = w == 3 ? OOB : 0;
      glds_in(dq, vq, s0, w * 3072);
      glds_in(dq, vq, (s0 + 4 * 3840) | kill, w * 3072 + 1024);
      glds_in(dq, vq, (s0 + 8 * 3840) | kill, w * 3072 + 2048);
    }
    const int xrt = 8 * P + (h >> 1), xc = 4 * (h & 1) + w;
    {
      const v4i dx = mkd(x0, 2048 * 1280);
#pragma unroll
      for (int i = 0; i < 4; i++) {
        const int k = (c16 - g - 4 * i) & 15;
        glds_in(dx, k < 10 ? g * 1280 + k * 16 : OOB, (xrt * 16 + 4 * i) * 1280 + xc * 160, 12288 + w * 4096 + i * 1024);
      }
    }
    float ep0 = ld4h<0>(dws, 0, 0), ep1 = ld4h<4>(dws, 0, 0);
    const v4i dw = mkd(wsp, 2048 * 4096);
    const int wst = opq_s(57344 + w * 25344), wb0 = opq_s(64 * pz * 4096);
    int wv[2];
#pragma unroll
    for (int i = 0; i < 2; i++) wv[i] = opq_v((l >> 5) * 4096 + pan(4 * i + ((l >> 3) & 3), pz, KQ) * 128 + ((l & 7) ^ (l >> 5)) * 16);
    auto ldW = [&](auto ni) AI { constexpr int n = ni.v; const int so = wb0 + ((w + (n >> 4)) & 3) * 65536 + (n & 7) * 8192, ld_ = wst + (n % 24) * 1056; if constexpr (n < NTN) glds_in(dw, wv[(n >> 3) & 1], so, ld_); else glds_w(dw, wv[(n >> 3) & 1], so, ld_); };
    const int wrd = opq_v(wst + (c16 >> 1) * 1056 + (c16 & 1) * 512 + (g ^ (c16 & 1)) * 16);
    auto rdW = [&](auto ni) AI { constexpr int n = ni.v; ldsa16<(n % 24 >> 3) * 8448 + (n & 7) * 64>(W[n >> 4][n & 15], wrd); };
    sfor<0, 8>(ldW);
    const int tau = jb, sl = xcd, stg = 28672 + w * 7168;
    const int ty = tau < 8 ? 0 : tau < 24 ? 1 : 2, ot = tau - ty * 8 - (ty >> 1) * 8, cc = ot & 7;
    const int o_kg = opq_u((ty < 2 ? 10 * sl : sl < 4 ? 14 * sl : 4 + 13 * sl) + w2), o_n = opq_u((ty < 2 ? 10 : sl < 4 ? 14 : 13) - w2);
    const int ldb = ty == 1 ? 3456 : 1280, lim = ty != 1 ? 9 : cc < 6 ? 13 : 11, chi = kh * 8 + (l & 7);
    const v4i dsrc = mkd(ty == 0 ? wo : ty == 1 ? win : wout, (ty == 2 ? 864 : 640) * ldb);
    const int ov = opq_v(ZPAD ? (chi <= lim ? (l >> 3) * ldb + chi * 16 : OOB) : (l >> 3) * ldb + (chi < lim ? chi : lim) * 16);
    const int o0 = opq_u(o_kg * 8 * ldb + (ty != 1 ? ot * 160 : (ot < 8 ? 1728 : 0) + (cc < 6 ? 7 * cc : 6 + 6 * cc) * 32));
    const int od = opq_u(16 * ldb), k5 = opq_u(o_n > 10 ? 0 : OOB), k6 = opq_u(o_n > 12 ? 0 : OOB);
    const int o_v = opq_v(kh * 4 + g < (ty == 1 ? 7 : 5) ? (kh * 4 + g) * 1024 + c16 * 16 : OOB);
    int o_d;
    {
      const int o_dst = ty == 0 ? WO_OFF + ot * WO_T : ty == 1 ? WI_OFF + ot * WI_T : WU_OFF + ot * WU_T, kb = ty < 2 ? 10 : 14, kstr = ty == 1 ? 7168 : 5120;
      const int kap = o_kg + 2 * l, ks = kap >> 2, hi = ks >= kb, kq = ks - (hi ? kb : 0), hs = kb >> 1, kw = kq >= hs, far = hi != (LOCF && cc >= 4);
      const int pos = 2 * (kq - (kw ? hs : 0) + (far ? hs - (ty == 2 && kw && !hi) : 0)) + kw;
      o_d = opq_v(2 * l < o_n ? o_dst + pos * kstr + (kap & 3) * 256 : OOB);
    }
    const int o_c = opq_u(ty == 1 ? CSI_OFF + (ot * 16 + sl * 2 + w2) * 512 + kh * 256 : OOB);
    asm volatile("s_nop 4");
#pragma unroll
    for (int k = 0; k < 7; k++) glds_in(dsrc, ov, (o0 + k * od) | (k == 5 ? k5 : k == 6 ? k6 : 0), stg + k * 1024);
    touch(dws, seloob(F0B + l * FARR + b * FS, 0x1FFFull), w == 1 ? 0 : OOB);
    const int ct = 2 * w + (g & 1), pq = l & 3;
    const int cb_ = ct == 2 ? 32 + (pq & 1) * 8 + (pq >> 1) * 80 : ct == 7 ? 192 + (pq & 1) * 8 : (ct < 2 ? ct * 48 : ((ct - 1) >> 1) * 80 + ((ct - 1) & 1) * 48) + pq * 8;
    const int trq = opq_v(((g >> 1) * 8 + (c16 >> 2)) * 256 + cb_);
    int qv[3], xa[3];
#pragma unroll
    for (int i = 0; i < 3; i++) {
      const int k = 2 * i + (g >> 1), kap = 5 * P + k;
      const int kr_ = (kap >> 2) + (LOCF && h >= 8 ? (kap >= 48 ? -12 : kap < 32 ? 12 : 0) : 0);
      qv[i] = opq_v((k < 5 && (ct < 7 || c16 < 8)) ? kr_ * 7680 + ct * 1024 + ((kap & 3) * (ct == 7 ? 8 : 16) + c16) * 16 : OOB);
      xa[i] = opq_v(12288 + w * 4096 + c16 * 256 + ((i * 4 + g + c16) & 15) * 16);
    }
    sfor<8, 16>(ldW);
    WAITV(24);
    pin(ep0); pin(ep1);
    STAMP(1);
    BAR0();
    {
      v4u xf[3]; v2u lo[3], hi[3];
#pragma unroll
      for (int i = 0; i < 3; i++) { lo[i] = trd(trq + i * 4096); hi[i] = trd(trq + i * 4096 + 1024); xf[i] = lds16(xa[i]); }
      SB();
      const int tile = ACT_OFF + A_XIN + xrt * XT;
#pragma unroll
      for (int i = 0; i < 3; i++) {
        st16(v4u{lo[i].x, lo[i].y, hi[i].x, hi[i].y}, rws, qv[i], WQ_OFF + h * WQ_T);
        st16(xf[i], rws, (i == 2 ? seloob(l16, M_LO) : l16) + i * 1024, tile + xc * 2560);
      }
      float s = 0.f, q = 0.f, cs = 0.f;
#pragma unroll
      for (int i = 0; i < 3; i++) {
#pragma unroll
        for (int e = 0; e < 4; e++) { s = dot2(xf[i][e], ONE2, s); q = dot2(xf[i][e], xf[i][e], q); }
        cs = dot2(lo[i].x, ONE2, cs); cs = dot2(lo[i].y, ONE2, cs); cs = dot2(hi[i].x, ONE2, cs); cs = dot2(hi[i].y, ONE2, cs);
      }
      __builtin_amdgcn_raw_buffer_store_b32(asu(xsum2(s, q)), rws, seloob(c16 * 8 + (g >> 1) * 4, M_GEVEN), tile + 20480 + xc * 128, SC1);
      const v2u t = sw32(asu(cs), asu(cs));
      __builtin_amdgcn_raw_buffer_store_b32(asu(asf(t.x) + asf(t.y)), rws, seloob(l * 4, M_LO), CSQ_OFF + (h * 16 + P) * 512 + w * 128, SC1);
    }
    FENCE();
    sfor<16, 24>(ldW);
    {
      const u64 epoch = ((u64)(unsigned)__builtin_amdgcn_readfirstlane(asu(ep1)) << 32) | (unsigned)__builtin_amdgcn_readfirstlane(asu(ep0));
      const u64 tag = epoch * 0x9E3779B97F4A7C15ull + 0xD1B54A32D192ED03ull;
      tag_lo = opq_s((unsigned)tag); tag_hi = opq_s((unsigned)(tag >> 32));
    }
#define LGK0() asm volatile("s_waitcnt lgkmcnt(0)" ::: "memory")
    WAITV(32); sfor<0, 8>(rdW); LGK0(); sfor<24, 32>(ldW);
    WAITV(24); sfor<8, 16>(rdW);
    {
      const int tro = stg + (c16 >> 2) * 128 + g * 32 + (l & 3) * 8;
      float ocs = 0.f;
#pragma unroll
      for (int i = 0; i < 7; i++) {
        const v2u lo = trd(tro + i * 1024), hi2 = trd(tro + i * 1024 + 512);
        if (i < 5) { ocs = dot2(lo.x, ONE2, ocs); ocs = dot2(lo.y, ONE2, ocs); ocs = dot2(hi2.x, ONE2, ocs); ocs = dot2(hi2.y, ONE2, ocs); }
        if (i == 0) { WAITV(16); BAR0(); flag(F0R + (P * 16 + h) * FS); STAMP(2); FENCE(); }
        st16(v4u{lo.x, lo.y, hi2.x, hi2.y}, rws, o_v, __builtin_amdgcn_readlane(o_d, i));
      }
      __builtin_amdgcn_raw_buffer_store_b32(asu(ocs), rws, l * 4, o_c, SC1);
      st16(v4u{0, 0, 0, 0}, rws, seloob(g * ST_LAY + c16 * 16, M_LO), ST_OFF + po * 8192 + w * 2048 + co * 256);
    }
    FENCE();
    LGK0(); sfor<32, 40>(ldW);
    WAITV(26); sfor<16, 24>(rdW); LGK0(); sfor<40, 48>(ldW);
    WAITV(26); sfor<24, 32>(rdW); LGK0(); sfor<48, 56>(ldW);
    WAITV(16); sfor<32, 40>(rdW);
    const bool on = l < 32 || (l < 34 && P > 0);
    const int pv = on ? F0R + (l < 16 ? P * 16 + l : l < 32 ? (l - 16) * 16 + h : P * 16 + l - 34) * FS : OOB;
    v2u f0 = ld8h<0>(dws, pv, w == 0 ? 0 : OOB);
    LGK0(); sfor<56, 64>(ldW);
    STAMP(3);
    WAITV(8); sfor<40, 56>(rdW);
    pin(f0);
    if (w == 0 && !__all(!on || (f0.x == tag_lo && f0.y == tag_hi))) poll(rws, pv, !on, tag_lo, tag_hi);
    BAR();
    flag(F0B + b * FS);
    const int qo = qslot_off(w), wq = WQ_OFF + h * WQ_T + qo;
    cp_run<0, 8>(dws, l16, wq, qo);
    WAITV(9);
    sfor<56, 64>(rdW);
    cp_run<0, 8>(dws, l16, wq + 30720, qo + 30720);
  }

#pragma unroll 1
  for (int Lr = 0;; Lr = 1) {
    const int L = opq_s(Lr), kill1 = L << 30;
    const int act = ACT_OFF + L * ACT_LAY, fl = FLAY + L * FLAY_SZ;
    const int sb = L * 24 + 5;
    f32x4 z1[7];
    unsigned braw;
    v2u rr[5];
    constexpr int RZ = 14;
    v4u Z[RZ];
    { LANE();
      const int qo = qslot_off(w), wq = (WHIT ? WQ_OFF : WQ_OFF + h * WQ_T) + qo;
      int vb[4], vh[4];
#pragma unroll
      for (int i = 0; i < 4; i++) { const int k = (i + w) & 3; vb[i] = opq_v(l16 + k * 7680); vh[i] = opq_v((ZPAD && c16 >= 8 ? 0x40000 : (g * 8 + (l & 7)) * 16) + k * 7680); }
      const int h2 = w < 2 ? w + 4 : w;
      const int c4 = opq_v(c16 * 4), g16 = opq_v(g * 16);
      const int wos = opq_s((WHIT ? WQ_OFF : WO_OFF + co * WO_T)), wzs = opq_s((WHIT ? WQ_OFF : WI_OFF + cz * WI_T));
      f32x4 acc[2][8];
      float mt[3], rt_[3], csn[3], hcn; f32x4 mn[3], rn[3], cst[5], ha[2][4], hcs;
      {
        v4u A[2][20], Hh[5], B[8];
        const int gsh = opq_s(LOCF ? (h >> 3) * 12288 : 0);
        int va[4], spo, xoff, hoff, fbo, cqo, rv;
        auto ldB = [&](auto fi) AI { constexpr int f = fi.v, t = f >> 3, ct = f & 7; B[f & 7] = lds16((ct < 7 ? vb[t & 3] : vh[t & 3]) + ((t >> 2) & 1) * 30720 + ct * 1024); };
        auto ldA = [&](auto ni) AI {
          constexpr int n = ni.v, t = q_step(n), i = (n % 9) & 1;
          const int go = (t >> 2) * 4096 + (t < 8 ? gsh : t < 12 ? 0 : -gsh);
          if constexpr (n % 9 == 0) Hh[t >> 2] = ld16h<0>(dws, va[0], hoff + go);
          else A[i][t] = ld16h<0>(dws, va[t & 3], xoff + i * XT + go);
        };
        rv = opq_v((g >> 1) * 256 + c16 * 16 + (g & 1) * 8);
#pragma unroll
        for (int i = 0; i < 4; i++) va[i] = opq_v(l16 + ((i + w) & 3) * 1024);
        xoff = opq_s(act + A_XIN + (8 * P + 2 * w) * XT); hoff = opq_s((act + A_XIN + (8 * P - 1) * XT) | ((P - 1) & OOB));
        spo = opq_v(seloob((int)sel0((2 * w + 1 + g) * XT, M_LO) + c16 * 8, P > 0 ? 0xFFFFFFFFFFFFull : M_LO));
        cqo = opq_v((l >> 3) * 512 + (l & 7) * 16);
        fbo = opq_v(l < 32 ? F0B + (((l >> 3) * 8 + (((l >> 3) + 1) & 2 ? cz : co)) * 8 + (l & 7)) * FS : l < 48 ? F0R + ((po >> 1) * 16 + l - 32) * FS : OOB);
        {
          const bool on = L == 1 && (l < 8 || (l < 16 && P > 0));
          const int pb_ = opq_s(FLAY + F6 + (P * 16 + h) * 2 * FS);
          const int pv = opq_v(on ? pb_ + (l < 8 ? w2 * 64 : FS - 64) + l * 8 : OOB);
          if (L == 1) poll(rws, pv, !on, tag_lo, tag_hi);
        }
        STAMP(sb - 1);
        u64 dg[12];
        if constexpr (DIAG == 1) dg[0] = tick();
        sfor<0, QPRE>(ldA);
        if (L == 0) { WAITV(8 + QPRE); BAR(); }
        sfor<0, 6>([&](auto fi) AI { ldB(IC<(fi.v ^ 1)>{}); SB(); });
        f32x4 hac[6];
        v2u sp[8], fb; float ss[4], sq[4]; v4u cq[2]; int xa, xb;
        sfor<0, 160>([&](auto fi) AI {
          constexpr int f = fi.v, t = f >> 3, ct = f & 7, hc = ct < 3 ? ct : ct - 2;
          constexpr bool halo = (t & 3) == 0 && (ct < 3 || ct >= 5);
          constexpr int qn = QT.ld[f], wu = QT.wu[f], ou = QT.ou[f];
          constexpr int si = q_si(f); (void)si;
          const v4u bb = B[f & 7];
          auto mm = [&](f32x4& d, const v4u& x) AI {
            if constexpr (ct < 5) mma<t == 0, false>(d, bb, x); else mma<t == 0, false>(d, x, bb);
            SB();
          };
          if constexpr (ct == 0) WAITV(QT.wt[t]);
          mm(acc[0][ct], A[0][t]);
          if constexpr (DIAG == 1 && f % 16 == 0) dg[1 + f / 16] = tick();
          if constexpr (f == 58 || f == 90 || f == 122) WAITV(QT.ws[(f - 58) / 32]);
          if constexpr (f == 101) WAITV(QT.cum[101] - QT.cum[86]);
          if constexpr (f == 26 || f == 58 || f == 90 || f == 122) BAR0();
          if constexpr (((f + 6) ^ 1) < 160) ldB(IC<((f + 6) ^ 1)>{});
          if constexpr ((t & 7) >= 4 && t < 16 && ct == 3) vb[t & 3] = gaddi<61440>(vb[t & 3]);
          if constexpr ((t & 7) >= 4 && t < 16 && ct == 5) vh[t & 3] = gaddi<61440>(vh[t & 3]);
          if constexpr (wu >= 0 && wu % 4 == 0) cp_arm(qo + (2 + wu / 8) * 30720 + (wu & 4) * 1024);
          if constexpr (ou == 0 || ou == 4) cp_arm(L_WO + w * 8192 + ou * 1024);
          {
            if constexpr (f >= 102 && f < 106) ss[f - 102] = gadd(asf(sp[f - 102].x), asf(sp[f - 98].x));
            if constexpr (f == 106 || f == 108) ss[(f - 106) / 2] = gadd(ss[(f - 106) / 2], ss[(f - 106) / 2 + 2]);
            if constexpr (f == 110) ss[0] = gadd(ss[0], ss[1]);
            if constexpr (f == 111) { ss[0] = ss[0] * (1.f / 640.f); pin(ss[0]); }
            if constexpr (f == 112) ss[1] = gmul(ss[0], ss[0]);
            if constexpr (f == 113) sq[1] = gadd(sq[1], sq[3]);
            if constexpr (f == 114) sq[0] = gadd(sq[0], sq[1]);
            if constexpr (f == 116) { sq[0] = sq[0] * (1.f / 640.f) - ss[1]; pin(sq[0]); }
            if constexpr (f == 118) { sq[0] += 1e-5f; pin(sq[0]); }
            if constexpr (f == 119) { sq[0] = __builtin_amdgcn_rsqf(sq[0]); pin(sq[0]); }
            if constexpr (f == 120) { xa = opq_v(L_XST1 + w * 512 + l * 4); *lp<float>(xa) = ss[0]; }
            if constexpr (f == 121) *lp<float>(xa + 256) = sq[0];
          }
          if constexpr (f == 126) xb = opq_v(L_XST1 + w * 512 + g16);
          if constexpr (f >= 127 && f < 130) mn[f - 127] = ldsf4(xb + (f - 127) * 64);
          if constexpr (f == 130 || f == 131 || f == 133) rn[f == 133 ? 2 : f - 130] = ldsf4(xb + 256 + (f == 133 ? 2 : f - 130) * 64);
          if constexpr (f == 134) xa = opq_v(L_HALO + w * 6144 + l16);
          if constexpr (f == 142) xb = opq_v(L_CSQ + g16);
          if constexpr (f >= 143 && f < 148) cst[f - 143] = ldsf4(xb + (f - 143) * 64);
          if constexpr (f == 148) xb = opq_v(xb + w * 64);
          if constexpr (f == 149) hcs = ldsf4(xb);
          if constexpr (f == 150) xa = opq_v(L_HALO + w * 1024 + l16);
          if constexpr (f == 151) xb = opq_v(L_HALO + h2 * 1024 + l16);
          if constexpr (f >= 152 && f < 156) ha[0][f - 152] = ldsf4(xa + (f - 152) * 6144);
          SB();
          mm(acc[1][ct], A[1][t]);
          {
            if constexpr (wu >= 0) cp_one<wu>(dws, l16, wq + (2 + wu / 8) * 30720 + (wu & 4) * 1024);
            if constexpr (f == 45 || f == 53) cq[f == 53] = ld16h<0>(dws, cqo, (CSQ_OFF + h * 8192 + w * 128 + (f == 53) * 4096) | kill1);
            if constexpr (f == 49) fb = ld8h<0>(dws, fbo, kill1);
            if constexpr (f == 101) {
              pin(fb); pin(cq[0]); pin(cq[1]);
              if (L == 0) {
                if (!__all(l >= 48 || (fb.x == tag_lo && fb.y == tag_hi))) poll(rws, fbo, l >= 48, tag_lo, tag_hi);
                f32x4 cs = __builtin_bit_cast(f32x4, cq[0]) + __builtin_bit_cast(f32x4, cq[1]);
#pragma unroll
                for (int e = 0; e < 4; e++) {
                  cs[e] += dpp<0x128>(cs[e]);
                  const v2u a = sw32(asu(cs[e]), asu(cs[e])); const float u = asf(a.x) + asf(a.y); const v2u d = sw16(asu(u), asu(u)); cs[e] = asf(d.x) + asf(d.y);
                }
                *lp<f32x4>(L_CSQ + w * 128 + (l & 7) * 16) = cs;
              }
            }
            if constexpr (qn >= 0) ldA(IC<qn>{});
            if constexpr (si >= 0) sp[si] = ld8h<si * 128>(dws, spo, act + A_XIN + (8 * P - 1) * XT + 20480);
            if constexpr (f >= 110 && f < 130 && f % 4 == 3) rr[(f - 110) / 4] = ld8h<(f - 110) / 4 * 512>(dws, rv, act + A_XIN + (4 * po + w) * XT + co * 2560);
            if constexpr (f >= 103 && f < 111 && (f & 1)) sq[(f - 103) / 2] = gadd(asf(sp[(f - 103) / 2].y), asf(sp[(f - 103) / 2 + 4].y));
            if constexpr (f == 113) sq[0] = gadd(sq[0], sq[2]);
            if constexpr (f == 117) { sq[0] = fmaxf(sq[0], 0.f); pin(sq[0]); }
          }
          if constexpr (f == 123) xa = opq_v(L_XST1 + w * 512 + c4);
          if constexpr (ou >= 0) cp_one<ou>(dws, l16, wos + w * 8192 + (ou & 4) * 1024);
          if constexpr (f == 125 || f == 127 || f == 131) { constexpr int i = f == 131 ? 2 : (f - 125) / 2; mt[i] = ldsf(xa + i * 64); rt_[i] = ldsf(xa + 256 + i * 64); }
          if constexpr (f >= 136 && f < 142) *lp<f32x4>(xa + (f - 136) * 1024) = hac[f - 136];
          if constexpr (f == 143) xa = opq_v(L_CSQ + c4);
          if constexpr (f == 144) { csn[0] = ldsf(xa + 320); csn[1] = ldsf(xa + 384); }
          if constexpr (f == 146) { csn[2] = ldsf(xa + 448); xa = opq_v(xa + (h2 + 2) * 64); }
          if constexpr (f == 148) hcn = ldsf(xa);
          if constexpr (f == 150) BAR0();
          if constexpr (f >= 152 && f < 156) ha[1][f - 152] = ldsf4(xb + (f - 152) * 6144);
          if constexpr (halo) { SB(); mm(hac[hc], Hh[t >> 2]); }
          SB();
        });
        if constexpr (DIAG == 1) { dg[11] = tick(); DUMP(); }
        mma_settle();
#pragma unroll
        for (int j = 0; j < 8; j++) { pin(acc[0][j]); pin(acc[1][j]); }
      }
      STAMP(sb + 0);
      const int nbw = opq_v(L_NB + l * 8), w3s = wos + 65536 + w * 9216, w3d = L_WO + 65536 + w * 9216;
      cp_run<0, 3>(dws, l16, wos + 32768 + w * 8192, L_WO + 32768 + w * 8192);
      {
        const f32x4 a0 = (ha[0][0] + ha[0][1]) + (ha[0][2] + ha[0][3]), a1 = (ha[1][0] + ha[1][1]) + (ha[1][2] + ha[1][3]);
        *lp<v2u>(nbw + (w < 3 ? w * 512 : 15360)) = pk4((a0 - mt[2] * hcs) * rt_[2]);
        *lp<v2u>(nbw + (w != 2 ? h2 * 512 : 15360)) = pk4((a1 - mn[2] * hcn) * rn[2]);
      }
      v2u tk[2][8];
      auto fix = [&](int i, int ct) AI { tk[i][ct] = ct < 5 ? pk4((acc[i][ct] - mt[i] * cst[ct]) * rt_[i]) : pk4((acc[i][ct] - mn[i] * csn[ct - 5]) * rn[i]); };
      fix(1, 0); fix(1, 1); fix(1, 2);
      pin(tk[1][2]);
      cp_run<3, 5>(dws, l16, wos + 32768 + w * 8192, L_WO + 32768 + w * 8192);
      fix(1, 5); fix(1, 6); fix(1, 7);
      const int nbr = opq_v(nbw + w * 3072);
#pragma unroll
      for (int i = 0; i < 6; i++) *lp<v2u>(nbr + 3072 + i * 512) = tk[1][i < 3 ? i : i + 2];
      cp_run<5, 8>(dws, l16, wos + 32768 + w * 8192, L_WO + 32768 + w * 8192);
      SB();
      fix(0, 0); fix(0, 1); fix(0, 2); fix(1, 3); fix(1, 4);
      BAR();
      v2u nb[6];
#pragma unroll
      for (int i = 0; i < 6; i++) { nb[i] = lds8(nbr + i * 512); asm volatile("" ::: "memory"); }
      cp_run<0, 3>(dws, l16, w3s, w3d);
      STAMP(sb + 1);
      SB();
      fix(0, 3); fix(0, 4);
      v6u K6[3]; v4u Q[2][2];
      {
        const v2u qx = sw32(tk[0][2].x, tk[1][2].x), qy = sw32(tk[0][2].y, tk[1][2].y);
        const unsigned q1x = sw32(qx.y, qx.y).y, q1y = sw32(qy.y, qy.y).y;
        Q[0][0] = v4u{tk[0][3].x, tk[0][3].y, sel0(qx.y, M_G0), sel0(qy.y, M_G0)};
        Q[0][1] = v4u{sel0(qx.y, M_G1), sel0(qy.y, M_G1), tk[0][4].x, tk[0][4].y};
        Q[1][0] = v4u{tk[1][3].x, tk[1][3].y, sel0(q1x, M_G0), sel0(q1y, M_G0)};
        Q[1][1] = v4u{sel0(q1x, M_G1), sel0(q1y, M_G1), tk[1][4].x, tk[1][4].y};
      }
#pragma unroll
      for (int i = 0; i < 2; i++) {
        K6[i + 1] = v6u{tk[i][0].x, tk[i][0].y, tk[i][2].x, tk[i][2].y, tk[i][1].x, tk[i][1].y};
        pin(Q[i][0]); pin(Q[i][1]); pin(K6[i + 1]);
      }
      K6[0] = v6u{nb[0].x, nb[0].y, nb[2].x, nb[2].y, nb[1].x, nb[1].y};
      pin(K6[0]);
      NOP2();
      f32x4 S[4][2];
#pragma unroll
      for (int u = 3; u >= 0; u--)
#pragma unroll
        for (int k = 1; k >= 0; k--) {
          const v6u kk = K6[(u >> 1) + k];
          mma<true, false>(S[u][k], (u & 1) ? __builtin_shufflevector(kk, kk, 2, 3, 4, 5) : __builtin_shufflevector(kk, kk, 0, 1, 2, 3), Q[u >> 1][u & 1]);
        }
      SB();
      cp_run<3, 6>(dws, l16, w3s, w3d);
      unsigned sel[4];
      {
        const unsigned zz = 0x0c0c0c0cu, a = ::sel(0x0c0c0504u, 0x05040c0cu, M_ODD), d = ::sel(0x0c0c0706u, 0x07060c0cu, M_ODD);
        sel[2] = ::sel(zz, a, le_mask(0)); sel[3] = ::sel(zz, d, le_mask(2));
        sel[0] = ::sel(a, zz, le_mask(0)); sel[1] = ::sel(d, zz, le_mask(2));
      }
      const int ovo = (g & 1) * 768 + (g >> 1) * 256 + c16 * 16, ovt = seloob(g * OT + 512 + c16 * 16, M_LO);
      fix(0, 5); fix(0, 6); fix(0, 7);
      v6u V6[3];
#pragma unroll
      for (int j = 0; j < 3; j++) { V6[j] = v6u{nb[3 + j].x, nb[3 + j].y, tk[0][5 + j].x, tk[0][5 + j].y, tk[1][5 + j].x, tk[1][5 + j].y}; pin(V6[j]); }
      SB();
#pragma unroll
      for (int u = 0; u < 8; u++) pin(S[u >> 1][u & 1]);
      if (P + w == 0) { S[0][0] = f32x4{-1e30f, -1e30f, -1e30f, -1e30f}; pin(S[0][0]); S[1][0] = S[0][0]; }
      float s0[4], s1[4], mx[4];
#pragma unroll
      for (int u = 0; u < 4; u++) {
        const f32x4 a = S[u][0], bq = S[u][1];
        s0[u] = self(self(a[0], a[1], M_ODD), self(bq[0], bq[1], M_ODD), le_mask(0));
        s1[u] = self(self(a[2], a[3], M_ODD), self(bq[2], bq[3], M_ODD), le_mask(2));
        mx[u] = vmax(s0[u], s1[u]);
      }
      auto spread = [&](float t, float (&o)[4]) AI {
        const v2u a = sw16(asu(t), asu(t)), x = sw32(a.x, a.x), y = sw32(a.y, a.y);
        o[0] = asf(x.x); o[1] = asf(x.y); o[2] = asf(y.x); o[3] = asf(y.y);
      };
      const float kk = 0.22360679774997896f * 1.4426950408889634f;
      {
        const v2u a = sw32(asu(mx[0]), asu(mx[1])), bq = sw32(asu(mx[2]), asu(mx[3]));
        const v2u d = sw16(asu(vmax(asf(a.x), asf(a.y))), asu(vmax(asf(bq.x), asf(bq.y))));
        spread(vmax(asf(d.x), asf(d.y)), mx);
      }
      SB();
      cp_run<6, 9>(dws, l16, w3s, w3d);
      float sm4[4];
#pragma unroll
      for (int u = 0; u < 4; u++) {
        const f32x2 e = (f32x2{s0[u], s1[u]} - mx[u]) * kk;
        s0[u] = __builtin_amdgcn_exp2f(e.x); s1[u] = __builtin_amdgcn_exp2f(e.y);
        sm4[u] = s0[u] + s1[u];
      }
      {
        const v2u a = sw32(asu(sm4[0]), asu(sm4[1])), bq = sw32(asu(sm4[2]), asu(sm4[3]));
        const v2u d = sw16(asu(asf(a.x) + asf(a.y)), asu(asf(bq.x) + asf(bq.y)));
        spread(__builtin_amdgcn_rcpf(asf(d.x) + asf(d.y)), sm4);
      }
      v4u pf[4];
#pragma unroll
      for (int u = 0; u < 4; u++) {
        const f32x2 pn = f32x2{s0[u], s1[u]} * sm4[u];
        const unsigned pp = pk(pn.x, pn.y);
        pf[u] = v4u{__builtin_amdgcn_perm(pp, 0u, sel[0]), __builtin_amdgcn_perm(pp, 0u, sel[1]), __builtin_amdgcn_perm(pp, 0u, sel[2]), __builtin_amdgcn_perm(pp, 0u, sel[3])};
        pin(pf[u]);
      }
      NOP2();
      f32x4 Om[4], Ot[4];
#pragma unroll
      for (int u = 0; u < 4; u++) { const v6u v = V6[u & 1]; mma<true, false>(Om[u], (u >> 1) ? __builtin_shufflevector(v, v, 2, 3, 4, 5) : __builtin_shufflevector(v, v, 0, 1, 2, 3), pf[u]); }
#pragma unroll
      for (int u = 0; u < 4; u++) { const v6u v = V6[2]; mma<true, false>(Ot[u], (u >> 1) ? __builtin_shufflevector(v, v, 2, 3, 4, 5) : __builtin_shufflevector(v, v, 0, 1, 2, 3), pf[u]); }
      asm volatile("s_nop 3");
#pragma unroll
      for (int u = 0; u < 4; u++) pin(Om[u]);
      const int so = act + A_O + (8 * P + 2 * w) * OT + 5 * h * 256;
      st16(join16(pk4(Om[0]), pk4(Om[1])), rws, ovo, so);
      st16(join16(pk4(Om[2]), pk4(Om[3])), rws, ovo, so + OT);
      SB();
#pragma unroll
      for (int u = 0; u < 4; u++) pin(Ot[u]);
      {
        const v2u x0_ = pk4(Ot[0]), y0_ = pk4(Ot[1]), x1_ = pk4(Ot[2]), y1_ = pk4(Ot[3]);
        st16(v4u{sw16(x0_.x, x1_.x).x, sw16(x0_.y, x1_.y).x, sw16(y0_.x, y1_.x).y, sw16(y0_.y, y1_.y).y}, rws, ovt, so);
      }
      STAMP(sb + 2);
      cp_run<0, 6>(dws, l16, wzs + w * 6144, w * 6144);
      cp_run<0, 4>(dws, l16, wzs + 40960 + w * 4096, 40960 + w * 4096);
      const int fv = opq_v(seloob(l * FS, M_G0)), fo = opq_s(fl + F1 + P * 16 * FS + h * 8);
      WAITV(10);
      BAR();
      push(fv, fo);
#pragma unroll
      for (int j = 0; j < 5; j++) pin(rr[j]);
      STAMP(sb + 3);
      touch(dws, (g < 2 && c16 < 10) ? g * OT + c16 * 128 : OOB, so);
      cp_run<0, 4>(dws, l16, wzs + 24576 + w * 4096, 24576 + w * 4096);
    }
    { LANE();
      f32x4 acc[2][5];
      const int tile = opq_s(act + A_X1 + (4 * po + w) * XT), wzs = opq_s((WHIT ? WQ_OFF : WI_OFF + cz * WI_T));
      const int vo = opq_v((g & 1) * 512 + (g >> 1) * 256 + c16 * 16), vo4 = opq_v(seloob(vo, M_GEVEN)), vs = opq_v(seloob(c16 * 8 + (g >> 1) * 4, M_GEVEN));
      const int xw = opq_v(L_X2 + (w ^ 1) * 5120 + l16), xr = opq_v(L_X2 + w * 5120 + l16);
      const int lo_ = LOCF ? (co >> 2) * 10240 : 0, dl = opq_s(10240 - 2 * lo_);
      const int a0 = opq_s(act + A_O + (4 * po + w) * OT + kh * 5120 + lo_), a1 = opq_s(act + A_O + (4 * po + (w ^ 1)) * OT + kh * 5120 + lo_);
      const int b0 = opq_v(L_WO + l16 + kh * 5120), b1 = opq_v(b0 + 51200);
      const int pv = opq_v(seloob(fl + F1 + (po * 8 + co) * FS + (kh * 4 + (l >> 2) * 4 + l) * 8, 0xFFull));
      v4u A[2][10], ci[4];
      const int civ = opq_v(g * 512 + c16 * 16), cis = opq_s((CSI_OFF + (w2 * 8 + cz) * 8192 + kh * 256) | kill1);
      gemm_m2k2<10, 5, 3, true, WINA, true, nv_o>([&](auto ti) AI { return ti.v < 5 ? b0 + ti.v * 10240 : b1 + (ti.v - 5) * 10240; }, xw, A, acc,
          [&]() AI {
#pragma unroll
        for (int j = 0; j < 5; j++) { acc[0][j] = unpk4(rr[j]); pin(acc[0][j]); }
        poll<PDLY>(rws, pv, l >= 8, tag_lo, tag_hi);
        STAMP(sb + 4);
      },
          [&](auto ii, auto ti) AI { return ld16h<(ti.v % 5 & 3) * 1024>(dws, l16, (ii.v ? a1 : a0) + (ti.v >= 5 ? dl : 0) + (ti.v % 5 >> 2) * 4096); },
          [&](auto fi) AI {
        constexpr int f = fi.v;
        if constexpr (f == 13 || f == 31) BAR0();
        if constexpr (f == 32 || f == 36) cp_arm(L_Z2B + 71680 + w * 7168 + (f - 32) * 1024);
        if constexpr (f == 35) cp_one<3>(dws, l16, wzs + 71680 + w * 7168);
        if constexpr (f == 39 || f == 43 || f == 47) cp_one<(f - 39) / 4>(dws, l16, wzs + 71680 + w * 7168 + 4096);
      },
          [&](auto fi) AI {
        constexpr int f = fi.v;
        if constexpr (f >= 17 && f < 25 && (f & 1)) ci[(f - 17) / 2] = ld16h<((f - 17) / 2 & 1) * 2048>(dws, civ, cis + (f - 17) / 4 * 4096);
        if constexpr (f >= 32) cp_one<f - 32>(dws, l16, wzs + 71680 + w * 7168);
      });
      STAMP(sb + 5);
      BAR();
      xchg_read<5>(xr, acc);
      store_x(acc[0], rws, tile + co * 2560, tile + 20480 + co * 128, vo, vo4, vs);
#pragma unroll
      for (int j = 0; j < 4; j++) pin(ci[j]);
      if (L == 0) {
        f32x4 t = (__builtin_bit_cast(f32x4, ci[0]) + __builtin_bit_cast(f32x4, ci[1])) + (__builtin_bit_cast(f32x4, ci[2]) + __builtin_bit_cast(f32x4, ci[3]));
#pragma unroll
        for (int e = 0; e < 4; e++) { const v2u a = sw32(asu(t[e]), asu(t[e])); const float u = asf(a.x) + asf(a.y); const v2u d = sw16(asu(u), asu(u)); t[e] = asf(d.x) + asf(d.y); }
        *lp<f32x4>(L_CS2 + w * 256 + c16 * 16) = t;
      }
      const int zc = 100352 + ((w * 10752) & ~1023);
      cp_run<0, 11>(dws, l16, wzs + zc, L_Z2B + zc);
      const int fv = opq_v(seloob(l * FS, 0xFFull)), fo = opq_s(fl + F2 + po * 8 * FS + co * 8);
      WAITV(11);
      BAR();
      push(fv, fo);
      STAMP(sb + 6);
      touch(dws, l < 20 ? co * 2560 + l * 128 : l == 20 ? 20480 + co * 128 : OOB, tile);
      cp_run<0, 4>(dws, l16, wzs + 57344 + ((w * 3584) & ~1023), L_Z2B + 57344 + ((w * 3584) & ~1023));
    }
    { LANE();
      const int wz1 = opq_s((WHIT ? WQ_OFF : WI_OFF + (8 + cz) * WI_T));
      const int lo_ = LOCF ? (cz >> 2) * 10240 : 0, dl = opq_s(10240 - 2 * lo_);
      const int a0 = opq_s(act + A_X1 + (4 * pz + w) * XT + kh * 5120 + lo_), a1 = opq_s(act + A_X1 + (4 * pz + (w ^ 1)) * XT + kh * 5120 + lo_);
      int xw = opq_v(L_X3 + (w ^ 1) * 7168 + l16), xr = opq_v(L_X3 + w * 7168 + l16);
      const int gso = opq_v(seloob(l * 4, 0x1111111111111111ull)), gro = opq_v(((l >> 2) & 3) * 256 + (4 * g + (l & 3)) * 16);
      const int zvo = opq_v((g & 1) * 1024 + (g >> 1) * 256 + c16 * 16), spo = opq_v(g * 256 + c16 * 8);
      const int sta = opq_v(L_XST3 + w * 128 + (g & 1) * 64 + c16 * 4), stb = opq_v(L_XST3 + w * 128 + g * 16), tv = opq_v(l * 128);
      const int b0 = opq_v(l16 + kh * 7168), b1 = opq_v(b0 + L_Z1B + 3 * 14336), b2 = opq_v(b1 + 4 * 14336);
      static_assert(L_Z1B == L_Z2B, "");
      const int sps = opq_s(act + A_X1 + (4 * pz + w) * XT + 20480), za0 = opq_s((w * 10752) & ~1023);
      float cs2[7];
#pragma unroll
      for (int j = 0; j < 7; j++) { cs2[j] = ldsf(L_CS2 + c16 * 4 + j * 64); pin(cs2[j]); }
      braw = __builtin_amdgcn_raw_buffer_load_b16(mk(bsp, 4096), c16 * 2, (64 * pz + 16 * w) * 2, 0);
      v4u A[2][10]; f32x4 acc[2][7];
      v2u sp0, sp1, u_;
      float m1, r1, st_; f32x4 m4, r4;
      gemm_m2k2<10, 7, 1, false, WINA, false, nv_z>([&](auto ti) AI { return ti.v < 4 ? b0 + ti.v * 14336 : ti.v < 7 ? b1 + (ti.v - 3) * 14336 : b2 + (ti.v - 7) * 14336; }, xw, A, acc,
          [&]() AI { poll<PDLY>(rws, seloob(fl + F2 + (pz * 8 + cz) * FS + l * 8, 0xFFull), l >= 8, tag_lo, tag_hi); pin(braw); STAMP(sb + 7); },
          [&](auto ii, auto ti) AI { return ld16h<(ti.v % 5 & 3) * 1024>(dws, l16, (ii.v ? a1 : a0) + (ti.v >= 5 ? dl : 0) + (ti.v % 5 >> 2) * 4096); },
          [&](auto fi) AI {
        constexpr int f = fi.v;
        if constexpr (f == 15 || f == 31) BAR0();
        if constexpr (f == 32 || f == 44 || f == 54) cp_arm(za0 + (f - 30) / 12 * 4096);
        if constexpr (f == 47) { u_ = sw32(asu(m1), asu(r1)); pin(u_); }
        if constexpr (f == 48) { st_ = gadd(asf(u_.x), asf(u_.y)); }
        if constexpr (f == 49) { u_ = sw16(asu(st_), asu(st_)); pin(u_); }
        if constexpr (f == 50) { st_ = gadd(asf(u_.x), asf(u_.y)); }
        if constexpr (f == 51) { u_ = sw32(asu(st_), asu(st_)); pin(u_); }
        if constexpr (f == 52) { m1 = asf(u_.x) * (1.f / 640.f); pin(m1); }
        if constexpr (f == 53) { st_ = gmul(m1, m1); }
        if constexpr (f == 55) { r1 = asf(u_.y) * (1.f / 640.f) - st_; pin(r1); }
        if constexpr (f == 56) { r1 = fmaxf(r1, 0.f); pin(r1); }
        if constexpr (f == 57) { r1 += 1e-5f; pin(r1); }
        if constexpr (f == 58) { r1 = __builtin_amdgcn_rsqf(r1); pin(r1); }
        if constexpr (f == 60) *lp<float>(sta) = self(r1, m1, M_GEVEN);
        if constexpr (f == 63) { m4 = ldsf4(stb); r4 = ldsf4(stb + 64); }
      },
          [&](auto fi) AI {
        constexpr int f = fi.v;
        if constexpr (f == 1) sp0 = ld8h<0>(dws, spo, sps);
        if constexpr (f == 3) sp1 = ld8h<128>(dws, spo, sps);
        if constexpr (f == 43) { m1 = gadd(asf(sp0.x), asf(sp1.x)); }
        if constexpr (f == 45) { r1 = gadd(asf(sp0.y), asf(sp1.y)); }
        if constexpr (f >= 32 && f % 3 == 2) cp_one<(f - 32) / 3>(dws, l16, wz1 + za0 + (f - 32) / 12 * 4096);
      });
      STAMP(sb + 8);
      BAR();
      xchg_read<7>(xr, acc);
      BAR();
      f32x4 z2[7], zs, zq;
      const int gz_ = 43008 + ((w * 17920) & ~1023), gs = wz1 + gz_, gd = L_Z1B + gz_;
      auto gel = [&](auto l0) AI {
        sfor<0, 7>([&](auto ji) AI {
          constexpr int j = ji.v;
          if constexpr (j < 6) cp_run<3 * j, 3 * j + 3>(dws, l16, gs, gd);
          SB();
          f32x4 v = gelu4((acc[0][j] - m4 * cs2[j]) * r4);
          if constexpr (j == 6 && !ZPAD) v *= cz < 6 ? 1.f : 0.f;
          z2[j] = v;
          if (j == 0) { zs = v; zq = v * v; } else { zs += v; zq += v * v; }
          pin(zs); pin(zq);
          SB();
        });
      };
      gel(IC<0>{});
      const int sto = ST_OFF + L * ST_LAY + pz * 8192 + w * 2048;
      {
        const f32x2 t = rowsum8(zs, zq);
        st16(v4u{asu(t.x), GTAG, asu(t.y), GTAG}, rws, gso, sto + cz * 256);
      }
      STAMP(sb + 9);
      const int zo = opq_s(act + A_Z + (cz * 64 + 2 * pz) * 7168), z8 = opq_s(114688 + w * 7168), us = opq_s((WHIT ? WQ_OFF : WU_OFF + co * WU_T));
      const int fv = opq_v(seloob(0, 1ull)), fo = opq_s(fl + F4 + (cz * 32 + pz) * FS);
      const int k6z = opq_s(ZPAD && cz >= 6 ? OOB : 0);
      f32x4 cs1[7];
      int tmp;
      xw += L_X3B - L_X3; xr += L_X3B - L_X3;
      v4u ga, gb;
      WAITV(19);
      BAR0();
      gemm_m2k2<10, 7, 2, true, 0, false, nv_none>([&](auto ti) AI { return ti.v < 3 ? b0 + ti.v * 14336 : ti.v < 7 ? b1 + (ti.v - 3) * 14336 : b2 + (ti.v - 7) * 14336; }, xw, A, acc,
          [&]() AI {}, [&](auto, auto) AI { return v4u{}; },
          [&](auto fi) AI {
        constexpr int f = fi.v;
        if constexpr (f == 0 || f == 8) cp_arm(L_Z1B + z8 + f * 512);
        if constexpr (f == 13) { WAITV(8); BAR0(); }
        if constexpr (f == 21 || f == 45) BAR0();
        if constexpr (f == 41 || f == 49) cp_arm(w * 7168 + (f - 41) * 512);
        if constexpr (f == PUBF) { WAITV(7); BAR0(); push(fv, fo); }
        if constexpr (f == 47) tmp = opq_v(L_CS1 + g * 16);
        if constexpr (f >= 56 && !(f & 1)) cs1[(f - 56) / 2] = ldsf4(tmp + (f - 56) * 32);
      },
          [&](auto fi) AI {
        constexpr int f = fi.v;
        if constexpr (f < 14 && !(f & 1)) cp_one<f / 2>(dws, l16, wz1 + z8 + f / 8 * 4096);
        if constexpr (f >= 42 && !(f & 1)) cp_one<(f - 42) / 2>(dws, l16, us + w * 7168 + (f - 42) / 8 * 4096);
        if constexpr (f == GLD) ga = ld16h<0>(dws, gro, sto);
        if constexpr (f == GLD + 2) gb = ld16h<1024>(dws, gro, sto);
        if constexpr (f == GLD + 14) {
          DRAIN(); pin(ga); pin(gb);
          for (unsigned spins = 0; spins < (1u << 18); ++spins) {
            if (__all(ga.y == GTAG && ga.w == GTAG && gb.y == GTAG && gb.w == GTAG)) break;
            asm volatile("" ::: "memory");
            ga = ld16<SC1>(rws, gro, sto); gb = ld16<SC1>(rws, gro + 1024, sto);
          }
          STAMP(sb + 10);
          float ts = asf(ga.x) + asf(gb.x), tq = asf(ga.z) + asf(gb.z);
          ts += dpp<0x128>(ts); tq += dpp<0x128>(tq); ts += dpp<0x124>(ts); tq += dpp<0x124>(tq);
          f32x2 zz = finish_stat(ts, tq, 1.f / 864.f);
          zz.x = -zz.x * zz.y;
          const f32x4 mm = {dpp<0x00>(zz.x), dpp<0x55>(zz.x), dpp<0xAA>(zz.x), dpp<0xFF>(zz.x)};
          const f32x4 rs = {dpp<0x00>(zz.y), dpp<0x55>(zz.y), dpp<0xAA>(zz.y), dpp<0xFF>(zz.y)};
          const int so = zo + w2 * 7168 + kh * 512;
          v2u d[7];
#pragma unroll
          for (int j = 0; j < 7; j++) d[j] = pk4(z2[j] * rs + mm);
          st16(join16(d[0], d[1]), rws, zvo, so);
          st16(join16(d[2], d[3]), rws, zvo + 2048, so);
          st16(join16(d[4], d[5]), rws, zvo, so + 4096);
          st16(join16(d[6], d[6]), rws, seloob(zvo, M_GEVEN) + 2048, (so + 4096) | k6z);
          STAMP(sb + 11);
        }
      });
      BAR();
      STAMP(sb + 12);
      touch(dws, seloob(tv, (1ull << 28) - 1), zo + w * 3584);
      xchg_read<7>(xr, acc);
      const int z0 = opq_s(act + A_Z + (cz * 32 + pan(0, pz, KQ)) * 14336);
      const int fno = opq_v(seloob(fl + F4 + (cz * 32 + (pz & 24) + 2 * KQ + l) * FS, 0x3ull));
      auto ldZ0 = [&](auto fi) AI { Z[fi.v] = ld16h<(fi.v & 3) * 1024>(dws, l16, (z0 + (fi.v >> 2) * 4096) | (fi.v % 7 == 6 ? k6z : 0)); };
      v2u fn_;
      sfor<0, 7>([&](auto ji) AI {
        constexpr int j = ji.v;
        if constexpr (j < 3) cp_run<(5 * j + 1) / 2, j == 2 ? 7 : (5 * j + 6) / 2>(dws, l16, us + 28672 + w * 7168, 28672 + w * 7168);
        if constexpr (j == NCHK - 2) fn_ = ld8<SC1>(rws, fno, 0);
        if constexpr (j == NCHK) { if (!__all(l >= 2 || (fn_.x == tag_lo && fn_.y == tag_hi))) poll(rws, fno, l >= 2, tag_lo, tag_hi); FENCE(); }
        if constexpr (j >= NCHK) sfor<4 * (j - NCHK), 4 * (j - NCHK) + 4>(ldZ0);
        SB();
        z1[j] = gelu4((acc[0][j] - m1 * cs1[j]) * r1);
        pin(z1[j]);
        SB();
      });
      STAMP(sb + 13);
      sfor<4 * (7 - NCHK), RZ>(ldZ0);
    }
    { LANE();
      const int ct0 = cz < 6 ? 7 * cz : 6 + 6 * cz, kq = opq_s(KQ);
      STAMP(sb + 14);
      f32x4 acc[4][7];
      const int so = act + A_G + (4 * pz + w) * GT + ct0 * 512;
      f32x4 bias = blo(braw);
      pin(bias);
      int zp[8], za, rw[3], rd, vo, rv; unsigned d[4];
      constexpr int RN = RZ + 1;
      v4u Y[RN];
      sfor<0, RZ>([&](auto fi) AI { Y[fi.v] = Z[fi.v]; });
      const int ut = opq_s((WHIT ? WQ_OFF : WU_OFF + co * WU_T) + 57344 + w * 10240);
      const int zb = opq_s(act + A_Z + cz * 32 * 14336), k6z = opq_s(ZPAD && cz >= 6 ? OOB : 0);
      zp[0] = opq_s(zb + pan(0, pz, kq) * 14336);
      const int fzo = opq_v(seloob(fl + F4 + (cz * 32 + (l >> 1) * 8 + 2 * kq + (l & 1)) * FS, 0xFFull));
      v2u fz;
      u64 dg[12];
      if constexpr (DIAG == 2) dg[0] = tick();
      NOP2();
      SB();
      sfor<0, 112>([&](auto fi) AI {
        constexpr int f = fi.v, s = sp_s(f), j = sp_j(f), fn = f + RZ;
        constexpr int q = f < 52 ? -1 : (f - 52) / 24, k = f < 52 ? 0 : (f - 52) % 24, buf = (q & 1) ? L_RED1 - L_RED0 : 0;
        auto fin = [&](auto ui) AI {
          constexpr int u = ui.v;
          if constexpr (q >= 0 && u >= 0 && u < 36) {
            constexpr int jj = 2 * q + (u < 32 ? (u >> 2) & 1 : (u - 32) >> 1), e = u & 3, st = u >> 3;
            if constexpr (st == 0) acc[0][jj][e] = gadd(acc[0][jj][e], acc[1][jj][e]);
            if constexpr (st == 1) acc[2][jj][e] = gadd(acc[2][jj][e], acc[3][jj][e]);
            if constexpr (st == 2) acc[0][jj][e] = gadd(acc[0][jj][e], acc[2][jj][e]);
            if constexpr (st == 3) acc[0][jj][e] = gmul(acc[0][jj][e], z1[jj][e]);
            if constexpr (st == 4) d[u - 32] = gcvt(acc[0][jj][(u & 1) * 2], acc[0][jj][(u & 1) * 2 + 1]);
          }
        };
        constexpr int per = q == 2 ? 7 : 4, u0 = (k - (q == 2 ? 9 : 10)) * per;
        const v4u z = Y[f % RN];
        WAITV(s_wait(f));
        mma<s == 0, true>(acc[1][j], z, W[1][s]);
        if constexpr (DIAG == 2 && (f == 0 || f == 14 || (f >= 28 && (f - 28) % 12 == 0 && f < 112))) dg[f == 0 ? 1 : f == 14 ? 2 : 3 + (f - 28) / 12] = tick();
        SB();
        if constexpr (fn < 28 && !(fn & 1)) za = opq_s(zp[0] + fn * 1024);
        if constexpr (fn >= 28 && fn < 112 && (fn >= 100 || !(fn & 1))) za = opq_s(zp[sp_s(fn) / 2] + ((sp_s(fn) & 1) * 7 + (sp_j(fn) & 6)) * 1024);
        if constexpr (q >= 0 && k >= 1 && k < 4) *lp<f32x4>(rw[k - 1] + buf) = acc[k][2 * q];
        if constexpr (q >= 0 && k >= 6 && k < 9) acc[k - 5][2 * q] = ldsf4(rd + buf + (k - 6) * 2048);
        fin(IC<u0>{});
        if constexpr (q == 2) fin(IC<u0 + 1>{});
        SB();
        mma<s == 0, true>(acc[2][j], z, W[2][s]);
        SB();
        if constexpr (fn < 112) Y[fn % RN] = ld16h<(fn < 100 ? fn & 1 : 0) * 1024>(dws, l16, za | (sp_j(fn) == 6 ? k6z : 0));
        if constexpr (f >= 98 && f < 108) cp_one<f - 98>(dws, l16, ut + (f - 98) / 4 * 4096);
        if constexpr (q == 2) { fin(IC<u0 + 2>{}); fin(IC<u0 + 3>{}); }
        SB();
        mma<s == 0, true>(acc[3][j], z, W[3][s]);
        SB();
        if constexpr (q >= 0 && k >= 2 && k < 5) *lp<f32x4>(rw[k - 2] + buf + 1024) = acc[k - 1][2 * q + 1];
        if constexpr (q >= 0 && k >= 6 && k < 9) acc[k - 5][2 * q + 1] = ldsf4(rd + buf + (k - 6) * 2048 + 1024);
        fin(IC<u0 + per - 3>{}); fin(IC<u0 + per - 2>{});
        if constexpr (f == 4) fz = ld8h<0>(dws, fzo, 0);
        if constexpr (f == 13) { WAITV(9); pin(fz); if (!__all(l >= 8 || (fz.x == tag_lo && fz.y == tag_hi))) poll(rws, fzo, l >= 8, tag_lo, tag_hi); }
        if constexpr (f >= 40 && f < 50 && !(f & 1)) rr[(f - 40) / 2] = ld8h<(f - 40) * 256>(dws, rv, act + A_X1 + (4 * po + w) * XT + co * 2560);
        if constexpr (f == 97 || f == 101 || f == 105) cp_arm(57344 + w * 10240 + (f - 97) * 1024);
        if constexpr (f % 4 == 2 && f / 4 + 2 < 8) zp[f / 4 + 2] = opq_s(pan(f / 4 + 2, pz, kq));
        if constexpr (f % 4 == 3 && f / 4 + 2 < 8) zp[f / 4 + 2] = opq_s(zb + zp[f / 4 + 2] * 14336);
        if constexpr (f == 31 || f == 33 || f == 35) rw[(f - 31) / 2] = opq_v(L_RED0 + ((((w + (f - 29) / 2) & 3) * 3 + (f - 31) / 2) * 2) * 1024 + l16);
        if constexpr (f == 37) rd = opq_v(L_RED0 + w * 6144 + l16);
        if constexpr (f == 24) vo = opq_v((g & 1) * 512 + c16 * 16);
        if constexpr (f == 26) vo = opq_v(vo + (g >> 1) * 256);
        if constexpr (f == 28) rv = opq_v(vo - (g & 1) * 504);
        if constexpr (q >= 0 && q < 2 && k == 20) { const v2u a = sw16(d[0], d[2]), e = sw16(d[1], d[3]); d[0] = a.x; d[2] = a.y; d[1] = e.x; d[3] = e.y; pin(d[0]); pin(d[1]); }
        if constexpr (q >= 0 && q < 2 && k == 21) st16(v4u{d[0], d[1], d[2], d[3]}, rws, vo + q * 1024, so);
        if constexpr (f == 39) BAR0();
        if constexpr (q >= 0 && k == 5) BAR();
        SB();
        if constexpr (s == 0) mma_c(acc[0][j], z, W[0][s], bias); else mma<false, true>(acc[0][j], z, W[0][s]);
        SB();
        fin(IC<u0 + per - 1>{});
        SB();
      });
#pragma unroll
      for (int e = 1; e < 4; e++) acc[0][5][e] = gadd(acc[0][5][e], acc[2][5][e]);
      if constexpr (DIAG == 2) { dg[10] = tick(); dg[11] = dg[10]; DUMP(); }
      const int rw2 = opq_v(L_RED2 + l16);
      asm volatile("s_nop 0" ::: "memory");
#pragma unroll
      for (int i = 1; i < 4; i++) pin(acc[i][6]);
      STAMP(sb + 15);
#pragma unroll
      for (int r = 1; r < 4; r++) *lp<f32x4>(rw2 + (((w + r) & 3) * 3 + r - 1) * 1024) = acc[r][6];
      st16(join16(pk4(acc[0][4] * z1[4]), pk4(acc[0][5] * z1[5])), rws, vo + 2048, so);
      BAR();
#pragma unroll
      for (int r = 1; r < 4; r++) acc[r][6] = ldsf4(rw2 + (w * 3 + r - 1) * 1024);
      pin(acc[0][6]);
      {
        const v2u dd = pk4(((acc[0][6] + acc[1][6]) + (acc[2][6] + acc[3][6])) * z1[6]);
        st16(join16(dd, dd), rws, seloob(vo, M_GEVEN) + 3072, cz < 6 ? so : OOB);
      }
      const int uc = 98304 + ((w * 9984) & ~1023);
      cp_run<0, 10>(dws, l16, (WHIT ? WQ_OFF : WU_OFF + co * WU_T) + uc, uc);
      const int fv = opq_v(seloob(l * FS, 0xFFull)), fo = opq_s(fl + F5 + pz * 8 * FS + cz * 8);
      WAITV(10);
      BAR();
      push(fv, fo);
      STAMP(sb + 16);
      touch(dws, l < (cz < 6 ? 28 : 24) ? l * 128 : OOB, so);
    }
    { LANE();
      f32x4 acc[2][5];
      const int vo = opq_v(L == 0 ? (g & 1) * 512 + (g >> 1) * 256 + c16 * 16 : c16 * 1280 + (g & 1) * 32 + (g >> 1) * 16);
      const int vo4 = opq_v(seloob(vo, M_GEVEN)), vs = opq_v(seloob(c16 * 8 + (g >> 1) * 4, M_GEVEN));
      const int xw = opq_v(L_X5 + (w ^ 1) * 5120 + l16), xr = opq_v(L_X5 + w * 5120 + l16);
      const int fl_ = LOCF ? co >> 2 : 0, sh6 = fl_ & kh;
      const int a0 = opq_s(act + A_G + (4 * po + w) * GT + kh * 7168), a1 = opq_s(act + A_G + (4 * po + (w ^ 1)) * GT + kh * 7168);
      const int dn = opq_s(fl_ * 14336), df = opq_s(14336 - fl_ * 14336 + sh6 * 1024), d6 = opq_s(sh6 ? 0 : fl_ * 14336 + 6144), d13 = opq_s((df + 6144) | (kh << 30));
      const int b0 = opq_v(l16 + kh * 5120), b1 = opq_v(b0 + 61440), b2 = opq_v(b0 + 122880), b3 = opq_v(l16 + 133120);
      const int qo = opq_s(qslot_off(w)), wq = opq_s(((WHIT ? WQ_OFF : WQ_OFF + h * WQ_T) + qo) | kill1);
      const int tile = opq_s(ACT_OFF + ACT_LAY + A_XIN + (4 * po + w) * XT);
      const int pv = opq_v(seloob(fl + F5 + (po * 8 + co) * FS + (kh * 2 + (l >> 1) * 2 + l) * 8, 0xFull));
      v4u A[2][14];
      gemm_m2k2<14, 5, 3, true, WINA, true, nv_u>([&](auto ti) AI { return ti.v < 6 ? b0 + ti.v * 10240 : ti.v < 12 ? b1 + (ti.v - 6) * 10240 : ti.v == 12 ? b2 : b3; }, xw, A, acc,
          [&]() AI {
#pragma unroll
        for (int j = 0; j < 5; j++) { acc[0][j] = unpk4(rr[j]); pin(acc[0][j]); }
        poll<PDLY>(rws, pv, l >= 4, tag_lo, tag_hi);
        STAMP(sb + 17);
      },
          [&](auto ii, auto ti) AI { constexpr int t = ti.v, u = t % 7; return ld16h<(u == 6 ? 0 : u & 3) * 1024>(dws, l16, (ii.v ? a1 : a0) + (t == 6 ? d6 : t == 13 ? d13 : (t < 7 ? dn : df) + (u >> 2) * 4096)); },
          [&](auto fi) AI {
        constexpr int f = fi.v;
        if constexpr (f == 21) BAR0();
        if constexpr (f == 43 || f == 51) cp_arm(qo + (f - 43) * 512);
        if constexpr (f == 55 || f == 59) cp_one<2 + (f - 55) / 4>(dws, l16, wq + 4096);
      },
          [&](auto fi) AI {
        constexpr int f = fi.v;
        if constexpr (f >= 44 && !(f & 1)) cp_one<(f - 44) / 2>(dws, l16, wq + (f - 44) / 8 * 4096);
      });
      STAMP(sb + 18);
      BAR();
      xchg_read<5>(xr, acc);
      if (L == 1) {
        const rsrc_t ro_ = mk(out, 2048 * 1280);
        v2u d[5];
#pragma unroll
        for (int j = 0; j < 5; j++) d[j] = pk4(acc[0][j]);
        const int so = (64 * po + 16 * w) * 1280 + co * 160;
        __builtin_amdgcn_raw_buffer_store_b128(join16(d[0], d[1]), ro_, vo, so, 0);
        __builtin_amdgcn_raw_buffer_store_b128(join16(d[2], d[3]), ro_, vo + 64, so, 0);
        __builtin_amdgcn_raw_buffer_store_b128(join16(d[4], d[4]), ro_, vo4 + 128, so, 0);
        flag(b == 0 ? 0 : OOB);
        __builtin_amdgcn_endpgm();
      }
      store_x(acc[0], rws, tile + co * 2560, tile + 20480 + co * 128, vo, vo4, vs);
      cp_run<0, 8>(dws, l16, wq + 30720, qo + 30720);
      const int fv = opq_v(l < 16 ? l * 2 * FS + (po & 1) * 64 : (l < 32 && (po & 1) && po < 31) ? (l * 2 + 1) * FS : OOB), fo = opq_s(fl + F6 + (po >> 1) * 32 * FS + co * 8);
      WAITV(8);
      BAR();
      push(fv, fo);
      STAMP(sb + 19);
      touch(dws, l < 20 ? co * 2560 + l * 128 : l == 20 ? 20480 + co * 128 : OOB, tile);
    }
  }
}
}

extern "C" void kernel_launch(void* const* d_in, const int* in_sizes, int n_in, void* d_out, int out_size, void* d_ws, size_t ws_size, hipStream_t stream) {
  (void)hipFuncSetAttribute((const void*)fused_kernel, hipFuncAttributeMaxDynamicSharedMemorySize, LDS_BYTES);
  fused_kernel<<<256, 256, LDS_BYTES, stream>>>((const char*)d_in[0], (const char*)d_in[1], (const char*)d_in[2], (const char*)d_in[4],
                                                (const char*)d_in[5], (const char*)d_in[6], (const char*)d_in[7], (char*)d_out, (char*)d_ws);
}
```

```cpp
#include <hip/hip_runtime.h>
#include <hip/hip_bf16.h>
#include <stdint.h>
#include <utility>

#define AI __attribute__((always_inline))
#define LAS __attribute__((address_space(3)))
#define GAS __attribute__((address_space(1)))
typedef unsigned v6u __attribute__((ext_vector_type(6)));
typedef unsigned v4u __attribute__((ext_vector_type(4)));
typedef unsigned v2u __attribute__((ext_vector_type(2)));
typedef int v4i __attribute__((ext_vector_type(4)));
typedef float f32x4 __attribute__((ext_vector_type(4)));
typedef float f32x2 __attribute__((ext_vector_type(2)));
typedef __bf16 bf2 __attribute__((ext_vector_type(2)));
typedef short s4 __attribute__((ext_vector_type(4)));
typedef unsigned long long u64;
typedef __amdgpu_buffer_rsrc_t rsrc_t;

#define PROF 0

namespace {
constexpr int SC1 = 16;
#define FS 128
constexpr int FARR = 256 * FS;
constexpr int F0R = 4096, F0B = F0R + FARR, FLAY = F0B + FARR;
constexpr int F1 = 0, F2 = FARR, F5 = 2 * FARR, F6 = 3 * FARR, F4 = 5 * FARR, FLAY_SZ = 6 * FARR;
constexpr int ST_OFF = 1 << 20;
constexpr int ST_LAY = 32 * 8192;
constexpr int CSQ_OFF = ST_OFF + 2 * ST_LAY;
constexpr int CSI_OFF = CSQ_OFF + 16 * 16 * 512;
constexpr int WQ_OFF = 2 << 20, WQ_T = 153600;
constexpr int WO_OFF = WQ_OFF + 16 * WQ_T, WO_T = 102400;
constexpr int WI_OFF = WO_OFF + 8 * WO_T, WI_T = 143360;
constexpr int WU_OFF = WI_OFF + 16 * WI_T, WU_T = 138240;
constexpr int ACT_OFF = WU_OFF + 8 * WU_T;
constexpr int XT = 21504, OT = 20480, GT = 27648;
constexpr int XSZ = 128 * XT, OSZ = 128 * OT, ZSZ = 8 * 64 * 7168, GSZ = 128 * GT;
constexpr int A_XIN = 0, A_O = A_XIN + XSZ, A_X1 = A_O + OSZ, A_Z = A_X1 + XSZ, A_G = A_Z + ZSZ, ACT_LAY = A_G + GSZ;
constexpr int PROF_OFF = ACT_OFF + 2 * ACT_LAY;
constexpr int WS_USED = PROF_OFF + 256 * 256 * 4;
static_assert(FLAY + 2 * FLAY_SZ <= ST_OFF, "");
static_assert(CSI_OFF + 16 * 16 * 512 <= WQ_OFF, "");
constexpr int OOB = 0x40000000;
constexpr int LDS_BYTES = 163840;
constexpr int L_TOP = 159744;
constexpr int L_CSQ = L_TOP, L_CS2 = L_CSQ + 512, L_CS1 = L_CS2 + 512, L_XST3 = L_CS1 + 512, L_JUNK = L_XST3 + 512, L_PROF = L_JUNK + 256;
constexpr int L_XST1 = 153600, L_HST = L_XST1 + 2048;
constexpr int L_HALO = 0, L_NB = 24576;
constexpr int L_WO = 57344, L_X2 = L_WO;
constexpr int L_Z2B = 73728 - 4 * 14336;
constexpr int L_Z1B = 59392 - 3 * 14336;
constexpr int L_X3 = 43008, L_X3B = 73728, L_X5 = 139264;
constexpr int L_RED0 = 98304, L_RED1 = L_RED0 + 24576, L_RED2 = L_RED1 + 24576;
static_assert(L_RED2 >= 138240 && L_RED2 + 12288 <= L_TOP, "");
#define MAPQ 1
#define MAPO 1
#define MAPZ 1
constexpr int SHQ = MAPQ ? 4 : 2, SHO = MAPO ? 8 : 4, SHZ = MAPZ ? 8 : 4;
#define LOCF 1
#define NTN 0
#define WPRE 8
#define PSLP 0
#define PDLY 0
#define FTCH 1
#define PAUX 16
#define FAUX 16
#define NCHK 5
#define GLD 24
#define PUBF 62
#define ZPAD 1
#define WHIT 0
#define REUSE 0
#define PRET 0
#define TCH 0
#define WINA 3
constexpr int CODE_BYTES = 41984;
constexpr unsigned GTAG = 0x5A5A5A5Bu;
constexpr unsigned ONE2 = 0x3f803f80u;

template <int I> struct IC { static constexpr int v = I; };
template <int B, class F, int... I> __device__ __forceinline__ void sfor_(F& f, std::integer_sequence<int, I...>) { (f(IC<B + I>{}), ...); }
template <int I, int N, class F> __device__ __forceinline__ void sfor(F&& f) {
  if constexpr (I < N) sfor_<I>(f, std::make_integer_sequence<int, N - I>{});
}
__device__ __forceinline__ int opq_s(int x) { asm volatile("" : "+s"(x)); return x; }
__device__ __forceinline__ int opq_u(int x) { return opq_s(__builtin_amdgcn_readfirstlane(x)); }
__device__ __forceinline__ int opq_v(int x) { asm volatile("" : "+v"(x)); return x; }
template <class T> __device__ __forceinline__ void keep(const T& c) { asm volatile("" ::"v"(c)); }
template <class T> __device__ __forceinline__ void pin(T& c) { asm volatile("" : "+v"(c)); }

template <bool FIRST> __device__ __forceinline__ void mma_aa(f32x4& c, v4u a, v4u b) {
  if constexpr (FIRST) asm volatile("v_mfma_f32_16x16x32_bf16 %0, %1, %2, 0" : "=&v"(c) : "a"(a), "v"(b));
  else asm volatile("v_mfma_f32_16x16x32_bf16 %0, %1, %2, %0" : "+v"(c) : "a"(a), "v"(b));
}
template <bool FIRST, bool BA> __device__ __forceinline__ void mma(f32x4& c, v4u a, v4u b) {
  if constexpr (FIRST) {
    if constexpr (BA) asm volatile("v_mfma_f32_16x16x32_bf16 %0, %1, %2, 0" : "=&v"(c) : "v"(a), "a"(b));
    else asm volatile("v_mfma_f32_16x16x32_bf16 %0, %1, %2, 0" : "=&v"(c) : "v"(a), "v"(b));
  } else {
    if constexpr (BA) asm volatile("v_mfma_f32_16x16x32_bf16 %0, %1, %2, %0" : "+v"(c) : "v"(a), "a"(b));
    else asm volatile("v_mfma_f32_16x16x32_bf16 %0, %1, %2, %0" : "+v"(c) : "v"(a), "v"(b));
  }
}
__device__ __forceinline__ void mma_c(f32x4& d, v4u a, v4u b, f32x4 c) { asm volatile("v_mfma_f32_16x16x32_bf16 %0, %1, %2, %3" : "=&v"(d) : "v"(a), "a"(b), "v"(c)); }
#define NOP2() asm volatile("s_nop 1")
__device__ __forceinline__ void mma_settle() { asm volatile("s_nop 7" ::: "memory"); }
__device__ __forceinline__ float dot2(unsigned a, unsigned b, float c) {
  return __builtin_amdgcn_fdot2_f32_bf16(__builtin_bit_cast(bf2, a), __builtin_bit_cast(bf2, b), c, false);
}
__device__ __forceinline__ unsigned pk(float lo, float hi) {
  f32x2 v = {lo, hi};
  return __builtin_bit_cast(unsigned, __builtin_convertvector(v, bf2));
}
__device__ __forceinline__ v2u pk4(f32x4 v) { return v2u{pk(v[0], v[1]), pk(v[2], v[3])}; }
__device__ __forceinline__ float blo(unsigned u) { return __builtin_bit_cast(float, u << 16); }
__device__ __forceinline__ float bhi(unsigned u) { return __builtin_bit_cast(float, u & 0xffff0000u); }
__device__ __forceinline__ f32x4 unpk4(v2u r) { return f32x4{blo(r.x), bhi(r.x), blo(r.y), bhi(r.y)}; }
__device__ __forceinline__ float asf(unsigned u) { return __builtin_bit_cast(float, u); }
__device__ __forceinline__ unsigned asu(float f) { return __builtin_bit_cast(unsigned, f); }
__device__ __forceinline__ unsigned sel(unsigned a, unsigned b, u64 m) { unsigned r; asm("v_cndmask_b32_e64 %0, %1, %2, %3" : "=v"(r) : "v"(a), "v"(b), "s"(m)); return r; }
__device__ __forceinline__ float self(float a, float b, u64 m) { return asf(sel(asu(a), asu(b), m)); }
__device__ __forceinline__ unsigned sel0(unsigned b, u64 m) { unsigned r; asm("v_cndmask_b32_e64 %0, 0, %1, %2" : "=v"(r) : "v"(b), "s"(m)); return r; }
__device__ __forceinline__ int seloob(int b, u64 m) { int r; asm("v_cndmask_b32_e64 %0, 2.0, %1, %2" : "=v"(r) : "v"(b), "s"(m)); return r; }
__device__ __forceinline__ float vmax(float a, float b) { return __builtin_elementwise_maximum(a, b); }
__device__ __forceinline__ float gadd(float a, float b) { float r; asm volatile("v_add_f32 %0, %1, %2" : "=v"(r) : "v"(a), "v"(b)); return r; }
__device__ __forceinline__ float gmul(float a, float b) { float r; asm volatile("v_mul_f32 %0, %1, %2" : "=v"(r) : "v"(a), "v"(b)); return r; }
__device__ __forceinline__ unsigned gcvt(float a, float b) { unsigned r; asm volatile("v_cvt_pk_bf16_f32 %0, %1, %2" : "=v"(r) : "v"(a), "v"(b)); return r; }
template <int C> __device__ __forceinline__ int gaddi(int a) { int r; asm volatile("v_add_u32 %0, %1, %2" : "=v"(r) : "n"(C), "v"(a)); return r; }
constexpr u64 M_ODD = 0xAAAAAAAAAAAAAAAAull, M_G0 = 0xFFFFull, M_G1 = 0xFFFF0000ull, M_GEVEN = 0x0000FFFF0000FFFFull, M_LO = 0xFFFFFFFFull;
constexpr u64 le_mask(int add) { u64 m = 0; for (int l = 0; l < 64; l++) if (4 * (l >> 4) + (l & 1) + add <= (l & 15)) m |= 1ull << l; return m; }
__device__ __forceinline__ rsrc_t mk(const void* p, int bytes) { return __builtin_amdgcn_make_buffer_rsrc((void*)p, 0, bytes, 0x00020000); }
__device__ __forceinline__ v4i mkd(const void* p, int bytes) {
  u64 a = (u64)p; v4i r; r.x = (int)(unsigned)a; r.y = (int)((unsigned)(a >> 32) & 0xffffu); r.z = bytes; r.w = 0x00020000; return r;
}
template <int AUX> __device__ __forceinline__ v4u ld16(rsrc_t r, int v, int s) { return __builtin_amdgcn_raw_buffer_load_b128(r, v, s, AUX); }
template <int AUX> __device__ __forceinline__ v2u ld8(rsrc_t r, int v, int s) { return __builtin_amdgcn_raw_buffer_load_b64(r, v, s, AUX); }
__device__ __forceinline__ void st16(v4u d, rsrc_t r, int v, int s) { __builtin_amdgcn_raw_buffer_store_b128(d, r, v, s, SC1); }
#define BAR() asm volatile("s_waitcnt lgkmcnt(0)\n\ts_barrier" ::: "memory")
#define BAR0() asm volatile("s_barrier" ::: "memory")
#define DRAIN() asm volatile("s_waitcnt vmcnt(0)" ::: "memory")
#define WAITV(n) asm volatile("s_waitcnt vmcnt(%0)" ::"n"(n) : "memory")
#define SB() __builtin_amdgcn_sched_barrier(0)
#define FENCE() do { asm volatile("" ::: "memory"); __builtin_amdgcn_sched_barrier(0); } while (0)
#define GL(o) "\n\tbuffer_load_dwordx4 %1, %2, %3 offen offset:" #o " sc1 lds"
#define GLARGS ::"s"(lds), "v"(voff), "s"(rs), "s"(soff)
#define NTW " nt"
#define NTX ""
__device__ __forceinline__ void glds_in(v4i rs, int voff, int soff, int lds) {
  asm volatile("s_mov_b32 m0, %0\n\ts_nop 0\n\tbuffer_load_dwordx4 %1, %2, %3 offen" NTW " lds" GLARGS : "memory");
}
#define NTS ""
__device__ __forceinline__ void glds_w(v4i rs, int voff, int soff, int lds) {
  asm volatile("s_mov_b32 m0, %0\n\ts_nop 0\n\tbuffer_load_dwordx4 %1, %2, %3 offen" NTS " lds" GLARGS : "memory");
}
__device__ __forceinline__ void touch_in(v4i rs, int voff, int soff, int lds) {
  asm volatile("s_mov_b32 m0, %0\n\ts_nop 0\n\tbuffer_load_dword %1, %2, %3 offen" NTX " lds" GLARGS : "memory");
}
template <bool FIRST> __device__ __forceinline__ void mma_gram(f32x4& c, v4u a) {
  if constexpr (FIRST) asm volatile("v_mfma_f32_16x16x32_bf16 %0, %1, %1, 0" : "=&v"(c) : "a"(a));
  else asm volatile("v_mfma_f32_16x16x32_bf16 %0, %1, %1, %0" : "+v"(c) : "a"(a));
}
template <int N> __device__ __forceinline__ void gldsn(v4i rs, int voff, int soff, int lds) {
  static_assert(N >= 1 && N <= 4, "");
  if constexpr (N == 1) asm volatile("s_mov_b32 m0, %0\n\ts_nop 0" GL(0) GLARGS : "memory");
  if constexpr (N == 2) asm volatile("s_mov_b32 m0, %0\n\ts_nop 0" GL(0) GL(1024) GLARGS : "memory");
  if constexpr (N == 3) asm volatile("s_mov_b32 m0, %0\n\ts_nop 0" GL(0) GL(1024) GL(2048) GLARGS : "memory");
  if constexpr (N == 4) asm volatile("s_mov_b32 m0, %0\n\ts_nop 0" GL(0) GL(1024) GL(2048) GL(3072) GLARGS : "memory");
}
template <int N0, int N1> __device__ __forceinline__ void cp_run(v4i rs, int voff, int src, int dst) {
  if constexpr (N0 < N1) {
    constexpr int n = N1 - N0 < 4 ? N1 - N0 : 4;
    gldsn<n>(rs, voff, src + N0 * 1024, dst + N0 * 1024);
    cp_run<N0 + n, N1>(rs, voff, src, dst);
  }
}
__device__ __forceinline__ void cp_arm(int lds) { asm volatile("s_mov_b32 m0, %0" ::"s"(lds) : "memory"); }
template <int U> __device__ __forceinline__ void cp_one(v4i rs, int voff, int soff) {
  asm volatile("buffer_load_dwordx4 %0, %1, %2 offen offset:%3 sc1 lds" ::"v"(voff), "s"(rs), "s"(soff), "n"((U & 3) * 1024) : "memory");
}
__device__ __forceinline__ void touch(v4i rs, int voff, int soff) {
  asm volatile("s_mov_b32 m0, %0\n\ts_nop 0\n\tbuffer_load_dword %1, %2, %3 offen sc1 lds" ::"s"(L_JUNK), "v"(voff), "s"(rs), "s"(soff) : "memory");
}
template <int OFF, bool WS> __device__ __forceinline__ void lda16(v4u& d, v4i rs, int voff, int soff) {
  if constexpr (WS) asm volatile("buffer_load_dwordx4 %0, %1, %2, %3 offen offset:%4 sc1" : "+a"(d) : "v"(voff), "s"(rs), "s"(soff), "n"(OFF) : "memory");
  else asm volatile("buffer_load_dwordx4 %0, %1, %2, %3 offen offset:%4" : "+a"(d) : "v"(voff), "s"(rs), "s"(soff), "n"(OFF) : "memory");
}
template <int OFF> __device__ __forceinline__ void sta16(const v4u& d, v4i rs, int voff, int soff) {
  asm volatile("buffer_store_dwordx4 %0, %1, %2, %3 offen offset:%4 sc1" ::"a"(d), "v"(voff), "s"(rs), "s"(soff), "n"(OFF) : "memory");
}
template <int OFF> __device__ __forceinline__ void ldsa16(v4u& d, int addr) { asm volatile("ds_read_b128 %0, %1 offset:%2" : "+a"(d) : "v"(addr), "n"(OFF) : "memory"); }
__device__ __forceinline__ void park(v4u& d, v4u v) { asm volatile("" : "=a"(d) : "0"(v)); }
__device__ __forceinline__ void tch(v4i rs, int voff, int soff) {
  asm volatile("buffer_load_dword %0, %1, %2 offen sc1 lds" ::"v"(voff), "s"(rs), "s"(soff) : "memory");
}
template <int OFF> __device__ __forceinline__ v4u ld16h(v4i rs, int voff, int soff) {
  v4u r; asm volatile("buffer_load_dwordx4 %0, %1, %2, %3 offen offset:%4 sc1" : "=v"(r) : "v"(voff), "s"(rs), "s"(soff), "n"(OFF) : "memory"); return r;
}
template <int OFF> __device__ __forceinline__ v2u ld8h(v4i rs, int voff, int soff) {
  v2u r; asm volatile("buffer_load_dwordx2 %0, %1, %2, %3 offen offset:%4 sc1" : "=v"(r) : "v"(voff), "s"(rs), "s"(soff), "n"(OFF) : "memory"); return r;
}
template <int OFF> __device__ __forceinline__ float ld4h(v4i rs, int voff, int soff) {
  float r; asm volatile("buffer_load_dword %0, %1, %2, %3 offen offset:%4 sc1" : "=v"(r) : "v"(voff), "s"(rs), "s"(soff), "n"(OFF) : "memory"); return r;
}
__device__ __forceinline__ int qslot_off(int w) { return (w * 7680) & ~1023; }
template <class T> __device__ __forceinline__ LAS T* lp(int off) { return (LAS T*)(size_t)(unsigned)off; }
__device__ __forceinline__ v4u lds16(int off) { return *lp<v4u>(off); }
__device__ __forceinline__ v2u lds8(int off) { return *lp<v2u>(off); }
__device__ __forceinline__ f32x4 ldsf4(int off) { return *lp<f32x4>(off); }
__device__ __forceinline__ float ldsf(int off) { return *lp<float>(off); }
template <int DLY = 0> __device__ __forceinline__ void poll(rsrc_t rws, int voff, bool off, unsigned lo, unsigned hi) {
  if (DLY) __builtin_amdgcn_s_sleep(DLY);
  for (unsigned spins = 0; spins < (1u << 18); ++spins) {
    asm volatile("" ::: "memory");
    const v2u v = ld8<PAUX>(rws, voff, 0);
    if (__all(off || (v.x == lo && v.y == hi))) break;
    if (PSLP) __builtin_amdgcn_s_sleep(PSLP);
  }
  asm volatile("" ::: "memory");
}
__device__ __forceinline__ v2u sw16(unsigned a, unsigned b) { auto r = __builtin_amdgcn_permlane16_swap(a, b, false, false); return v2u{r[0], r[1]}; }
__device__ __forceinline__ v2u sw32(unsigned a, unsigned b) { auto r = __builtin_amdgcn_permlane32_swap(a, b, false, false); return v2u{r[0], r[1]}; }
__device__ __forceinline__ v4u join16(v2u x, v2u y) { const v2u a = sw16(x.x, y.x), b = sw16(x.y, y.y); return v4u{a.x, b.x, a.y, b.y}; }
template <int CTRL> __device__ __forceinline__ float dpp(float v) {
  return __builtin_bit_cast(float, __builtin_amdgcn_update_dpp(0, __builtin_bit_cast(int, v), CTRL, 0xf, 0xf, true));
}
__device__ __forceinline__ float xsum2(float s, float q) {
  const v2u a = sw32(asu(s), asu(q)); const float t = asf(a.x) + asf(a.y);
  const v2u b = sw16(asu(t), asu(t)); return asf(b.x) + asf(b.y);
}
__device__ __forceinline__ f32x2 finish_stat(float s, float q, float inv_n) {
  const float m = s * inv_n;
  return f32x2{m, __builtin_amdgcn_rsqf(fmaxf(q * inv_n - m * m, 0.f) + 1e-5f)};
}
__device__ __forceinline__ f32x4 gelu4(f32x4 v) {
  const float k1 = -2.0f * 0.7978845608028654f * 1.4426950408889634f, k2 = k1 * 0.044715f;
  f32x4 e = v * ((v * v) * k2 + k1);
  asm("" : "+v"(e));
#pragma unroll
  for (int i = 0; i < 4; i++) e[i] = __builtin_amdgcn_exp2f(e[i]);
  e += 1.0f;
#pragma unroll
  for (int i = 0; i < 4; i++) e[i] = __builtin_amdgcn_rcpf(e[i]);
  return v * e;
}
__device__ __forceinline__ f32x2 rowsum8(f32x4 s, f32x4 q) {
  float a, b, c, d;
  asm volatile("s_nop 1\n\t"
      "v_add_f32_dpp %0, %4, %4 row_ror:8 row_mask:0xf bank_mask:0x3\n\t"
      "v_add_f32_dpp %1, %5, %5 row_ror:8 row_mask:0xf bank_mask:0x3\n\t"
      "v_add_f32_dpp %2, %8, %8 row_ror:8 row_mask:0xf bank_mask:0x3\n\t"
      "v_add_f32_dpp %3, %9, %9 row_ror:8 row_mask:0xf bank_mask:0x3\n\t"
      "v_add_f32_dpp %0, %6, %6 row_ror:8 row_mask:0xf bank_mask:0xc\n\t"
      "v_add_f32_dpp %1, %7, %7 row_ror:8 row_mask:0xf bank_mask:0xc\n\t"
      "v_add_f32_dpp %2, %10, %10 row_ror:8 row_mask:0xf bank_mask:0xc\n\t"
      "v_add_f32_dpp %3, %11, %11 row_ror:8 row_mask:0xf bank_mask:0xc\n\t"
      "v_add_f32_dpp %0, %0, %0 row_half_mirror row_mask:0xf bank_mask:0x5\n\t"
      "v_add_f32_dpp %0, %1, %1 row_half_mirror row_mask:0xf bank_mask:0xa\n\t"
      "v_add_f32_dpp %2, %2, %2 row_half_mirror row_mask:0xf bank_mask:0x5\n\t"
      "v_add_f32_dpp %2, %3, %3 row_half_mirror row_mask:0xf bank_mask:0xa\n\t"
      "v_add_f32_dpp %0, %0, %0 quad_perm:[1,0,3,2] row_mask:0xf bank_mask:0xf\n\t"
      "s_nop 0\n\t"
      "v_add_f32_dpp %2, %2, %2 quad_perm:[1,0,3,2] row_mask:0xf bank_mask:0xf\n\t"
      "v_add_f32_dpp %0, %0, %0 quad_perm:[2,3,0,1] row_mask:0xf bank_mask:0xf\n\t"
      "s_nop 0\n\t"
      "v_add_f32_dpp %2, %2, %2 quad_perm:[2,3,0,1] row_mask:0xf bank_mask:0xf"
      : "=&v"(a), "=&v"(b), "=&v"(c), "=&v"(d)
      : "v"(s[0]), "v"(s[1]), "v"(s[2]), "v"(s[3]), "v"(q[0]), "v"(q[1]), "v"(q[2]), "v"(q[3]));
  return f32x2{a, c};
}
__device__ __forceinline__ v4u bperm4(int idx, v4u v) {
  v4u r;
  r.x = __builtin_amdgcn_ds_bpermute(idx, v.x); r.y = __builtin_amdgcn_ds_bpermute(idx, v.y);
  r.z = __builtin_amdgcn_ds_bpermute(idx, v.z); r.w = __builtin_amdgcn_ds_bpermute(idx, v.w);
  return r;
}
__device__ __forceinline__ v2u trd(int off) { return __builtin_bit_cast(v2u, __builtin_amdgcn_ds_read_tr16_b64_v4i16(lp<s4>(off))); }
#define STAMP(i) do {} while (0)
#define CSTAMP(i) do {} while (0)
#define LANE() const int l = opq_v((int)threadIdx.x) & 63, l16 = l * 16, g = l >> 4, c16 = l & 15; (void)g; (void)c16; (void)l16; \
  const int w = opq_s(w_), po = opq_s(po_), co = opq_s(co_), pz = opq_s(pz_), cz = opq_s(cz_), P = opq_s(P_), h = opq_s(h_); \
  (void)w; (void)po; (void)co; (void)pz; (void)cz; (void)P; (void)h; \
  const int kh = w & 1, w2 = w >> 1; (void)kh; (void)w2; \
  auto flag = [&](int off) AI { __builtin_amdgcn_raw_buffer_store_b64(v2u{tag_lo, tag_hi}, rws, seloob(0, 1ull), w == 0 ? off : OOB, FAUX); }; (void)flag; \
  auto push = [&](int voff, int off) AI { __builtin_amdgcn_raw_buffer_store_b64(v2u{tag_lo, tag_hi}, rws, voff, w == 0 ? off : OOB, SC1); }; (void)push;
#define QROT (2 * cz)
#define KQ ((w + QROT) & 3)
#define RROT 3
__device__ __forceinline__ int pan(int e, int pz, int kq) {
  int r = e - 2 + RROT * (pz & 1); r = r >= 6 ? r - 6 : r;
  const int u = e < 2 ? e : r + 2;
  return (((pz >> 3) + (u >> 1)) & 3) * 8 + 2 * kq + (u & 1);
}

#define DIAG 0
__device__ __forceinline__ u64 tick() { u64 t; asm volatile("s_memtime %0" : "=s"(t)); return t; }
#define DUMP() do { asm volatile("s_waitcnt lgkmcnt(0)" ::: "memory"); _Pragma("unroll") for (int i = 0; i < 12; i++) { asm volatile("" : "+s"(dg[i])); *lp<unsigned>(L_PROF + w * 256 + (50 + i) * 4) = (unsigned)dg[i]; } } while (0)
template <int NS, int NCT, int WIN, int (*NV)(int)> constexpr int younger(int t, int fe) {
  const int n = 2 * t + 1, f0 = n < 2 * WIN ? -1 : (n - 2 * WIN) * 2;
  int c = n < 2 * WIN ? 2 * WIN - 1 - n : 0;
  for (int f = f0 + 1; f < fe; f++) c += NV(f) + (f % 2 == 0 && 2 * WIN + f / 2 < 2 * NS);
  return c;
}
template <int NS, int NCT, int TL, bool TR, int WIN, bool INIT, int (*NV)(int), class BA, class GO, class LD, class HA, class HB>
__device__ __forceinline__ void gemm_m2k2(BA&& badr, int xw, v4u (&A)[2][NS], f32x4 (&acc)[2][NCT], GO&& go, LD&& lda, HA&& ha, HB&& hb) {
  constexpr int NF = NS * NCT, NT = TL * NCT, NL = NF - NT;
  static_assert(NF % 2 == 0 && (WIN == 0 || 4 * (NS - WIN) <= NL + 1), "");
  v4u B[NF];
  auto ldB = [&](auto fi) AI { constexpr int f = fi.v; if constexpr (f < NF) B[f] = lds16(badr(IC<f / NCT>{}) + (f % NCT) * 1024); };
  auto mm = [&](auto ii, auto fi) AI {
    constexpr int i = ii.v, f = fi.v, t = f / NCT, ct = f % NCT;
    if (TR) mma<t == 0 && !(INIT && i == 0), false>(acc[i][ct], B[f], A[i][t]); else mma<t == 0 && !(INIT && i == 0), false>(acc[i][ct], A[i][t], B[f]);
    SB();
  };
  sfor<0, 6>([&](auto fi) AI { ldB(IC<(fi.v ^ 1)>{}); SB(); });
  go();
  if constexpr (WIN > 0) sfor<0, WIN>([&](auto ti) AI { A[1][ti.v] = lda(IC<1>{}, ti); A[0][ti.v] = lda(IC<0>{}, ti); });
  SB();
  sfor<0, NL>([&](auto fi) AI {
    constexpr int f = fi.v;
    if constexpr (WIN > 0 && f % NCT == 0) WAITV((younger<NS, NCT, WIN, NV>(f / NCT, f)));
    mm(IC<0>{}, fi);
    ldB(IC<((f + 6) ^ 1)>{});
    ha(fi);
    SB();
    mm(IC<1>{}, fi);
    if constexpr (WIN > 0) {
      constexpr int n = 2 * WIN + f / 2;
      if constexpr (f % 2 == 0 && n < 2 * NS) A[(n & 1) ^ 1][n >> 1] = lda(IC<((n & 1) ^ 1)>{}, IC<(n >> 1)>{});
    }
    if constexpr (f >= NL - 4) ldB(IC<((f + 10) ^ 1)>{});
    hb(fi);
    SB();
  });
  sfor<NL, NF>([&](auto fi) AI {
    constexpr int f = fi.v;
    if constexpr (WIN > 0 && f % NCT == 0) WAITV((younger<NS, NCT, WIN, NV>(f / NCT, NL)));
    mm(IC<1>{}, fi);
    ldB(IC<((f + 10) ^ 1)>{});
    SB();
  });
  sfor<NL, NF>([&](auto fi) AI {
    constexpr int k = fi.v - NL, D = NCT == 5 ? 3 : 1, SP = TL == 1 ? 1 : 2;
    mm(IC<0>{}, fi);
    if constexpr (k >= D && (k - D) % SP == 0 && (k - D) / SP < NCT) { pin(acc[1][(k - D) / SP]); *lp<f32x4>(xw + (k - D) / SP * 1024) = acc[1][(k - D) / SP]; }
    else ha(fi);
    if constexpr (k == NT - 1) sfor<(NT - 1 - D) / SP + 1, NCT>([&](auto ki) AI { pin(acc[1][ki.v]); *lp<f32x4>(xw + ki.v * 1024) = acc[1][ki.v]; });
    SB();
  });
}
template <int NCT> __device__ __forceinline__ void xchg_read(int src, f32x4 (&acc)[2][NCT]) {
#pragma unroll
  for (int j = 0; j < NCT; j++) acc[1][j] = ldsf4(src + j * 1024);
  SB();
#pragma unroll
  for (int j = 0; j < NCT; j++) { pin(acc[0][j]); acc[0][j] += acc[1][j]; }
}
__device__ __forceinline__ void store_x(const f32x4 (&x)[5], rsrc_t rws, int so, int ss, int vo, int vo4, int vs) {
  v2u d[5];
#pragma unroll
  for (int j = 0; j < 5; j++) d[j] = pk4(x[j]);
  float s0 = 0.f, q0 = 0.f;
#pragma unroll
  for (int j = 0; j < 5; j++) { s0 = dot2(d[j].x, ONE2, s0); q0 = dot2(d[j].x, d[j].x, q0); s0 = dot2(d[j].y, ONE2, s0); q0 = dot2(d[j].y, d[j].y, q0); }
  st16(join16(d[0], d[1]), rws, vo, so);
  st16(join16(d[2], d[3]), rws, vo + 1024, so);
  st16(join16(d[4], d[4]), rws, vo4 + 2048, so);
  __builtin_amdgcn_raw_buffer_store_b32(asu(xsum2(s0, q0)), rws, vs, ss, SC1);
}
constexpr int nv_none(int) { return 0; }
constexpr int nv_o(int f) { return (f >= 17 && f < 25 && (f & 1)) + (f >= 32 && f < 35); }
constexpr int nv_z(int f) { return (f == 1 || f == 3) + (f >= 32 && f % 3 == 2); }
constexpr int nv_u(int f) { return f >= 44 && !(f & 1); }
#define QPRE 7
#define QWSP 2
#define QAHEAD 6
constexpr int q_step(int n) { return n % 9 == 0 ? n / 9 * 4 : n / 9 * 4 + (n % 9 - 1) / 2; }
constexpr int q_si(int f) { return f >= 57 && f < 89 && f % 4 == 1 ? (f - 57) / 4 : -1; }
struct QTab { int ld[160], wu[160], ou[160], cum[161], wt[20], ws[3]; };
constexpr QTab q_tab() {
  QTab t{}; int n = QPRE, u = 0, o = 0, lastw[3] = {0, 0, 0};
  for (int i = 0; i < 160; i++) {
    t.ld[i] = t.wu[i] = t.ou[i] = -1;
    if (i % 2 == 0) {
      const int da = n < 45 ? 8 * q_step(n) : 1000, dw = u < 24 ? 58 + u / 8 * 32 - 2 * (7 - u % 8) * QWSP : 1000;
      if (da <= dw && n < 45 && q_step(n) <= i / 8 + QAHEAD) t.ld[i] = n++;
      else if (u < 24) { lastw[u / 8] = i; t.wu[i] = u++; }
      else if (i >= 124 && o < 8 && i % 4 == 0) t.ou[i] = o++;
    }
    t.cum[i + 1] = t.cum[i] + (t.ld[i] >= 0) + (t.wu[i] >= 0) + (t.ou[i] >= 0) + (q_si(i) >= 0) + (i >= 110 && i < 130 && i % 4 == 3) + (i == 45 || i == 49 || i == 53);
  }
  for (int s = 0; s < 20; s++) {
    const int last = s / 4 * 9 + 2 + (s & 3) * 2;
    int f0 = -1;
    for (int i = 0; i < 160; i++) if (t.ld[i] == last) f0 = i;
    t.wt[s] = (last < QPRE ? QPRE - 1 - last : 0) + t.cum[8 * s] - t.cum[f0 + 1];
  }
  for (int k = 0; k < 3; k++) t.ws[k] = t.cum[58 + 32 * k] - t.cum[lastw[k] + 1];
  return t;
}
constexpr QTab QT = q_tab();
constexpr int s_vm(int f) { return (f == 73 || f == 97) + (f == 4) + (f >= 40 && f < 50 && !(f & 1)) + (f >= 98 && f < 108); }
constexpr int s_wait(int f) { int c = f < 14 ? 13 - f : 0; for (int i = f < 14 ? 0 : f - 13; i < f; i++) c += (i < 98) + s_vm(i); return c; }
constexpr int sp_s(int f) { return f < 28 ? f / 7 : f < 100 ? 4 + ((f - 28) % 24) / 2 : 4 + (f - 100); }
constexpr int sp_j(int f) { return f < 28 ? f % 7 : f < 100 ? (f - 28) / 24 * 2 + (f & 1) : 6; }

__global__ __launch_bounds__(256, 1) void fused_kernel(const char* __restrict__ x0, const char* __restrict__ wqkv, const char* __restrict__ wo,
                                                       const char* __restrict__ win, const char* __restrict__ wsp, const char* __restrict__ bsp,
                                                       const char* __restrict__ wout, char* out, char* ws) {
  {
    u64 pc; asm volatile("s_getpc_b64 %0" : "=s"(pc));
    const int i = (blockIdx.x >> 3) * 16 + (threadIdx.x >> 4);
    touch_in(mkd((const void*)(pc & ~127ull), CODE_BYTES), (threadIdx.x & 15) == 0 ? i * 128 : OOB, 0, L_JUNK);
  }
  const int w_ = __builtin_amdgcn_readfirstlane(threadIdx.x >> 6);
  const int b = blockIdx.x, xcd = b & 7, jb = b >> 3;
  const int pa = xcd * 4 + (jb & 3), ca = jb >> 2, pb = (xcd >> 1) * 8 + (jb & 7), cb = (xcd & 1) * 4 + (jb >> 3);
  const int po_ = MAPO ? pb : pa, co_ = MAPO ? cb : ca, pz_ = MAPZ ? pb : pa, cz_ = MAPZ ? cb : ca;
  const int P_ = MAPQ ? (xcd >> 1) * 4 + (jb & 3) : xcd * 2 + (jb & 1), h_ = MAPQ ? (xcd & 1) * 8 + (jb >> 2) : jb >> 1;
  const rsrc_t rws = mk(ws, WS_USED);
  const v4i dws = mkd(ws, WS_USED);
  STAMP(0); CSTAMP(62);
  unsigned tag_lo, tag_hi;
  v4u W[4][16];
#pragma unroll
  for (int r = 0; r < 4; r++)
#pragma unroll
    for (int s = 0; s < 16; s++) asm volatile("" : "=a"(W[r][s]));
  {
    LANE();
    {
      const v4i dq = mkd(wqkv, 640 * 3840);
      const int vq = g * 3840 + c16 * 16 + (c16 < 5 ? 1280 : c16 < 10 ? -80 : 2400), s0 = (P * 40 + w * 12) * 3840 + 80 * h, kill = w == 3 ? OOB : 0;
      glds_in(dq, vq, s0, w * 3072);
      glds_in(dq, vq, (s0 + 4 * 3840) | kill, w * 3072 + 1024);
      glds_in(dq, vq, (s0 + 8 * 3840) | kill, w * 3072 + 2048);
    }
    const int xrt = 8 * P + (h >> 1), xc = 4 * (h & 1) + w;
    {
      const v4i dx = mkd(x0, 2048 * 1280);
#pragma unroll
      for (int i = 0; i < 4; i++) {
        const int k = (c16 - g - 4 * i) & 15;
        glds_in(dx, k < 10 ? g * 1280 + k * 16 : OOB, (xrt * 16 + 4 * i) * 1280 + xc * 160, 12288 + w * 4096 + i * 1024);
      }
    }
    float ep0 = ld4h<0>(dws, 0, 0), ep1 = ld4h<4>(dws, 0, 0);
    const v4i dw = mkd(wsp, 2048 * 4096);
    const int wst = opq_s(57344 + w * 25344), wb0 = opq_s(64 * pz * 4096);
    int wv[2];
#pragma unroll
    for (int i = 0; i < 2; i++) wv[i] = opq_v((l >> 5) * 4096 + pan(4 * i + ((l >> 3) & 3), pz, KQ) * 128 + ((l & 7) ^ (l >> 5)) * 16);
    auto ldW = [&](auto ni) AI { constexpr int n = ni.v; const int so = wb0 + ((w + (n >> 4)) & 3) * 65536 + (n & 7) * 8192, ld_ = wst + (n % 24) * 1056; if constexpr (n < NTN) glds_in(dw, wv[(n >> 3) & 1], so, ld_); else glds_w(dw, wv[(n >> 3) & 1], so, ld_); };
    const int wrd = opq_v(wst + (c16 >> 1) * 1056 + (c16 & 1) * 512 + (g ^ (c16 & 1)) * 16);
    auto rdW = [&](auto ni) AI { constexpr int n = ni.v; ldsa16<(n % 24 >> 3) * 8448 + (n & 7) * 64>(W[n >> 4][n & 15], wrd); };
    sfor<0, 8>(ldW);
    const int tau = jb, sl = xcd, stg = 28672 + w * 7168;
    const int ty = tau < 8 ? 0 : tau < 24 ? 1 : 2, ot = tau - ty * 8 - (ty >> 1) * 8, cc = ot & 7;
    const int o_kg = opq_u((ty < 2 ? 10 * sl : sl < 4 ? 14 * sl : 4 + 13 * sl) + w2), o_n = opq_u((ty < 2 ? 10 : sl < 4 ? 14 : 13) - w2);
    const int ldb = ty == 1 ? 3456 : 1280, lim = ty != 1 ? 9 : cc < 6 ? 13 : 11, chi = kh * 8 + (l & 7);
    const v4i dsrc = mkd(ty == 0 ? wo : ty == 1 ? win : wout, (ty == 2 ? 864 : 640) * ldb);
    const int ov = opq_v(ZPAD ? (chi <= lim ? (l >> 3) * ldb + chi * 16 : OOB) : (l >> 3) * ldb + (chi < lim ? chi : lim) * 16);
    const int o0 = opq_u(o_kg * 8 * ldb + (ty != 1 ? ot * 160 : (ot < 8 ? 1728 : 0) + (cc < 6 ? 7 * cc : 6 + 6 * cc) * 32));
    const int od = opq_u(16 * ldb), k5 = opq_u(o_n > 10 ? 0 : OOB), k6 = opq_u(o_n > 12 ? 0 : OOB);
    const int o_v = opq_v(kh * 4 + g < (ty == 1 ? 7 : 5) ? (kh * 4 + g) * 1024 + c16 * 16 : OOB);
    int o_d;
    {
      const int o_dst = ty == 0 ? WO_OFF + ot * WO_T : ty == 1 ? WI_OFF + ot * WI_T : WU_OFF + ot * WU_T, kb = ty < 2 ? 10 : 14, kstr = ty == 1 ? 7168 : 5120;
      const int kap = o_kg + 2 * l, ks = kap >> 2, hi = ks >= kb, kq = ks - (hi ? kb : 0), hs = kb >> 1, kw = kq >= hs, far = hi != (LOCF && cc >= 4);
      const int pos = 2 * (kq - (kw ? hs : 0) + (far ? hs - (ty == 2 && kw && !hi) : 0)) + kw;
      o_d = opq_v(2 * l < o_n ? o_dst + pos * kstr + (kap & 3) * 256 : OOB);
    }
    const int o_c = opq_u(ty == 1 ? CSI_OFF + (ot * 16 + sl * 2 + w2) * 512 + kh * 256 : OOB);
    asm volatile("s_nop 4");
#pragma unroll
    for (int k = 0; k < 7; k++) glds_in(dsrc, ov, (o0 + k * od) | (k == 5 ? k5 : k == 6 ? k6 : 0), stg + k * 1024);
    touch(dws, seloob(F0B + l * FARR + b * FS, 0x1FFFull), w == 1 ? 0 : OOB);
    const int ct = 2 * w + (g & 1), pq = l & 3;
    const int cb_ = ct == 2 ? 32 + (pq & 1) * 8 + (pq >> 1) * 80 : ct == 7 ? 192 + (pq & 1) * 8 : (ct < 2 ? ct * 48 : ((ct - 1) >> 1) * 80 + ((ct - 1) & 1) * 48) + pq * 8;
    const int trq = opq_v(((g >> 1) * 8 + (c16 >> 2)) * 256 + cb_);
    int qv[3], xa[3];
#pragma unroll
    for (int i = 0; i < 3; i++) {
      const int k = 2 * i + (g >> 1), kap = 5 * P + k;
      const int kr_ = (kap >> 2) + (LOCF && h >= 8 ? (kap >= 48 ? -12 : kap < 32 ? 12 : 0) : 0);
      qv[i] = opq_v((k < 5 && (ct < 7 || c16 < 8)) ? kr_ * 7680 + ct * 1024 + ((kap & 3) * (ct == 7 ? 8 : 16) + c16) * 16 : OOB);
      xa[i] = opq_v(12288 + w * 4096 + c16 * 256 + ((i * 4 + g + c16) & 15) * 16);
    }
    sfor<8, 16>(ldW);
    WAITV(24);
    pin(ep0); pin(ep1);
    STAMP(1);
    BAR0();
    {
      v4u xf[3]; v2u lo[3], hi[3];
#pragma unroll
      for (int i = 0; i < 3; i++) { lo[i] = trd(trq + i * 4096); hi[i] = trd(trq + i * 4096 + 1024); xf[i] = lds16(xa[i]); }
      SB();
      const int tile = ACT_OFF + A_XIN + xrt * XT;
#pragma unroll
      for (int i = 0; i < 3; i++) {
        st16(v4u{lo[i].x, lo[i].y, hi[i].x, hi[i].y}, rws, qv[i], WQ_OFF + h * WQ_T);
        st16(xf[i], rws, (i == 2 ? seloob(l16, M_LO) : l16) + i * 1024, tile + xc * 2560);
      }
      float s = 0.f, q = 0.f, cs = 0.f;
#pragma unroll
      for (int i = 0; i < 3; i++) {
#pragma unroll
        for (int e = 0; e < 4; e++) { s = dot2(xf[i][e], ONE2, s); q = dot2(xf[i][e], xf[i][e], q); }
        cs = dot2(lo[i].x, ONE2, cs); cs = dot2(lo[i].y, ONE2, cs); cs = dot2(hi[i].x, ONE2, cs); cs = dot2(hi[i].y, ONE2, cs);
      }
      __builtin_amdgcn_raw_buffer_store_b32(asu(xsum2(s, q)), rws, seloob(c16 * 8 + (g >> 1) * 4, M_GEVEN), tile + 20480 + xc * 128, SC1);
      const v2u t = sw32(asu(cs), asu(cs));
      __builtin_amdgcn_raw_buffer_store_b32(asu(asf(t.x) + asf(t.y)), rws, seloob(l * 4, M_LO), CSQ_OFF + (h * 16 + P) * 512 + w * 128, SC1);
    }
    FENCE();
    sfor<16, 24>(ldW);
    {
      const u64 epoch = ((u64)(unsigned)__builtin_amdgcn_readfirstlane(asu(ep1)) << 32) | (unsigned)__builtin_amdgcn_readfirstlane(asu(ep0));
      const u64 tag = epoch * 0x9E3779B97F4A7C15ull + 0xD1B54A32D192ED03ull;
      tag_lo = opq_s((unsigned)tag); tag_hi = opq_s((unsigned)(tag >> 32));
    }
#define LGK0() asm volatile("s_waitcnt lgkmcnt(0)" ::: "memory")
    WAITV(32); sfor<0, 8>(rdW); LGK0(); sfor<24, 32>(ldW);
    WAITV(24); sfor<8, 16>(rdW);
    {
      const int tro = stg + (c16 >> 2) * 128 + g * 32 + (l & 3) * 8;
      float ocs = 0.f;
#pragma unroll
      for (int i = 0; i < 7; i++) {
        const v2u lo = trd(tro + i * 1024), hi2 = trd(tro + i * 1024 + 512);
        if (i < 5) { ocs = dot2(lo.x, ONE2, ocs); ocs = dot2(lo.y, ONE2, ocs); ocs = dot2(hi2.x, ONE2, ocs); ocs = dot2(hi2.y, ONE2, ocs); }
        if (i == 0) { WAITV(16); BAR0(); flag(F0R + (P * 16 + h) * FS); STAMP(2); FENCE(); }
        st16(v4u{lo.x, lo.y, hi2.x, hi2.y}, rws, o_v, __builtin_amdgcn_readlane(o_d, i));
      }
      __builtin_amdgcn_raw_buffer_store_b32(asu(ocs), rws, l * 4, o_c, SC1);
      st16(v4u{0, 0, 0, 0}, rws, seloob(g * ST_LAY + c16 * 16, M_LO), ST_OFF + po * 8192 + w * 2048 + co * 256);
    }
    FENCE();
    LGK0(); sfor<32, 40>(ldW);
    WAITV(26); sfor<16, 24>(rdW); LGK0(); sfor<40, 48>(ldW);
    WAITV(26); sfor<24, 32>(rdW); LGK0(); sfor<48, 56>(ldW);
    WAITV(16); sfor<32, 40>(rdW);
    const bool on = l < 32 || (l < 34 && P > 0);
    const int pv = on ? F0R + (l < 16 ? P * 16 + l : l < 32 ? (l - 16) * 16 + h : P * 16 + l - 34) * FS : OOB;
    v2u f0 = ld8h<0>(dws, pv, w == 0 ? 0 : OOB);
    LGK0(); sfor<56, 64>(ldW);
    STAMP(3);
    WAITV(8); sfor<40, 56>(rdW);
    pin(f0);
    if (w == 0 && !__all(!on || (f0.x == tag_lo && f0.y == tag_hi))) poll(rws, pv, !on, tag_lo, tag_hi);
    BAR();
    flag(F0B + b * FS);
    const int qo = qslot_off(w), wq = WQ_OFF + h * WQ_T + qo;
    cp_run<0, 8>(dws, l16, wq, qo);
    WAITV(9);
    sfor<56, 64>(rdW);
    cp_run<0, 8>(dws, l16, wq + 30720, qo + 30720);
  }

#pragma unroll 1
  for (int Lr = 0;; Lr = 1) {
    const int L = opq_s(Lr), kill1 = L << 30;
    const int act = ACT_OFF + L * ACT_LAY, acr = REUSE ? ACT_OFF : act, fl = FLAY + L * FLAY_SZ;
    const int sb = L * 24 + 5;
    f32x4 z1[7];
    unsigned braw;
    v2u rr[5];
    constexpr int RZ = 14;
    v4u Z[RZ];
    { LANE();
      const int qo = qslot_off(w), wq = (WHIT ? WQ_OFF : WQ_OFF + h * WQ_T) + qo;
      int vb[4], vh[4];
#pragma unroll
      for (int i = 0; i < 4; i++) { const int k = (i + w) & 3; vb[i] = opq_v(l16 + k * 7680); vh[i] = opq_v((ZPAD && c16 >= 8 ? 0x40000 : (g * 8 + (l & 7)) * 16) + k * 7680); }
      const int h2 = w < 2 ? w + 4 : w;
      const int c4 = opq_v(c16 * 4), g16 = opq_v(g * 16);
      const int wos = opq_s((WHIT ? WQ_OFF : WO_OFF + co * WO_T)), wzs = opq_s((WHIT ? WQ_OFF : WI_OFF + cz * WI_T));
      f32x4 acc[2][8];
      float mt[3], rt_[3], csn[3], hcn; f32x4 mn[3], rn[3], cst[5], ha[2][4], hcs;
      {
        v4u A[2][20], Hh[5], B[8];
        const int gsh = opq_s(LOCF ? (h >> 3) * 12288 : 0);
        int va[4], spo, xoff, hoff, fbo, cqo, rv;
        auto ldB = [&](auto fi) AI { constexpr int f = fi.v, t = f >> 3, ct = f & 7; B[f & 7] = lds16((ct < 7 ? vb[t & 3] : vh[t & 3]) + ((t >> 2) & 1) * 30720 + ct * 1024); };
        auto ldA = [&](auto ni) AI {
          constexpr int n = ni.v, t = q_step(n), i = (n % 9) & 1;
          const int go = (t >> 2) * 4096 + (t < 8 ? gsh : t < 12 ? 0 : -gsh);
          if constexpr (n % 9 == 0) Hh[t >> 2] = ld16h<0>(dws, va[0], hoff + go);
          else A[i][t] = ld16h<0>(dws, va[t & 3], xoff + i * XT + go);
        };
        rv = opq_v((g >> 1) * 256 + c16 * 16 + (g & 1) * 8);
#pragma unroll
        for (int i = 0; i < 4; i++) va[i] = opq_v(l16 + ((i + w) & 3) * 1024);
        xoff = opq_s(act + A_XIN + (8 * P + 2 * w) * XT); hoff = opq_s((act + A_XIN + (8 * P - 1) * XT) | ((P - 1) & OOB));
        spo = opq_v(seloob((int)sel0((2 * w + 1 + g) * XT, M_LO) + c16 * 8, P > 0 ? 0xFFFFFFFFFFFFull : M_LO));
        cqo = opq_v((l >> 3) * 512 + (l & 7) * 16);
        fbo = opq_v(l < 32 ? F0B + (((l >> 3) * 8 + (((l >> 3) + 1) & 2 ? cz : co)) * 8 + (l & 7)) * FS : l < 48 ? F0R + ((po >> 1) * 16 + l - 32) * FS : OOB);
        {
          const bool on = L == 1 && (l < 8 || (l < 16 && P > 0));
          const int pb_ = opq_s(FLAY + F6 + (P * 16 + h) * 2 * FS);
          const int pv = opq_v(on ? pb_ + (l < 8 ? w2 * 64 : FS - 64) + l * 8 : OOB);
          if (L == 1) poll(rws, pv, !on, tag_lo, tag_hi);
        }
        STAMP(sb - 1);
        u64 dg[12];
        if constexpr (DIAG == 1) dg[0] = tick();
        sfor<0, QPRE>(ldA);
        if (L == 0) { WAITV(8 + QPRE); BAR(); }
        sfor<0, 6>([&](auto fi) AI { ldB(IC<(fi.v ^ 1)>{}); SB(); });
        f32x4 hac[6];
        v2u sp[8], fb; float ss[4], sq[4]; v4u cq[2]; int xa, xb;
        sfor<0, 160>([&](auto fi) AI {
          constexpr int f = fi.v, t = f >> 3, ct = f & 7, hc = ct < 3 ? ct : ct - 2;
          constexpr bool halo = (t & 3) == 0 && (ct < 3 || ct >= 5);
          constexpr int qn = QT.ld[f], wu = QT.wu[f], ou = QT.ou[f];
          constexpr int si = q_si(f); (void)si;
          const v4u bb = B[f & 7];
          auto mm = [&](f32x4& d, const v4u& x) AI {
            if constexpr (ct < 5) mma<t == 0, false>(d, bb, x); else mma<t == 0, false>(d, x, bb);
            SB();
          };
          if constexpr (ct == 0) WAITV(QT.wt[t]);
          mm(acc[0][ct], A[0][t]);
          if constexpr (DIAG == 1 && f % 16 == 0) dg[1 + f / 16] = tick();
          if constexpr (f == 58 || f == 90 || f == 122) WAITV(QT.ws[(f - 58) / 32]);
          if constexpr (f == 101) WAITV(QT.cum[101] - QT.cum[86]);
          if constexpr (f == 26 || f == 58 || f == 90 || f == 122) BAR0();
          if constexpr (((f + 6) ^ 1) < 160) ldB(IC<((f + 6) ^ 1)>{});
          if constexpr ((t & 7) >= 4 && t < 16 && ct == 3) vb[t & 3] = gaddi<61440>(vb[t & 3]);
          if constexpr ((t & 7) >= 4 && t < 16 && ct == 5) vh[t & 3] = gaddi<61440>(vh[t & 3]);
          if constexpr (wu >= 0 && wu % 4 == 0) cp_arm(qo + (2 + wu / 8) * 30720 + (wu & 4) * 1024);
          if constexpr (ou == 0 || ou == 4) cp_arm(L_WO + w * 8192 + ou * 1024);
          {
            if constexpr (f >= 102 && f < 106) ss[f - 102] = gadd(asf(sp[f - 102].x), asf(sp[f - 98].x));
            if constexpr (f == 106 || f == 108) ss[(f - 106) / 2] = gadd(ss[(f - 106) / 2], ss[(f - 106) / 2 + 2]);
            if constexpr (f == 110) ss[0] = gadd(ss[0], ss[1]);
            if constexpr (f == 111) { ss[0] = ss[0] * (1.f / 640.f); pin(ss[0]); }
            if constexpr (f == 112) ss[1] = gmul(ss[0], ss[0]);
            if constexpr (f == 113) sq[1] = gadd(sq[1], sq[3]);
            if constexpr (f == 114) sq[0] = gadd(sq[0], sq[1]);
            if constexpr (f == 116) { sq[0] = sq[0] * (1.f / 640.f) - ss[1]; pin(sq[0]); }
            if constexpr (f == 118) { sq[0] += 1e-5f; pin(sq[0]); }
            if constexpr (f == 119) { sq[0] = __builtin_amdgcn_rsqf(sq[0]); pin(sq[0]); }
            if constexpr (f == 120) { xa = opq_v(L_XST1 + w * 512 + l * 4); *lp<float>(xa) = ss[0]; }
            if constexpr (f == 121) *lp<float>(xa + 256) = sq[0];
          }
          if constexpr (f == 126) xb = opq_v(L_XST1 + w * 512 + g16);
          if constexpr (f >= 127 && f < 130) mn[f - 127] = ldsf4(xb + (f - 127) * 64);
          if constexpr (f == 130 || f == 131 || f == 133) rn[f == 133 ? 2 : f - 130] = ldsf4(xb + 256 + (f == 133 ? 2 : f - 130) * 64);
          if constexpr (f == 134) xa = opq_v(L_HALO + w * 6144 + l16);
          if constexpr (f == 142) xb = opq_v(L_CSQ + g16);
          if constexpr (f >= 143 && f < 148) cst[f - 143] = ldsf4(xb + (f - 143) * 64);
          if constexpr (f == 148) xb = opq_v(xb + w * 64);
          if constexpr (f == 149) hcs = ldsf4(xb);
          if constexpr (f == 150) xa = opq_v(L_HALO + w * 1024 + l16);
          if constexpr (f == 151) xb = opq_v(L_HALO + h2 * 1024 + l16);
          if constexpr (f >= 152 && f < 156) ha[0][f - 152] = ldsf4(xa + (f - 152) * 6144);
          SB();
          mm(acc[1][ct], A[1][t]);
          {
            if constexpr (wu >= 0) cp_one<wu>(dws, l16, wq + (2 + wu / 8) * 30720 + (wu & 4) * 1024);
            if constexpr (f == 45 || f == 53) cq[f == 53] = ld16h<0>(dws, cqo, (CSQ_OFF + h * 8192 + w * 128 + (f == 53) * 4096) | kill1);
            if constexpr (f == 49) fb = ld8h<0>(dws, fbo, kill1);
            if constexpr (f == 101) {
              pin(fb); pin(cq[0]); pin(cq[1]);
              if (L == 0) {
                if (!__all(l >= 48 || (fb.x == tag_lo && fb.y == tag_hi))) poll(rws, fbo, l >= 48, tag_lo, tag_hi);
                f32x4 cs = __builtin_bit_cast(f32x4, cq[0]) + __builtin_bit_cast(f32x4, cq[1]);
#pragma unroll
                for (int e = 0; e < 4; e++) {
                  cs[e] += dpp<0x128>(cs[e]);
                  const v2u a = sw32(asu(cs[e]), asu(cs[e])); const float u = asf(a.x) + asf(a.y); const v2u d = sw16(asu(u), asu(u)); cs[e] = asf(d.x) + asf(d.y);
                }
                *lp<f32x4>(L_CSQ + w * 128 + (l & 7) * 16) = cs;
              }
            }
            if constexpr (qn >= 0) ldA(IC<qn>{});
            if constexpr (si >= 0) sp[si] = ld8h<si * 128>(dws, spo, act + A_XIN + (8 * P - 1) * XT + 20480);
            if constexpr (f >= 110 && f < 130 && f % 4 == 3) rr[(f - 110) / 4] = ld8h<(f - 110) / 4 * 512>(dws, rv, act + A_XIN + (4 * po + w) * XT + co * 2560);
            if constexpr (f >= 103 && f < 111 && (f & 1)) sq[(f - 103) / 2] = gadd(asf(sp[(f - 103) / 2].y), asf(sp[(f - 103) / 2 + 4].y));
            if constexpr (f == 113) sq[0] = gadd(sq[0], sq[2]);
            if constexpr (f == 117) { sq[0] = fmaxf(sq[0], 0.f); pin(sq[0]); }
          }
          if constexpr (f == 123) xa = opq_v(L_XST1 + w * 512 + c4);
          if constexpr (ou >= 0) cp_one<ou>(dws, l16, wos + w * 8192 + (ou & 4) * 1024);
          if constexpr (f == 125 || f == 127 || f == 131) { constexpr int i = f == 131 ? 2 : (f - 125) / 2; mt[i] = ldsf(xa + i * 64); rt_[i] = ldsf(xa + 256 + i * 64); }
          if constexpr (f >= 136 && f < 142) *lp<f32x4>(xa + (f - 136) * 1024) = hac[f - 136];
          if constexpr (f == 143) xa = opq_v(L_CSQ + c4);
          if constexpr (f == 144) { csn[0] = ldsf(xa + 320); csn[1] = ldsf(xa + 384); }
          if constexpr (f == 146) { csn[2] = ldsf(xa + 448); xa = opq_v(xa + (h2 + 2) * 64); }
          if constexpr (f == 148) hcn = ldsf(xa);
          if constexpr (f == 150) BAR0();
          if constexpr (f >= 152 && f < 156) ha[1][f - 152] = ldsf4(xb + (f - 152) * 6144);
          if constexpr (halo) { SB(); mm(hac[hc], Hh[t >> 2]); }
          SB();
        });
        if constexpr (DIAG == 1) { dg[11] = tick(); DUMP(); }
        if constexpr (PRET) {
#pragma unroll
          for (int i = 0; i < 2; i++) touch(dws, l * 128, act + A_X1 + ((b * 4 + w) * 2 + i) * 6400);
        }
        mma_settle();
#pragma unroll
        for (int j = 0; j < 8; j++) { pin(acc[0][j]); pin(acc[1][j]); }
      }
      STAMP(sb + 0);
      const int nbw = opq_v(L_NB + l * 8), w3s = wos + 65536 + w * 9216, w3d = L_WO + 65536 + w * 9216;
      cp_run<0, 3>(dws, l16, wos + 32768 + w * 8192, L_WO + 32768 + w * 8192);
      {
        const f32x4 a0 = (ha[0][0] + ha[0][1]) + (ha[0][2] + ha[0][3]), a1 = (ha[1][0] + ha[1][1]) + (ha[1][2] + ha[1][3]);
        *lp<v2u>(nbw + (w < 3 ? w * 512 : 15360)) = pk4((a0 - mt[2] * hcs) * rt_[2]);
        *lp<v2u>(nbw + (w != 2 ? h2 * 512 : 15360)) = pk4((a1 - mn[2] * hcn) * rn[2]);
      }
      v2u tk[2][8];
      auto fix = [&](int i, int ct) AI { tk[i][ct] = ct < 5 ? pk4((acc[i][ct] - mt[i] * cst[ct]) * rt_[i]) : pk4((acc[i][ct] - mn[i] * csn[ct - 5]) * rn[i]); };
      fix(1, 0); fix(1, 1); fix(1, 2);
      pin(tk[1][2]);
      cp_run<3, 5>(dws, l16, wos + 32768 + w * 8192, L_WO + 32768 + w * 8192);
      fix(1, 5); fix(1, 6); fix(1, 7);
      const int nbr = opq_v(nbw + w * 3072);
#pragma unroll
      for (int i = 0; i < 6; i++) *lp<v2u>(nbr + 3072 + i * 512) = tk[1][i < 3 ? i : i + 2];
      cp_run<5, 8>(dws, l16, wos + 32768 + w * 8192, L_WO + 32768 + w * 8192);
      SB();
      fix(0, 0); fix(0, 1); fix(0, 2); fix(1, 3); fix(1, 4);
      BAR();
      v2u nb[6];
#pragma unroll
      for (int i = 0; i < 6; i++) { nb[i] = lds8(nbr + i * 512); asm volatile("" ::: "memory"); }
      cp_run<0, 3>(dws, l16, w3s, w3d);
      STAMP(sb + 1);
      SB();
      fix(0, 3); fix(0, 4);
      v6u K6[3]; v4u Q[2][2];
      {
        const v2u qx = sw32(tk[0][2].x, tk[1][2].x), qy = sw32(tk[0][2].y, tk[1][2].y);
        const unsigned q1x = sw32(qx.y, qx.y).y, q1y = sw32(qy.y, qy.y).y;
        Q[0][0] = v4u{tk[0][3].x, tk[0][3].y, sel0(qx.y, M_G0), sel0(qy.y, M_G0)};
        Q[0][1] = v4u{sel0(qx.y, M_G1), sel0(qy.y, M_G1), tk[0][4].x, tk[0][4].y};
        Q[1][0] = v4u{tk[1][3].x, tk[1][3].y, sel0(q1x, M_G0), sel0(q1y, M_G0)};
        Q[1][1] = v4u{sel0(q1x, M_G1), sel0(q1y, M_G1), tk[1][4].x, tk[1][4].y};
      }
#pragma unroll
      for (int i = 0; i < 2; i++) {
        K6[i + 1] = v6u{tk[i][0].x, tk[i][0].y, tk[i][2].x, tk[i][2].y, tk[i][1].x, tk[i][1].y};
        pin(Q[i][0]); pin(Q[i][1]); pin(K6[i + 1]);
      }
      K6[0] = v6u{nb[0].x, nb[0].y, nb[2].x, nb[2].y, nb[1].x, nb[1].y};
      pin(K6[0]);
      NOP2();
      f32x4 S[4][2];
#pragma unroll
      for (int u = 3; u >= 0; u--)
#pragma unroll
        for (int k = 1; k >= 0; k--) {
          const v6u kk = K6[(u >> 1) + k];
          mma<true, false>(S[u][k], (u & 1) ? __builtin_shufflevector(kk, kk, 2, 3, 4, 5) : __builtin_shufflevector(kk, kk, 0, 1, 2, 3), Q[u >> 1][u & 1]);
        }
      SB();
      cp_run<3, 6>(dws, l16, w3s, w3d);
      unsigned sel[4];
      {
        const unsigned zz = 0x0c0c0c0cu, a = ::sel(0x0c0c0504u, 0x05040c0cu, M_ODD), d = ::sel(0x0c0c0706u, 0x07060c0cu, M_ODD);
        sel[2] = ::sel(zz, a, le_mask(0)); sel[3] = ::sel(zz, d, le_mask(2));
        sel[0] = ::sel(a, zz, le_mask(0)); sel[1] = ::sel(d, zz, le_mask(2));
      }
      const int ovo = (g & 1) * 768 + (g >> 1) * 256 + c16 * 16, ovt = seloob(g * OT + 512 + c16 * 16, M_LO);
      fix(0, 5); fix(0, 6); fix(0, 7);
      v6u V6[3];
#pragma unroll
      for (int j = 0; j < 3; j++) { V6[j] = v6u{nb[3 + j].x, nb[3 + j].y, tk[0][5 + j].x, tk[0][5 + j].y, tk[1][5 + j].x, tk[1][5 + j].y}; pin(V6[j]); }
      SB();
#pragma unroll
      for (int u = 0; u < 8; u++) pin(S[u >> 1][u & 1]);
      if (P + w == 0) { S[0][0] = f32x4{-1e30f, -1e30f, -1e30f, -1e30f}; pin(S[0][0]); S[1][0] = S[0][0]; }
      float s0[4], s1[4], mx[4];
#pragma unroll
      for (int u = 0; u < 4; u++) {
        const f32x4 a = S[u][0], bq = S[u][1];
        s0[u] = self(self(a[0], a[1], M_ODD), self(bq[0], bq[1], M_ODD), le_mask(0));
        s1[u] = self(self(a[2], a[3], M_ODD), self(bq[2], bq[3], M_ODD), le_mask(2));
        mx[u] = vmax(s0[u], s1[u]);
      }
      auto spread = [&](float t, float (&o)[4]) AI {
        const v2u a = sw16(asu(t), asu(t)), x = sw32(a.x, a.x), y = sw32(a.y, a.y);
        o[0] = asf(x.x); o[1] = asf(x.y); o[2] = asf(y.x); o[3] = asf(y.y);
      };
      const float kk = 0.22360679774997896f * 1.4426950408889634f;
      {
        const v2u a = sw32(asu(mx[0]), asu(mx[1])), bq = sw32(asu(mx[2]), asu(mx[3]));
        const v2u d = sw16(asu(vmax(asf(a.x), asf(a.y))), asu(vmax(asf(bq.x), asf(bq.y))));
        spread(vmax(asf(d.x), asf(d.y)), mx);
      }
      SB();
      cp_run<6, 9>(dws, l16, w3s, w3d);
      float sm4[4];
#pragma unroll
      for (int u = 0; u < 4; u++) {
        const f32x2 e = (f32x2{s0[u], s1[u]} - mx[u]) * kk;
        s0[u] = __builtin_amdgcn_exp2f(e.x); s1[u] = __builtin_amdgcn_exp2f(e.y);
        sm4[u] = s0[u] + s1[u];
      }
      {
        const v2u a = sw32(asu(sm4[0]), asu(sm4[1])), bq = sw32(asu(sm4[2]), asu(sm4[3]));
        const v2u d = sw16(asu(asf(a.x) + asf(a.y)), asu(asf(bq.x) + asf(bq.y)));
        spread(__builtin_amdgcn_rcpf(asf(d.x) + asf(d.y)), sm4);
      }
      v4u pf[4];
#pragma unroll
      for (int u = 0; u < 4; u++) {
        const f32x2 pn = f32x2{s0[u], s1[u]} * sm4[u];
        const unsigned pp = pk(pn.x, pn.y);
        pf[u] = v4u{__builtin_amdgcn_perm(pp, 0u, sel[0]), __builtin_amdgcn_perm(pp, 0u, sel[1]), __builtin_amdgcn_perm(pp, 0u, sel[2]), __builtin_amdgcn_perm(pp, 0u, sel[3])};
        pin(pf[u]);
      }
      NOP2();
      f32x4 Om[4], Ot[4];
#pragma unroll
      for (int u = 0; u < 4; u++) { const v6u v = V6[u & 1]; mma<true, false>(Om[u], (u >> 1) ? __builtin_shufflevector(v, v, 2, 3, 4, 5) : __builtin_shufflevector(v, v, 0, 1, 2, 3), pf[u]); }
#pragma unroll
      for (int u = 0; u < 4; u++) { const v6u v = V6[2]; mma<true, false>(Ot[u], (u >> 1) ? __builtin_shufflevector(v, v, 2, 3, 4, 5) : __builtin_shufflevector(v, v, 0, 1, 2, 3), pf[u]); }
      asm volatile("s_nop 3");
#pragma unroll
      for (int u = 0; u < 4; u++) pin(Om[u]);
      const int so = acr + A_O + (8 * P + 2 * w) * OT + 5 * h * 256;
      st16(join16(pk4(Om[0]), pk4(Om[1])), rws, ovo, so);
      st16(join16(pk4(Om[2]), pk4(Om[3])), rws, ovo, so + OT);
      SB();
#pragma unroll
      for (int u = 0; u < 4; u++) pin(Ot[u]);
      {
        const v2u x0_ = pk4(Ot[0]), y0_ = pk4(Ot[1]), x1_ = pk4(Ot[2]), y1_ = pk4(Ot[3]);
        st16(v4u{sw16(x0_.x, x1_.x).x, sw16(x0_.y, x1_.y).x, sw16(y0_.x, y1_.x).y, sw16(y0_.y, y1_.y).y}, rws, ovt, so);
      }
      STAMP(sb + 2);
      cp_run<0, 6>(dws, l16, wzs + w * 6144, w * 6144);
      cp_run<0, 4>(dws, l16, wzs + 40960 + w * 4096, 40960 + w * 4096);
      const int fv = opq_v(seloob(l * FS, M_G0)), fo = opq_s(fl + F1 + P * 16 * FS + h * 8);
      WAITV(10);
      BAR();
      push(fv, fo);
#pragma unroll
      for (int j = 0; j < 5; j++) pin(rr[j]);
      STAMP(sb + 3);
      touch(dws, (g < 2 && c16 < 10) ? g * OT + c16 * 128 : OOB, so);
      cp_run<0, 4>(dws, l16, wzs + 24576 + w * 4096, 24576 + w * 4096);
    }
    { LANE();
      f32x4 acc[2][5];
      const int tile = opq_s(acr + A_X1 + (4 * po + w) * XT), wzs = opq_s((WHIT ? WQ_OFF : WI_OFF + cz * WI_T));
      const int vo = opq_v((g & 1) * 512 + (g >> 1) * 256 + c16 * 16), vo4 = opq_v(seloob(vo, M_GEVEN)), vs = opq_v(seloob(c16 * 8 + (g >> 1) * 4, M_GEVEN));
      const int xw = opq_v(L_X2 + (w ^ 1) * 5120 + l16), xr = opq_v(L_X2 + w * 5120 + l16);
      const int lo_ = LOCF ? (co >> 2) * 10240 : 0, dl = opq_s(10240 - 2 * lo_);
      const int a0 = opq_s(acr + A_O + (4 * po + w) * OT + kh * 5120 + lo_), a1 = opq_s(acr + A_O + (4 * po + (w ^ 1)) * OT + kh * 5120 + lo_);
      const int b0 = opq_v(L_WO + l16 + kh * 5120), b1 = opq_v(b0 + 51200);
      const int pv = opq_v(seloob(fl + F1 + (po * 8 + co) * FS + (kh * 4 + (l >> 2) * 4 + l) * 8, 0xFFull));
      v4u A[2][10], ci[4];
      const int civ = opq_v(g * 512 + c16 * 16), cis = opq_s((CSI_OFF + (w2 * 8 + cz) * 8192 + kh * 256) | kill1);
      gemm_m2k2<10, 5, 3, true, WINA, true, nv_o>([&](auto ti) AI { return ti.v < 5 ? b0 + ti.v * 10240 : b1 + (ti.v - 5) * 10240; }, xw, A, acc,
          [&]() AI {
#pragma unroll
        for (int j = 0; j < 5; j++) { acc[0][j] = unpk4(rr[j]); pin(acc[0][j]); }
        poll<PDLY>(rws, pv, l >= 8, tag_lo, tag_hi);
        STAMP(sb + 4);
      },
          [&](auto ii, auto ti) AI { return ld16h<(ti.v % 5 & 3) * 1024>(dws, l16, (ii.v ? a1 : a0) + (ti.v >= 5 ? dl : 0) + (ti.v % 5 >> 2) * 4096); },
          [&](auto fi) AI {
        constexpr int f = fi.v;
        if constexpr (f == 13 || f == 31) BAR0();
        if constexpr (f == 32 || f == 36) cp_arm(L_Z2B + 71680 + w * 7168 + (f - 32) * 1024);
        if constexpr (f == 35) cp_one<3>(dws, l16, wzs + 71680 + w * 7168);
        if constexpr (f == 39 || f == 43 || f == 47) cp_one<(f - 39) / 4>(dws, l16, wzs + 71680 + w * 7168 + 4096);
      },
          [&](auto fi) AI {
        constexpr int f = fi.v;
        if constexpr (f >= 17 && f < 25 && (f & 1)) ci[(f - 17) / 2] = ld16h<((f - 17) / 2 & 1) * 2048>(dws, civ, cis + (f - 17) / 4 * 4096);
        if constexpr (f >= 32) cp_one<f - 32>(dws, l16, wzs + 71680 + w * 7168);
      });
      STAMP(sb + 5);
      BAR();
      xchg_read<5>(xr, acc);
      store_x(acc[0], rws, tile + co * 2560, tile + 20480 + co * 128, vo, vo4, vs);
#pragma unroll
      for (int j = 0; j < 4; j++) pin(ci[j]);
      if (L == 0) {
        f32x4 t = (__builtin_bit_cast(f32x4, ci[0]) + __builtin_bit_cast(f32x4, ci[1])) + (__builtin_bit_cast(f32x4, ci[2]) + __builtin_bit_cast(f32x4, ci[3]));
#pragma unroll
        for (int e = 0; e < 4; e++) { const v2u a = sw32(asu(t[e]), asu(t[e])); const float u = asf(a.x) + asf(a.y); const v2u d = sw16(asu(u), asu(u)); t[e] = asf(d.x) + asf(d.y); }
        *lp<f32x4>(L_CS2 + w * 256 + c16 * 16) = t;
      }
      const int zc = 100352 + ((w * 10752) & ~1023);
      cp_run<0, 11>(dws, l16, wzs + zc, L_Z2B + zc);
      const int fv = opq_v(seloob(l * FS, 0xFFull)), fo = opq_s(fl + F2 + po * 8 * FS + co * 8);
      WAITV(11);
      BAR();
      push(fv, fo);
      STAMP(sb + 6);
      touch(dws, l < 20 ? co * 2560 + l * 128 : l == 20 ? 20480 + co * 128 : OOB, tile);
      cp_run<0, 4>(dws, l16, wzs + 57344 + ((w * 3584) & ~1023), L_Z2B + 57344 + ((w * 3584) & ~1023));
    }
    { LANE();
      const int wz1 = opq_s((WHIT ? WQ_OFF : WI_OFF + (8 + cz) * WI_T));
      const int lo_ = LOCF ? (cz >> 2) * 10240 : 0, dl = opq_s(10240 - 2 * lo_);
      const int a0 = opq_s(acr + A_X1 + (4 * pz + w) * XT + kh * 5120 + lo_), a1 = opq_s(acr + A_X1 + (4 * pz + (w ^ 1)) * XT + kh * 5120 + lo_);
      int xw = opq_v(L_X3 + (w ^ 1) * 7168 + l16), xr = opq_v(L_X3 + w * 7168 + l16);
      const int gso = opq_v(seloob(l * 4, 0x1111111111111111ull)), gro = opq_v(((l >> 2) & 3) * 256 + (4 * g + (l & 3)) * 16);
      const int zvo = opq_v((g & 1) * 1024 + (g >> 1) * 256 + c16 * 16), spo = opq_v(g * 256 + c16 * 8);
      const int sta = opq_v(L_XST3 + w * 128 + (g & 1) * 64 + c16 * 4), stb = opq_v(L_XST3 + w * 128 + g * 16), tv = opq_v(l * 128);
      const int b0 = opq_v(l16 + kh * 7168), b1 = opq_v(b0 + L_Z1B + 3 * 14336), b2 = opq_v(b1 + 4 * 14336);
      static_assert(L_Z1B == L_Z2B, "");
      const int sps = opq_s(acr + A_X1 + (4 * pz + w) * XT + 20480), za0 = opq_s((w * 10752) & ~1023);
      float cs2[7];
#pragma unroll
      for (int j = 0; j < 7; j++) { cs2[j] = ldsf(L_CS2 + c16 * 4 + j * 64); pin(cs2[j]); }
      braw = __builtin_amdgcn_raw_buffer_load_b16(mk(bsp, 4096), c16 * 2, (64 * pz + 16 * w) * 2, 0);
      v4u A[2][10]; f32x4 acc[2][7];
      v2u sp0, sp1, u_;
      float m1, r1, st_; f32x4 m4, r4;
      gemm_m2k2<10, 7, 1, false, WINA, false, nv_z>([&](auto ti) AI { return ti.v < 4 ? b0 + ti.v * 14336 : ti.v < 7 ? b1 + (ti.v - 3) * 14336 : b2 + (ti.v - 7) * 14336; }, xw, A, acc,
          [&]() AI { poll<PDLY>(rws, seloob(fl + F2 + (pz * 8 + cz) * FS + l * 8, 0xFFull), l >= 8, tag_lo, tag_hi); pin(braw); STAMP(sb + 7); },
          [&](auto ii, auto ti) AI { return ld16h<(ti.v % 5 & 3) * 1024>(dws, l16, (ii.v ? a1 : a0) + (ti.v >= 5 ? dl : 0) + (ti.v % 5 >> 2) * 4096); },
          [&](auto fi) AI {
        constexpr int f = fi.v;
        if constexpr (f == 15 || f == 31) BAR0();
        if constexpr (f == 32 || f == 44 || f == 54) cp_arm(za0 + (f - 30) / 12 * 4096);
        if constexpr (f == 47) { u_ = sw32(asu(m1), asu(r1)); pin(u_); }
        if constexpr (f == 48) { st_ = gadd(asf(u_.x), asf(u_.y)); }
        if constexpr (f == 49) { u_ = sw16(asu(st_), asu(st_)); pin(u_); }
        if constexpr (f == 50) { st_ = gadd(asf(u_.x), asf(u_.y)); }
        if constexpr (f == 51) { u_ = sw32(asu(st_), asu(st_)); pin(u_); }
        if constexpr (f == 52) { m1 = asf(u_.x) * (1.f / 640.f); pin(m1); }
        if constexpr (f == 53) { st_ = gmul(m1, m1); }
        if constexpr (f == 55) { r1 = asf(u_.y) * (1.f / 640.f) - st_; pin(r1); }
        if constexpr (f == 56) { r1 = fmaxf(r1, 0.f); pin(r1); }
        if constexpr (f == 57) { r1 += 1e-5f; pin(r1); }
        if constexpr (f == 58) { r1 = __builtin_amdgcn_rsqf(r1); pin(r1); }
        if constexpr (f == 60) *lp<float>(sta) = self(r1, m1, M_GEVEN);
        if constexpr (f == 63) { m4 = ldsf4(stb); r4 = ldsf4(stb + 64); }
      },
          [&](auto fi) AI {
        constexpr int f = fi.v;
        if constexpr (f == 1) sp0 = ld8h<0>(dws, spo, sps);
        if constexpr (f == 3) sp1 = ld8h<128>(dws, spo, sps);
        if constexpr (f == 43) { m1 = gadd(asf(sp0.x), asf(sp1.x)); }
        if constexpr (f == 45) { r1 = gadd(asf(sp0.y), asf(sp1.y)); }
        if constexpr (f >= 32 && f % 3 == 2) cp_one<(f - 32) / 3>(dws, l16, wz1 + za0 + (f - 32) / 12 * 4096);
      });
      STAMP(sb + 8);
      BAR();
      xchg_read<7>(xr, acc);
      BAR();
      f32x4 z2[7], zs, zq;
      const int gz_ = 43008 + ((w * 17920) & ~1023), gs = wz1 + gz_, gd = L_Z1B + gz_;
      auto gel = [&](auto l0) AI {
        sfor<0, 7>([&](auto ji) AI {
          constexpr int j = ji.v;
          if constexpr (j < 6) cp_run<3 * j, 3 * j + 3>(dws, l16, gs, gd);
          SB();
          f32x4 v = gelu4((acc[0][j] - m4 * cs2[j]) * r4);
          if constexpr (j == 6 && !ZPAD) v *= cz < 6 ? 1.f : 0.f;
          z2[j] = v;
          if (j == 0) { zs = v; zq = v * v; } else { zs += v; zq += v * v; }
          pin(zs); pin(zq);
          SB();
        });
      };
      gel(IC<0>{});
      const int sto = ST_OFF + L * ST_LAY + pz * 8192 + w * 2048;
      {
        const f32x2 t = rowsum8(zs, zq);
        st16(v4u{asu(t.x), GTAG, asu(t.y), GTAG}, rws, gso, sto + cz * 256);
      }
      STAMP(sb + 9);
      const int zo = opq_s(act + A_Z + (cz * 64 + 2 * pz) * 7168), z8 = opq_s(114688 + w * 7168), us = opq_s((WHIT ? WQ_OFF : WU_OFF + co * WU_T));
      const int fv = opq_v(seloob(0, 1ull)), fo = opq_s(fl + F4 + (cz * 32 + pz) * FS);
      const int k6z = opq_s(ZPAD && cz >= 6 ? OOB : 0);
      f32x4 cs1[7];
      int tmp;
      xw += L_X3B - L_X3; xr += L_X3B - L_X3;
      v4u ga, gb;
      WAITV(19);
      BAR0();
      gemm_m2k2<10, 7, 2, true, 0, false, nv_none>([&](auto ti) AI { return ti.v < 3 ? b0 + ti.v * 14336 : ti.v < 7 ? b1 + (ti.v - 3) * 14336 : b2 + (ti.v - 7) * 14336; }, xw, A, acc,
          [&]() AI {}, [&](auto, auto) AI { return v4u{}; },
          [&](auto fi) AI {
        constexpr int f = fi.v;
        if constexpr (f == 0 || f == 8) cp_arm(L_Z1B + z8 + f * 512);
        if constexpr (f == 13) { WAITV(8); BAR0(); }
        if constexpr (f == 21 || f == 45) BAR0();
        if constexpr (f == 41 || f == 49) cp_arm(w * 7168 + (f - 41) * 512);
        if constexpr (f == PUBF) { WAITV(7); BAR0(); push(fv, fo); }
        if constexpr (f == 47) tmp = opq_v(L_CS1 + g * 16);
        if constexpr (f >= 56 && !(f & 1)) cs1[(f - 56) / 2] = ldsf4(tmp + (f - 56) * 32);
      },
          [&](auto fi) AI {
        constexpr int f = fi.v;
        if constexpr (f < 14 && !(f & 1)) cp_one<f / 2>(dws, l16, wz1 + z8 + f / 8 * 4096);
        if constexpr (f >= 42 && !(f & 1)) cp_one<(f - 42) / 2>(dws, l16, us + w * 7168 + (f - 42) / 8 * 4096);
        if constexpr (f == GLD) ga = ld16h<0>(dws, gro, sto);
        if constexpr (f == GLD + 2) gb = ld16h<1024>(dws, gro, sto);
        if constexpr (f == GLD + 14) {
          DRAIN(); pin(ga); pin(gb);
          for (unsigned spins = 0; spins < (1u << 18); ++spins) {
            if (__all(ga.y == GTAG && ga.w == GTAG && gb.y == GTAG && gb.w == GTAG)) break;
            asm volatile("" ::: "memory");
            ga = ld16<SC1>(rws, gro, sto); gb = ld16<SC1>(rws, gro + 1024, sto);
          }
          STAMP(sb + 10);
          float ts = asf(ga.x) + asf(gb.x), tq = asf(ga.z) + asf(gb.z);
          ts += dpp<0x128>(ts); tq += dpp<0x128>(tq); ts += dpp<0x124>(ts); tq += dpp<0x124>(tq);
          f32x2 zz = finish_stat(ts, tq, 1.f / 864.f);
          zz.x = -zz.x * zz.y;
          const f32x4 mm = {dpp<0x00>(zz.x), dpp<0x55>(zz.x), dpp<0xAA>(zz.x), dpp<0xFF>(zz.x)};
          const f32x4 rs = {dpp<0x00>(zz.y), dpp<0x55>(zz.y), dpp<0xAA>(zz.y), dpp<0xFF>(zz.y)};
          const int so = zo + w2 * 7168 + kh * 512;
          v2u d[7];
#pragma unroll
          for (int j = 0; j < 7; j++) d[j] = pk4(z2[j] * rs + mm);
          st16(join16(d[0], d[1]), rws, zvo, so);
          st16(join16(d[2], d[3]), rws, zvo + 2048, so);
          st16(join16(d[4], d[5]), rws, zvo, so + 4096);
          st16(join16(d[6], d[6]), rws, seloob(zvo, M_GEVEN) + 2048, (so + 4096) | k6z);
          STAMP(sb + 11);
        }
      });
      BAR();
      STAMP(sb + 12);
      touch(dws, seloob(tv, (1ull << 28) - 1), zo + w * 3584);
      xchg_read<7>(xr, acc);
      const int z0 = opq_s(act + A_Z + (cz * 32 + pan(0, pz, KQ)) * 14336);
      const int fno = opq_v(seloob(fl + F4 + (cz * 32 + (pz & 24) + 2 * KQ + l) * FS, 0x3ull));
      auto ldZ0 = [&](auto fi) AI { Z[fi.v] = ld16h<(fi.v & 3) * 1024>(dws, l16, (z0 + (fi.v >> 2) * 4096) | (fi.v % 7 == 6 ? k6z : 0)); };
      v2u fn_;
      sfor<0, 7>([&](auto ji) AI {
        constexpr int j = ji.v;
        if constexpr (j < 3) cp_run<(5 * j + 1) / 2, j == 2 ? 7 : (5 * j + 6) / 2>(dws, l16, us + 28672 + w * 7168, 28672 + w * 7168);
        if constexpr (j == NCHK - 2) fn_ = ld8<SC1>(rws, fno, 0);
        if constexpr (j == NCHK) { if (!__all(l >= 2 || (fn_.x == tag_lo && fn_.y == tag_hi))) poll(rws, fno, l >= 2, tag_lo, tag_hi); FENCE(); }
        if constexpr (j >= NCHK) sfor<4 * (j - NCHK), 4 * (j - NCHK) + 4>(ldZ0);
        SB();
        z1[j] = gelu4((acc[0][j] - m1 * cs1[j]) * r1);
        pin(z1[j]);
        SB();
      });
      STAMP(sb + 13);
      sfor<4 * (7 - NCHK), RZ>(ldZ0);
    }
    { LANE();
      const int ct0 = cz < 6 ? 7 * cz : 6 + 6 * cz, kq = opq_s(KQ);
      STAMP(sb + 14);
      f32x4 acc[4][7];
      const int so = acr + A_G + (4 * pz + w) * GT + ct0 * 512;
      f32x4 bias = blo(braw);
      pin(bias);
      int zp[8], za, rw[3], rd, vo, rv; unsigned d[4];
      constexpr int RN = RZ + 1;
      v4u Y[RN];
      sfor<0, RZ>([&](auto fi) AI { Y[fi.v] = Z[fi.v]; });
      const int ut = opq_s((WHIT ? WQ_OFF : WU_OFF + co * WU_T) + 57344 + w * 10240);
      const int zb = opq_s(act + A_Z + cz * 32 * 14336), k6z = opq_s(ZPAD && cz >= 6 ? OOB : 0);
      zp[0] = opq_s(zb + pan(0, pz, kq) * 14336);
      const int fzo = opq_v(seloob(fl + F4 + (cz * 32 + (l >> 1) * 8 + 2 * kq + (l & 1)) * FS, 0xFFull));
      v2u fz;
      u64 dg[12];
      if constexpr (DIAG == 2) dg[0] = tick();
      NOP2();
      SB();
      sfor<0, 112>([&](auto fi) AI {
        constexpr int f = fi.v, s = sp_s(f), j = sp_j(f), fn = f + RZ;
        constexpr int q = f < 52 ? -1 : (f - 52) / 24, k = f < 52 ? 0 : (f - 52) % 24, buf = (q & 1) ? L_RED1 - L_RED0 : 0;
        auto fin = [&](auto ui) AI {
          constexpr int u = ui.v;
          if constexpr (q >= 0 && u >= 0 && u < 36) {
            constexpr int jj = 2 * q + (u < 32 ? (u >> 2) & 1 : (u - 32) >> 1), e = u & 3, st = u >> 3;
            if constexpr (st == 0) acc[0][jj][e] = gadd(acc[0][jj][e], acc[1][jj][e]);
            if constexpr (st == 1) acc[2][jj][e] = gadd(acc[2][jj][e], acc[3][jj][e]);
            if constexpr (st == 2) acc[0][jj][e] = gadd(acc[0][jj][e], acc[2][jj][e]);
            if constexpr (st == 3) acc[0][jj][e] = gmul(acc[0][jj][e], z1[jj][e]);
            if constexpr (st == 4) d[u - 32] = gcvt(acc[0][jj][(u & 1) * 2], acc[0][jj][(u & 1) * 2 + 1]);
          }
        };
        constexpr int per = q == 2 ? 7 : 4, u0 = (k - (q == 2 ? 9 : 10)) * per;
        const v4u z = Y[f % RN];
        WAITV(s_wait(f));
        mma<s == 0, true>(acc[1][j], z, W[1][s]);
        if constexpr (DIAG == 2 && (f == 0 || f == 14 || (f >= 28 && (f - 28) % 12 == 0 && f < 112))) dg[f == 0 ? 1 : f == 14 ? 2 : 3 + (f - 28) / 12] = tick();
        SB();
        if constexpr (fn < 28 && !(fn & 1)) za = opq_s(zp[0] + fn * 1024);
        if constexpr (fn >= 28 && fn < 112 && (fn >= 100 || !(fn & 1))) za = opq_s(zp[sp_s(fn) / 2] + ((sp_s(fn) & 1) * 7 + (sp_j(fn) & 6)) * 1024);
        if constexpr (q >= 0 && k >= 1 && k < 4) *lp<f32x4>(rw[k - 1] + buf) = acc[k][2 * q];
        if constexpr (q >= 0 && k >= 6 && k < 9) acc[k - 5][2 * q] = ldsf4(rd + buf + (k - 6) * 2048);
        fin(IC<u0>{});
        if constexpr (q == 2) fin(IC<u0 + 1>{});
        SB();
        mma<s == 0, true>(acc[2][j], z, W[2][s]);
        SB();
        if constexpr (fn < 112) Y[fn % RN] = ld16h<(fn < 100 ? fn & 1 : 0) * 1024>(dws, l16, za | (sp_j(fn) == 6 ? k6z : 0));
        if constexpr (f >= 98 && f < 108) cp_one<f - 98>(dws, l16, ut + (f - 98) / 4 * 4096);
        if constexpr (q == 2) { fin(IC<u0 + 2>{}); fin(IC<u0 + 3>{}); }
        SB();
        mma<s == 0, true>(acc[3][j], z, W[3][s]);
        SB();
        if constexpr (q >= 0 && k >= 2 && k < 5) *lp<f32x4>(rw[k - 2] + buf + 1024) = acc[k - 1][2 * q + 1];
        if constexpr (q >= 0 && k >= 6 && k < 9) acc[k - 5][2 * q + 1] = ldsf4(rd + buf + (k - 6) * 2048 + 1024);
        fin(IC<u0 + per - 3>{}); fin(IC<u0 + per - 2>{});
        if constexpr (f == 4) fz = ld8h<0>(dws, fzo, 0);
        if constexpr (f == 13) { WAITV(9); pin(fz); if (!__all(l >= 8 || (fz.x == tag_lo && fz.y == tag_hi))) poll(rws, fzo, l >= 8, tag_lo, tag_hi); }
        if constexpr (f >= 40 && f < 50 && !(f & 1)) rr[(f - 40) / 2] = ld8h<(f - 40) * 256>(dws, rv, acr + A_X1 + (4 * po + w) * XT + co * 2560);
        if constexpr (f == 97 || f == 101 || f == 105) cp_arm(57344 + w * 10240 + (f - 97) * 1024);
        if constexpr (f % 4 == 2 && f / 4 + 2 < 8) zp[f / 4 + 2] = opq_s(pan(f / 4 + 2, pz, kq));
        if constexpr (f % 4 == 3 && f / 4 + 2 < 8) zp[f / 4 + 2] = opq_s(zb + zp[f / 4 + 2] * 14336);
        if constexpr (f == 31 || f == 33 || f == 35) rw[(f - 31) / 2] = opq_v(L_RED0 + ((((w + (f - 29) / 2) & 3) * 3 + (f - 31) / 2) * 2) * 1024 + l16);
        if constexpr (f == 37) rd = opq_v(L_RED0 + w * 6144 + l16);
        if constexpr (f == 24) vo = opq_v((g & 1) * 512 + c16 * 16);
        if constexpr (f == 26) vo = opq_v(vo + (g >> 1) * 256);
        if constexpr (f == 28) rv = opq_v(vo - (g & 1) * 504);
        if constexpr (q >= 0 && q < 2 && k == 20) { const v2u a = sw16(d[0], d[2]), e = sw16(d[1], d[3]); d[0] = a.x; d[2] = a.y; d[1] = e.x; d[3] = e.y; pin(d[0]); pin(d[1]); }
        if constexpr (q >= 0 && q < 2 && k == 21) st16(v4u{d[0], d[1], d[2], d[3]}, rws, vo + q * 1024, so);
        if constexpr (f == 39) BAR0();
        if constexpr (q >= 0 && k == 5) BAR();
        SB();
        if constexpr (s == 0) mma_c(acc[0][j], z, W[0][s], bias); else mma<false, true>(acc[0][j], z, W[0][s]);
        SB();
        fin(IC<u0 + per - 1>{});
        SB();
      });
#pragma unroll
      for (int e = 1; e < 4; e++) acc[0][5][e] = gadd(acc[0][5][e], acc[2][5][e]);
      if constexpr (DIAG == 2) { dg[10] = tick(); dg[11] = dg[10]; DUMP(); }
      const int rw2 = opq_v(L_RED2 + l16);
      asm volatile("s_nop 0" ::: "memory");
#pragma unroll
      for (int i = 1; i < 4; i++) pin(acc[i][6]);
      STAMP(sb + 15);
#pragma unroll
      for (int r = 1; r < 4; r++) *lp<f32x4>(rw2 + (((w + r) & 3) * 3 + r - 1) * 1024) = acc[r][6];
      st16(join16(pk4(acc[0][4] * z1[4]), pk4(acc[0][5] * z1[5])), rws, vo + 2048, so);
      BAR();
#pragma unroll
      for (int r = 1; r < 4; r++) acc[r][6] = ldsf4(rw2 + (w * 3 + r - 1) * 1024);
      pin(acc[0][6]);
      {
        const v2u dd = pk4(((acc[0][6] + acc[1][6]) + (acc[2][6] + acc[3][6])) * z1[6]);
        st16(join16(dd, dd), rws, seloob(vo, M_GEVEN) + 3072, cz < 6 ? so : OOB);
      }
      const int uc = 98304 + ((w * 9984) & ~1023);
      cp_run<0, 10>(dws, l16, (WHIT ? WQ_OFF : WU_OFF + co * WU_T) + uc, uc);
      const int fv = opq_v(seloob(l * FS, 0xFFull)), fo = opq_s(fl + F5 + pz * 8 * FS + cz * 8);
      WAITV(10);
      BAR();
      push(fv, fo);
      STAMP(sb + 16);
      touch(dws, l < (cz < 6 ? 28 : 24) ? l * 128 : OOB, so);
    }
    { LANE();
      f32x4 acc[2][5];
      const int vo = opq_v(L == 0 ? (g & 1) * 512 + (g >> 1) * 256 + c16 * 16 : c16 * 1280 + (g & 1) * 32 + (g >> 1) * 16);
      const int vo4 = opq_v(seloob(vo, M_GEVEN)), vs = opq_v(seloob(c16 * 8 + (g >> 1) * 4, M_GEVEN));
      const int xw = opq_v(L_X5 + (w ^ 1) * 5120 + l16), xr = opq_v(L_X5 + w * 5120 + l16);
      const int fl_ = LOCF ? co >> 2 : 0, sh6 = fl_ & kh;
      const int a0 = opq_s(acr + A_G + (4 * po + w) * GT + kh * 7168), a1 = opq_s(acr + A_G + (4 * po + (w ^ 1)) * GT + kh * 7168);
      const int dn = opq_s(fl_ * 14336), df = opq_s(14336 - fl_ * 14336 + sh6 * 1024), d6 = opq_s(sh6 ? 0 : fl_ * 14336 + 6144), d13 = opq_s((df + 6144) | (kh << 30));
      const int b0 = opq_v(l16 + kh * 5120), b1 = opq_v(b0 + 61440), b2 = opq_v(b0 + 122880), b3 = opq_v(l16 + 133120);
      const int qo = opq_s(qslot_off(w)), wq = opq_s(((WHIT ? WQ_OFF : WQ_OFF + h * WQ_T) + qo) | kill1);
      const int tile = opq_s(ACT_OFF + ACT_LAY + A_XIN + (4 * po + w) * XT);
      const int pv = opq_v(seloob(fl + F5 + (po * 8 + co) * FS + (kh * 2 + (l >> 1) * 2 + l) * 8, 0xFull));
      v4u A[2][14];
      gemm_m2k2<14, 5, 3, true, WINA, true, nv_u>([&](auto ti) AI { return ti.v < 6 ? b0 + ti.v * 10240 : ti.v < 12 ? b1 + (ti.v - 6) * 10240 : ti.v == 12 ? b2 : b3; }, xw, A, acc,
          [&]() AI {
#pragma unroll
        for (int j = 0; j < 5; j++) { acc[0][j] = unpk4(rr[j]); pin(acc[0][j]); }
        poll<PDLY>(rws, pv, l >= 4, tag_lo, tag_hi);
        STAMP(sb + 17);
      },
          [&](auto ii, auto ti) AI { constexpr int t = ti.v, u = t % 7; return ld16h<(u == 6 ? 0 : u & 3) * 1024>(dws, l16, (ii.v ? a1 : a0) + (t == 6 ? d6 : t == 13 ? d13 : (t < 7 ? dn : df) + (u >> 2) * 4096)); },
          [&](auto fi) AI {
        constexpr int f = fi.v;
        if constexpr (f == 21) BAR0();
        if constexpr (f == 43 || f == 51) cp_arm(qo + (f - 43) * 512);
        if constexpr (f == 55 || f == 59) cp_one<2 + (f - 55) / 4>(dws, l16, wq + 4096);
      },
          [&](auto fi) AI {
        constexpr int f = fi.v;
        if constexpr (f >= 44 && !(f & 1)) cp_one<(f - 44) / 2>(dws, l16, wq + (f - 44) / 8 * 4096);
      });
      STAMP(sb + 18);
      BAR();
      xchg_read<5>(xr, acc);
      if (L == 1) {
        const rsrc_t ro_ = mk(out, 2048 * 1280);
        v2u d[5];
#pragma unroll
        for (int j = 0; j < 5; j++) d[j] = pk4(acc[0][j]);
        const int so = (64 * po + 16 * w) * 1280 + co * 160;
        __builtin_amdgcn_raw_buffer_store_b128(join16(d[0], d[1]), ro_, vo, so, 0);
        __builtin_amdgcn_raw_buffer_store_b128(join16(d[2], d[3]), ro_, vo + 64, so, 0);
        __builtin_amdgcn_raw_buffer_store_b128(join16(d[4], d[4]), ro_, vo4 + 128, so, 0);
        flag(b == 0 ? 0 : OOB);
        __builtin_amdgcn_endpgm();
      }
      store_x(acc[0], rws, tile + co * 2560, tile + 20480 + co * 128, vo, vo4, vs);
      cp_run<0, 8>(dws, l16, wq + 30720, qo + 30720);
      const int fv = opq_v(l < 16 ? l * 2 * FS + (po & 1) * 64 : (l < 32 && (po & 1) && po < 31) ? (l * 2 + 1) * FS : OOB), fo = opq_s(fl + F6 + (po >> 1) * 32 * FS + co * 8);
      WAITV(8);
      BAR();
      push(fv, fo);
      STAMP(sb + 19);
      touch(dws, l < 20 ? co * 2560 + l * 128 : l == 20 ? 20480 + co * 128 : OOB, tile);
    }
  }
}
}

extern "C" void kernel_launch(void* const* d_in, const int* in_sizes, int n_in, void* d_out, int out_size, void* d_ws, size_t ws_size, hipStream_t stream) {
  (void)hipFuncSetAttribute((const void*)fused_kernel, hipFuncAttributeMaxDynamicSharedMemorySize, LDS_BYTES);
  fused_kernel<<<256, 256, LDS_BYTES, stream>>>((const char*)d_in[0], (const char*)d_in[1], (const char*)d_in[2], (const char*)d_in[4],
                                                (const char*)d_in[5], (const char*)d_in[6], (const char*)d_in[7], (char*)d_out, (char*)d_ws);
}
```

```cpp
#include <hip/hip_runtime.h>
#include <hip/hip_bf16.h>
#include <stdint.h>
#include <utility>

#define AI __attribute__((always_inline))
#define LAS __attribute__((address_space(3)))
#define GAS __attribute__((address_space(1)))
typedef unsigned v6u __attribute__((ext_vector_type(6)));
typedef unsigned v4u __attribute__((ext_vector_type(4)));
typedef unsigned v2u __attribute__((ext_vector_type(2)));
typedef int v4i __attribute__((ext_vector_type(4)));
typedef float f32x4 __attribute__((ext_vector_type(4)));
typedef float f32x2 __attribute__((ext_vector_type(2)));
typedef __bf16 bf2 __attribute__((ext_vector_type(2)));
typedef short s4 __attribute__((ext_vector_type(4)));
typedef unsigned long long u64;
typedef __amdgpu_buffer_rsrc_t rsrc_t;

#define PROF 0

namespace {
constexpr int SC1 = 16;
#define FS 128
constexpr int FARR = 256 * FS;
constexpr int F0R = 4096, F0B = F0R + FARR, FLAY = F0B + FARR;
constexpr int F1 = 0, F2 = FARR, F5 = 2 * FARR, F6 = 3 * FARR, F4 = 5 * FARR, FLAY_SZ = 6 * FARR;
constexpr int ST_OFF = 1 << 20;
constexpr int ST_LAY = 32 * 8192;
constexpr int CSQ_OFF = ST_OFF + 2 * ST_LAY;
constexpr int CSI_OFF = CSQ_OFF + 16 * 16 * 512;
constexpr int WQ_OFF = 2 << 20, WQ_T = 153600;
constexpr int WO_OFF = WQ_OFF + 16 * WQ_T, WO_T = 102400;
constexpr int WI_OFF = WO_OFF + 8 * WO_T, WI_T = 143360;
constexpr int WU_OFF = WI_OFF + 16 * WI_T, WU_T = 138240;
constexpr int ACT_OFF = WU_OFF + 8 * WU_T;
constexpr int XT = 21504, OT = 20480, GT = 27648;
constexpr int XSZ = 128 * XT, OSZ = 128 * OT, ZSZ = 8 * 64 * 7168, GSZ = 128 * GT;
constexpr int A_XIN = 0, A_O = A_XIN + XSZ, A_X1 = A_O + OSZ, A_Z = A_X1 + XSZ, A_G = A_Z + ZSZ, ACT_LAY = A_G + GSZ;
constexpr int PROF_OFF = ACT_OFF + 2 * ACT_LAY;
constexpr int WS_USED = PROF_OFF + 256 * 256 * 4;
static_assert(FLAY + 2 * FLAY_SZ <= ST_OFF, "");
static_assert(CSI_OFF + 16 * 16 * 512 <= WQ_OFF, "");
constexpr int OOB = 0x40000000;
constexpr int LDS_BYTES = 163840;
constexpr int L_TOP = 159744;
constexpr int L_CSQ = L_TOP, L_CS2 = L_CSQ + 512, L_CS1 = L_CS2 + 512, L_XST3 = L_CS1 + 512, L_JUNK = L_XST3 + 512, L_PROF = L_JUNK + 256;
constexpr int L_XST1 = 153600, L_HST = L_XST1 + 2048;
constexpr int L_HALO = 0, L_NB = 24576;
constexpr int L_WO = 57344, L_X2 = L_WO;
constexpr int L_Z2B = 73728 - 4 * 14336;
constexpr int L_Z1B = 59392 - 3 * 14336;
constexpr int L_X3 = 43008, L_X3B = 73728, L_X5 = 139264;
constexpr int L_RED0 = 98304, L_RED1 = L_RED0 + 24576, L_RED2 = L_RED1 + 24576;
static_assert(L_RED2 >= 138240 && L_RED2 + 12288 <= L_TOP, "");
#define MAPQ 1
#define MAPO 1
#define MAPZ 1
constexpr int SHQ = MAPQ ? 4 : 2, SHO = MAPO ? 8 : 4, SHZ = MAPZ ? 8 : 4;
#define LOCF 1
#define WPRE 6
#define PSLP 0
#define PDLY 0
#define FTCH 1
#define PAUX 16
#define FAUX 16
#define NCHK 5
#define GLD 24
#define PUBF 62
#define TCH 0
#define WINA 3
constexpr int CODE_BYTES = 47104;
constexpr unsigned GTAG = 0x5A5A5A5Bu;
constexpr unsigned ONE2 = 0x3f803f80u;

template <int I> struct IC { static constexpr int v = I; };
template <int B, class F, int... I> __device__ __forceinline__ void sfor_(F& f, std::integer_sequence<int, I...>) { (f(IC<B + I>{}), ...); }
template <int I, int N, class F> __device__ __forceinline__ void sfor(F&& f) {
  if constexpr (I < N) sfor_<I>(f, std::make_integer_sequence<int, N - I>{});
}
__device__ __forceinline__ int opq_s(int x) { asm volatile("" : "+s"(x)); return x; }
__device__ __forceinline__ int opq_u(int x) { return opq_s(__builtin_amdgcn_readfirstlane(x)); }
__device__ __forceinline__ int opq_v(int x) { asm volatile("" : "+v"(x)); return x; }
template <class T> __device__ __forceinline__ void keep(const T& c) { asm volatile("" ::"v"(c)); }
template <class T> __device__ __forceinline__ void pin(T& c) { asm volatile("" : "+v"(c)); }

template <bool FIRST> __device__ __forceinline__ void mma_aa(f32x4& c, v4u a, v4u b) {
  if constexpr (FIRST) asm volatile("v_mfma_f32_16x16x32_bf16 %0, %1, %2, 0" : "=&v"(c) : "a"(a), "v"(b));
  else asm volatile("v_mfma_f32_16x16x32_bf16 %0, %1, %2, %0" : "+v"(c) : "a"(a), "v"(b));
}
template <bool FIRST, bool BA> __device__ __forceinline__ void mma(f32x4& c, v4u a, v4u b) {
  if constexpr (FIRST) {
    if constexpr (BA) asm volatile("v_mfma_f32_16x16x32_bf16 %0, %1, %2, 0" : "=&v"(c) : "v"(a), "a"(b));
    else asm volatile("v_mfma_f32_16x16x32_bf16 %0, %1, %2, 0" : "=&v"(c) : "v"(a), "v"(b));
  } else {
    if constexpr (BA) asm volatile("v_mfma_f32_16x16x32_bf16 %0, %1, %2, %0" : "+v"(c) : "v"(a), "a"(b));
    else asm volatile("v_mfma_f32_16x16x32_bf16 %0, %1, %2, %0" : "+v"(c) : "v"(a), "v"(b));
  }
}
__device__ __forceinline__ void mma_c(f32x4& d, v4u a, v4u b, f32x4 c) { asm volatile("v_mfma_f32_16x16x32_bf16 %0, %1, %2, %3" : "=&v"(d) : "v"(a), "a"(b), "v"(c)); }
#define NOP2() asm volatile("s_nop 1")
__device__ __forceinline__ void mma_settle() { asm volatile("s_nop 7" ::: "memory"); }
__device__ __forceinline__ float dot2(unsigned a, unsigned b, float c) {
  return __builtin_amdgcn_fdot2_f32_bf16(__builtin_bit_cast(bf2, a), __builtin_bit_cast(bf2, b), c, false);
}
__device__ __forceinline__ unsigned pk(float lo, float hi) {
  f32x2 v = {lo, hi};
  return __builtin_bit_cast(unsigned, __builtin_convertvector(v, bf2));
}
__device__ __forceinline__ v2u pk4(f32x4 v) { return v2u{pk(v[0], v[1]), pk(v[2], v[3])}; }
__device__ __forceinline__ float blo(unsigned u) { return __builtin_bit_cast(float, u << 16); }
__device__ __forceinline__ float bhi(unsigned u) { return __builtin_bit_cast(float, u & 0xffff0000u); }
__device__ __forceinline__ f32x4 unpk4(v2u r) { return f32x4{blo(r.x), bhi(r.x), blo(r.y), bhi(r.y)}; }
__device__ __forceinline__ float asf(unsigned u) { return __builtin_bit_cast(float, u); }
__device__ __forceinline__ unsigned asu(float f) { return __builtin_bit_cast(unsigned, f); }
__device__ __forceinline__ unsigned sel(unsigned a, unsigned b, u64 m) { unsigned r; asm("v_cndmask_b32_e64 %0, %1, %2, %3" : "=v"(r) : "v"(a), "v"(b), "s"(m)); return r; }
__device__ __forceinline__ float self(float a, float b, u64 m) { return asf(sel(asu(a), asu(b), m)); }
__device__ __forceinline__ unsigned sel0(unsigned b, u64 m) { unsigned r; asm("v_cndmask_b32_e64 %0, 0, %1, %2" : "=v"(r) : "v"(b), "s"(m)); return r; }
__device__ __forceinline__ int seloob(int b, u64 m) { int r; asm("v_cndmask_b32_e64 %0, 2.0, %1, %2" : "=v"(r) : "v"(b), "s"(m)); return r; }
__device__ __forceinline__ float vmax(float a, float b) { return __builtin_elementwise_maximum(a, b); }
__device__ __forceinline__ float gadd(float a, float b) { float r; asm volatile("v_add_f32 %0, %1, %2" : "=v"(r) : "v"(a), "v"(b)); return r; }
__device__ __forceinline__ float gmul(float a, float b) { float r; asm volatile("v_mul_f32 %0, %1, %2" : "=v"(r) : "v"(a), "v"(b)); return r; }
__device__ __forceinline__ unsigned gcvt(float a, float b) { unsigned r; asm volatile("v_cvt_pk_bf16_f32 %0, %1, %2" : "=v"(r) : "v"(a), "v"(b)); return r; }
template <int C> __device__ __forceinline__ int gaddi(int a) { int r; asm volatile("v_add_u32 %0, %1, %2" : "=v"(r) : "n"(C), "v"(a)); return r; }
constexpr u64 M_ODD = 0xAAAAAAAAAAAAAAAAull, M_G0 = 0xFFFFull, M_G1 = 0xFFFF0000ull, M_GEVEN = 0x0000FFFF0000FFFFull, M_LO = 0xFFFFFFFFull;
constexpr u64 le_mask(int add) { u64 m = 0; for (int l = 0; l < 64; l++) if (4 * (l >> 4) + (l & 1) + add <= (l & 15)) m |= 1ull << l; return m; }
__device__ __forceinline__ rsrc_t mk(const void* p, int bytes) { return __builtin_amdgcn_make_buffer_rsrc((void*)p, 0, bytes, 0x00020000); }
__device__ __forceinline__ v4i mkd(const void* p, int bytes) {
  u64 a = (u64)p; v4i r; r.x = (int)(unsigned)a; r.y = (int)((unsigned)(a >> 32) & 0xffffu); r.z = bytes; r.w = 0x00020000; return r;
}
template <int AUX> __device__ __forceinline__ v4u ld16(rsrc_t r, int v, int s) { return __builtin_amdgcn_raw_buffer_load_b128(r, v, s, AUX); }
template <int AUX> __device__ __forceinline__ v2u ld8(rsrc_t r, int v, int s) { return __builtin_amdgcn_raw_buffer_load_b64(r, v, s, AUX); }
__device__ __forceinline__ void st16(v4u d, rsrc_t r, int v, int s) { __builtin_amdgcn_raw_buffer_store_b128(d, r, v, s, SC1); }
#define BAR() asm volatile("s_waitcnt lgkmcnt(0)\n\ts_barrier" ::: "memory")
#define BAR0() asm volatile("s_barrier" ::: "memory")
#define DRAIN() asm volatile("s_waitcnt vmcnt(0)" ::: "memory")
#define WAITV(n) asm volatile("s_waitcnt vmcnt(%0)" ::"n"(n) : "memory")
#define SB() __builtin_amdgcn_sched_barrier(0)
#define FENCE() do { asm volatile("" ::: "memory"); __builtin_amdgcn_sched_barrier(0); } while (0)
#define GL(o) "\n\tbuffer_load_dwordx4 %1, %2, %3 offen offset:" #o " sc1 lds"
#define GLARGS ::"s"(lds), "v"(voff), "s"(rs), "s"(soff)
#define NTW " nt"
#define NTX ""
__device__ __forceinline__ void glds_in(v4i rs, int voff, int soff, int lds) {
  asm volatile("s_mov_b32 m0, %0\n\ts_nop 0\n\tbuffer_load_dwordx4 %1, %2, %3 offen" NTW " lds" GLARGS : "memory");
}
#define NTS ""
__device__ __forceinline__ void glds_w(v4i rs, int voff, int soff, int lds) {
  asm volatile("s_mov_b32 m0, %0\n\ts_nop 0\n\tbuffer_load_dwordx4 %1, %2, %3 offen" NTS " lds" GLARGS : "memory");
}
__device__ __forceinline__ void touch_in(v4i rs, int voff, int soff, int lds) {
  asm volatile("s_mov_b32 m0, %0\n\ts_nop 0\n\tbuffer_load_dword %1, %2, %3 offen" NTX " lds" GLARGS : "memory");
}
template <bool FIRST> __device__ __forceinline__ void mma_gram(f32x4& c, v4u a) {
  if constexpr (FIRST) asm volatile("v_mfma_f32_16x16x32_bf16 %0, %1, %1, 0" : "=&v"(c) : "a"(a));
  else asm volatile("v_mfma_f32_16x16x32_bf16 %0, %1, %1, %0" : "+v"(c) : "a"(a));
}
template <int N> __device__ __forceinline__ void gldsn(v4i rs, int voff, int soff, int lds) {
  static_assert(N >= 1 && N <= 4, "");
  if constexpr (N == 1) asm volatile("s_mov_b32 m0, %0\n\ts_nop 0" GL(0) GLARGS : "memory");
  if constexpr (N == 2) asm volatile("s_mov_b32 m0, %0\n\ts_nop 0" GL(0) GL(1024) GLARGS : "memory");
  if constexpr (N == 3) asm volatile("s_mov_b32 m0, %0\n\ts_nop 0" GL(0) GL(1024) GL(2048) GLARGS : "memory");
  if constexpr (N == 4) asm volatile("s_mov_b32 m0, %0\n\ts_nop 0" GL(0) GL(1024) GL(2048) GL(3072) GLARGS : "memory");
}
template <int N0, int N1> __device__ __forceinline__ void cp_run(v4i rs, int voff, int src, int dst) {
  if constexpr (N0 < N1) {
    constexpr int n = N1 - N0 < 4 ? N1 - N0 : 4;
    gldsn<n>(rs, voff, src + N0 * 1024, dst + N0 * 1024);
    cp_run<N0 + n, N1>(rs, voff, src, dst);
  }
}
__device__ __forceinline__ void cp_arm(int lds) { asm volatile("s_mov_b32 m0, %0" ::"s"(lds) : "memory"); }
template <int U> __device__ __forceinline__ void cp_one(v4i rs, int voff, int soff) {
  asm volatile("buffer_load_dwordx4 %0, %1, %2 offen offset:%3 sc1 lds" ::"v"(voff), "s"(rs), "s"(soff), "n"((U & 3) * 1024) : "memory");
}
__device__ __forceinline__ void touch(v4i rs, int voff, int soff) {
  asm volatile("s_mov_b32 m0, %0\n\ts_nop 0\n\tbuffer_load_dword %1, %2, %3 offen sc1 lds" ::"s"(L_JUNK), "v"(voff), "s"(rs), "s"(soff) : "memory");
}
template <int OFF, bool WS> __device__ __forceinline__ void lda16(v4u& d, v4i rs, int voff, int soff) {
  if constexpr (WS) asm volatile("buffer_load_dwordx4 %0, %1, %2, %3 offen offset:%4 sc1" : "+a"(d) : "v"(voff), "s"(rs), "s"(soff), "n"(OFF) : "memory");
  else asm volatile("buffer_load_dwordx4 %0, %1, %2, %3 offen offset:%4" : "+a"(d) : "v"(voff), "s"(rs), "s"(soff), "n"(OFF) : "memory");
}
template <int OFF> __device__ __forceinline__ void sta16(const v4u& d, v4i rs, int voff, int soff) {
  asm volatile("buffer_store_dwordx4 %0, %1, %2, %3 offen offset:%4 sc1" ::"a"(d), "v"(voff), "s"(rs), "s"(soff), "n"(OFF) : "memory");
}
template <int OFF> __device__ __forceinline__ void ldsa16(v4u& d, int addr) { asm volatile("ds_read_b128 %0, %1 offset:%2" : "+a"(d) : "v"(addr), "n"(OFF) : "memory"); }
__device__ __forceinline__ void park(v4u& d, v4u v) { asm volatile("" : "=a"(d) : "0"(v)); }
__device__ __forceinline__ void tch(v4i rs, int voff, int soff) {
  asm volatile("buffer_load_dword %0, %1, %2 offen sc1 lds" ::"v"(voff), "s"(rs), "s"(soff) : "memory");
}
template <int OFF> __device__ __forceinline__ v4u ld16h(v4i rs, int voff, int soff) {
  v4u r; asm volatile("buffer_load_dwordx4 %0, %1, %2, %3 offen offset:%4 sc1" : "=v"(r) : "v"(voff), "s"(rs), "s"(soff), "n"(OFF) : "memory"); return r;
}
template <int OFF> __device__ __forceinline__ v2u ld8h(v4i rs, int voff, int soff) {
  v2u r; asm volatile("buffer_load_dwordx2 %0, %1, %2, %3 offen offset:%4 sc1" : "=v"(r) : "v"(voff), "s"(rs), "s"(soff), "n"(OFF) : "memory"); return r;
}
template <int OFF> __device__ __forceinline__ float ld4h(v4i rs, int voff, int soff) {
  float r; asm volatile("buffer_load_dword %0, %1, %2, %3 offen offset:%4 sc1" : "=v"(r) : "v"(voff), "s"(rs), "s"(soff), "n"(OFF) : "memory"); return r;
}
__device__ __forceinline__ int qslot_off(int w) { return (w * 7680) & ~1023; }
template <class T> __device__ __forceinline__ LAS T* lp(int off) { return (LAS T*)(size_t)(unsigned)off; }
__device__ __forceinline__ v4u lds16(int off) { return *lp<v4u>(off); }
__device__ __forceinline__ v2u lds8(int off) { return *lp<v2u>(off); }
__device__ __forceinline__ f32x4 ldsf4(int off) { return *lp<f32x4>(off); }
__device__ __forceinline__ float ldsf(int off) { return *lp<float>(off); }
template <int DLY = 0> __device__ __forceinline__ void poll(rsrc_t rws, int voff, bool off, unsigned lo, unsigned hi) {
  if (DLY) __builtin_amdgcn_s_sleep(DLY);
  for (unsigned spins = 0; spins < (1u << 18); ++spins) {
    asm volatile("" ::: "memory");
    const v2u v = ld8<PAUX>(rws, voff, 0);
    if (__all(off || (v.x == lo && v.y == hi))) break;
    if (PSLP) __builtin_amdgcn_s_sleep(PSLP);
  }
  asm volatile("" ::: "memory");
}
__device__ __forceinline__ v2u sw16(unsigned a, unsigned b) { auto r = __builtin_amdgcn_permlane16_swap(a, b, false, false); return v2u{r[0], r[1]}; }
__device__ __forceinline__ v2u sw32(unsigned a, unsigned b) { auto r = __builtin_amdgcn_permlane32_swap(a, b, false, false); return v2u{r[0], r[1]}; }
__device__ __forceinline__ v4u join16(v2u x, v2u y) { const v2u a = sw16(x.x, y.x), b = sw16(x.y, y.y); return v4u{a.x, b.x, a.y, b.y}; }
template <int CTRL> __device__ __forceinline__ float dpp(float v) {
  return __builtin_bit_cast(float, __builtin_amdgcn_update_dpp(0, __builtin_bit_cast(int, v), CTRL, 0xf, 0xf, true));
}
__device__ __forceinline__ float xsum2(float s, float q) {
  const v2u a = sw32(asu(s), asu(q)); const float t = asf(a.x) + asf(a.y);
  const v2u b = sw16(asu(t), asu(t)); return asf(b.x) + asf(b.y);
}
__device__ __forceinline__ f32x2 finish_stat(float s, float q, float inv_n) {
  const float m = s * inv_n;
  return f32x2{m, __builtin_amdgcn_rsqf(fmaxf(q * inv_n - m * m, 0.f) + 1e-5f)};
}
__device__ __forceinline__ f32x4 gelu4(f32x4 v) {
  const float k1 = -2.0f * 0.7978845608028654f * 1.4426950408889634f, k2 = k1 * 0.044715f;
  f32x4 e = v * ((v * v) * k2 + k1);
  asm("" : "+v"(e));
#pragma unroll
  for (int i = 0; i < 4; i++) e[i] = __builtin_amdgcn_exp2f(e[i]);
  e += 1.0f;
#pragma unroll
  for (int i = 0; i < 4; i++) e[i] = __builtin_amdgcn_rcpf(e[i]);
  return v * e;
}
__device__ __forceinline__ f32x2 rowsum8(f32x4 s, f32x4 q) {
  float a, b, c, d;
  asm volatile("s_nop 1\n\t"
      "v_add_f32_dpp %0, %4, %4 row_ror:8 row_mask:0xf bank_mask:0x3\n\t"
      "v_add_f32_dpp %1, %5, %5 row_ror:8 row_mask:0xf bank_mask:0x3\n\t"
      "v_add_f32_dpp %2, %8, %8 row_ror:8 row_mask:0xf bank_mask:0x3\n\t"
      "v_add_f32_dpp %3, %9, %9 row_ror:8 row_mask:0xf bank_mask:0x3\n\t"
      "v_add_f32_dpp %0, %6, %6 row_ror:8 row_mask:0xf bank_mask:0xc\n\t"
      "v_add_f32_dpp %1, %7, %7 row_ror:8 row_mask:0xf bank_mask:0xc\n\t"
      "v_add_f32_dpp %2, %10, %10 row_ror:8 row_mask:0xf bank_mask:0xc\n\t"
      "v_add_f32_dpp %3, %11, %11 row_ror:8 row_mask:0xf bank_mask:0xc\n\t"
      "v_add_f32_dpp %0, %0, %0 row_half_mirror row_mask:0xf bank_mask:0x5\n\t"
      "v_add_f32_dpp %0, %1, %1 row_half_mirror row_mask:0xf bank_mask:0xa\n\t"
      "v_add_f32_dpp %2, %2, %2 row_half_mirror row_mask:0xf bank_mask:0x5\n\t"
      "v_add_f32_dpp %2, %3, %3 row_half_mirror row_mask:0xf bank_mask:0xa\n\t"
      "v_add_f32_dpp %0, %0, %0 quad_perm:[1,0,3,2] row_mask:0xf bank_mask:0xf\n\t"
      "s_nop 0\n\t"
      "v_add_f32_dpp %2, %2, %2 quad_perm:[1,0,3,2] row_mask:0xf bank_mask:0xf\n\t"
      "v_add_f32_dpp %0, %0, %0 quad_perm:[2,3,0,1] row_mask:0xf bank_mask:0xf\n\t"
      "s_nop 0\n\t"
      "v_add_f32_dpp %2, %2, %2 quad_perm:[2,3,0,1] row_mask:0xf bank_mask:0xf"
      : "=&v"(a), "=&v"(b), "=&v"(c), "=&v"(d)
      : "v"(s[0]), "v"(s[1]), "v"(s[2]), "v"(s[3]), "v"(q[0]), "v"(q[1]), "v"(q[2]), "v"(q[3]));
  return f32x2{a, c};
}
__device__ __forceinline__ v4u bperm4(int idx, v4u v) {
  v4u r;
  r.x = __builtin_amdgcn_ds_bpermute(idx, v.x); r.y = __builtin_amdgcn_ds_bpermute(idx, v.y);
  r.z = __builtin_amdgcn_ds_bpermute(idx, v.z); r.w = __builtin_amdgcn_ds_bpermute(idx, v.w);
  return r;
}
__device__ __forceinline__ v2u trd(int off) { return __builtin_bit_cast(v2u, __builtin_amdgcn_ds_read_tr16_b64_v4i16(lp<s4>(off))); }
#define STAMP(i) do {} while (0)
#define CSTAMP(i) do {} while (0)
#define LANE() const int l = opq_v((int)threadIdx.x) & 63, l16 = l * 16, g = l >> 4, c16 = l & 15; (void)g; (void)c16; (void)l16; \
  const int w = opq_s(w_), po = opq_s(po_), co = opq_s(co_), pz = opq_s(pz_), cz = opq_s(cz_), P = opq_s(P_), h = opq_s(h_); \
  (void)w; (void)po; (void)co; (void)pz; (void)cz; (void)P; (void)h; \
  const int kh = w & 1, w2 = w >> 1; (void)kh; (void)w2; \
  auto flag = [&](int off) AI { __builtin_amdgcn_raw_buffer_store_b64(v2u{tag_lo, tag_hi}, rws, seloob(0, 1ull), w == 0 ? off : OOB, FAUX); }; (void)flag; \
  auto push = [&](int voff, int off) AI { __builtin_amdgcn_raw_buffer_store_b64(v2u{tag_lo, tag_hi}, rws, voff, w == 0 ? off : OOB, SC1); }; (void)push;
#define QROT (2 * cz)
#define KQ ((w + QROT) & 3)
#define RROT 3
__device__ __forceinline__ int pan(int e, int pz, int kq) {
  int r = e - 2 + RROT * (pz & 1); r = r >= 6 ? r - 6 : r;
  const int u = e < 2 ? e : r + 2;
  return (((pz >> 3) + (u >> 1)) & 3) * 8 + 2 * kq + (u & 1);
}

template <int NS, int NCT, int WIN, int (*NV)(int)> constexpr int younger(int t, int fe) {
  const int n = 2 * t + 1, f0 = n < 2 * WIN ? -1 : (n - 2 * WIN) * 2;
  int c = n < 2 * WIN ? 2 * WIN - 1 - n : 0;
  for (int f = f0 + 1; f < fe; f++) c += NV(f) + (f % 2 == 0 && 2 * WIN + f / 2 < 2 * NS);
  return c;
}
template <int NS, int NCT, int TL, bool TR, int WIN, bool INIT, int (*NV)(int), class BA, class GO, class LD, class HA, class HB>
__device__ __forceinline__ void gemm_m2k2(BA&& badr, int xw, v4u (&A)[2][NS], f32x4 (&acc)[2][NCT], GO&& go, LD&& lda, HA&& ha, HB&& hb) {
  constexpr int NF = NS * NCT, NT = TL * NCT, NL = NF - NT;
  static_assert(NF % 2 == 0 && (WIN == 0 || 4 * (NS - WIN) <= NL + 1), "");
  v4u B[NF];
  auto ldB = [&](auto fi) AI { constexpr int f = fi.v; if constexpr (f < NF) B[f] = lds16(badr(IC<f / NCT>{}) + (f % NCT) * 1024); };
  auto mm = [&](auto ii, auto fi) AI {
    constexpr int i = ii.v, f = fi.v, t = f / NCT, ct = f % NCT;
    if (TR) mma<t == 0 && !(INIT && i == 0), false>(acc[i][ct], B[f], A[i][t]); else mma<t == 0 && !(INIT && i == 0), false>(acc[i][ct], A[i][t], B[f]);
    SB();
  };
  sfor<0, 6>([&](auto fi) AI { ldB(IC<(fi.v ^ 1)>{}); SB(); });
  go();
  if constexpr (WIN > 0) sfor<0, WIN>([&](auto ti) AI { A[1][ti.v] = lda(IC<1>{}, ti); A[0][ti.v] = lda(IC<0>{}, ti); });
  SB();
  sfor<0, NL>([&](auto fi) AI {
    constexpr int f = fi.v;
    if constexpr (WIN > 0 && f % NCT == 0) WAITV((younger<NS, NCT, WIN, NV>(f / NCT, f)));
    mm(IC<0>{}, fi);
    ldB(IC<((f + 6) ^ 1)>{});
    ha(fi);
    SB();
    mm(IC<1>{}, fi);
    if constexpr (WIN > 0) {
      constexpr int n = 2 * WIN + f / 2;
      if constexpr (f % 2 == 0 && n < 2 * NS) A[(n & 1) ^ 1][n >> 1] = lda(IC<((n & 1) ^ 1)>{}, IC<(n >> 1)>{});
    }
    if constexpr (f >= NL - 4) ldB(IC<((f + 10) ^ 1)>{});
    hb(fi);
    SB();
  });
  sfor<NL, NF>([&](auto fi) AI {
    constexpr int f = fi.v;
    if constexpr (WIN > 0 && f % NCT == 0) WAITV((younger<NS, NCT, WIN, NV>(f / NCT, NL)));
    mm(IC<1>{}, fi);
    ldB(IC<((f + 10) ^ 1)>{});
    SB();
  });
  sfor<NL, NF>([&](auto fi) AI {
    constexpr int k = fi.v - NL, D = NCT == 5 ? 3 : 1, SP = TL == 1 ? 1 : 2;
    mm(IC<0>{}, fi);
    if constexpr (k >= D && (k - D) % SP == 0 && (k - D) / SP < NCT) { pin(acc[1][(k - D) / SP]); *lp<f32x4>(xw + (k - D) / SP * 1024) = acc[1][(k - D) / SP]; }
    else ha(fi);
    if constexpr (k == NT - 1) sfor<(NT - 1 - D) / SP + 1, NCT>([&](auto ki) AI { pin(acc[1][ki.v]); *lp<f32x4>(xw + ki.v * 1024) = acc[1][ki.v]; });
    SB();
  });
}
template <int NCT> __device__ __forceinline__ void xchg_read(int src, f32x4 (&acc)[2][NCT]) {
#pragma unroll
  for (int j = 0; j < NCT; j++) acc[1][j] = ldsf4(src + j * 1024);
  SB();
#pragma unroll
  for (int j = 0; j < NCT; j++) { pin(acc[0][j]); acc[0][j] += acc[1][j]; }
}
__device__ __forceinline__ void store_x(const f32x4 (&x)[5], rsrc_t rws, int so, int ss, int vo, int vo4, int vs) {
  v2u d[5];
#pragma unroll
  for (int j = 0; j < 5; j++) d[j] = pk4(x[j]);
  float s0 = 0.f, q0 = 0.f;
#pragma unroll
  for (int j = 0; j < 5; j++) { s0 = dot2(d[j].x, ONE2, s0); q0 = dot2(d[j].x, d[j].x, q0); s0 = dot2(d[j].y, ONE2, s0); q0 = dot2(d[j].y, d[j].y, q0); }
  st16(join16(d[0], d[1]), rws, vo, so);
  st16(join16(d[2], d[3]), rws, vo + 1024, so);
  st16(join16(d[4], d[4]), rws, vo4 + 2048, so);
  __builtin_amdgcn_raw_buffer_store_b32(asu(xsum2(s0, q0)), rws, vs, ss, SC1);
}
constexpr int nv_none(int) { return 0; }
constexpr int nv_o(int f) { return (f >= 17 && f < 25 && (f & 1)) + (f >= 32 && f < 35); }
constexpr int nv_z(int f) { return (f == 1 || f == 3) + (f >= 32 && f % 3 == 2); }
constexpr int nv_u(int f) { return f >= 44 && !(f & 1); }
constexpr int QPRE = 9;
constexpr int q_step(int n) { return n % 9 == 0 ? n / 9 * 4 : n / 9 * 4 + (n % 9 - 1) / 2; }
constexpr int q_si(int f) { return f == 33 ? 0 : f == 37 ? 1 : f == 39 ? 2 : f == 62 ? 3 : f == 65 ? 4 : f == 68 ? 5 : f == 69 ? 6 : f == 71 ? 7 : -1; }
struct QTab { int ld[160], fr[160], cum[161], wt[20]; };
constexpr QTab q_tab(bool L0) {
  QTab t{}; int n = QPRE, k = 0;
  for (int i = 0; i < 160; i++) {
    t.ld[i] = -1; t.fr[i] = -100;
    if (i % 2 == 0) { if (n < 45 && q_step(n) <= i / 8 + 6) t.ld[i] = n++; else t.fr[i] = k++; }
    t.cum[i + 1] = t.cum[i] + (t.ld[i] >= 0) + (t.fr[i] >= 0 && t.fr[i] < 8) + (q_si(i) >= 0) + (i >= 76 && i < 96 && i % 4 == 1) + (i >= 108 && i < 156 && i % 3 == 0) +
                   (L0 ? (i < 96 && i % 4 == 3) + (i == 49) : (i >= 8 && i < 24 && i % 4 == 3));
  }
  for (int s = 0; s < 20; s++) {
    const int last = s / 4 * 9 + 2 + (s & 3) * 2;
    int f0 = -1;
    for (int i = 0; i < 160; i++) if (t.ld[i] == last) f0 = i;
    t.wt[s] = (last < QPRE ? QPRE - 1 - last + (L0 ? 8 : 0) : 0) + t.cum[8 * s] - t.cum[f0 + 1];
  }
  return t;
}
constexpr QTab QT0 = q_tab(true), QT1 = q_tab(false);
constexpr int s_vm(int f) { return (f == 73 || f == 97) + (f == 4) + (f >= 40 && f < 50 && !(f & 1)) + (f >= 98 && f < 108); }
constexpr int s_wait(int f) { int c = f < 14 ? 13 - f : 0; for (int i = f < 14 ? 0 : f - 13; i < f; i++) c += (i < 98) + s_vm(i); return c; }
constexpr int sp_s(int f) { return f < 28 ? f / 7 : f < 100 ? 4 + ((f - 28) % 24) / 2 : 4 + (f - 100); }
constexpr int sp_j(int f) { return f < 28 ? f % 7 : f < 100 ? (f - 28) / 24 * 2 + (f & 1) : 6; }

__global__ __launch_bounds__(256, 1) void fused_kernel(const char* __restrict__ x0, const char* __restrict__ wqkv, const char* __restrict__ wo,
                                                       const char* __restrict__ win, const char* __restrict__ wsp, const char* __restrict__ bsp,
                                                       const char* __restrict__ wout, char* out, char* ws) {
  {
    u64 pc; asm volatile("s_getpc_b64 %0" : "=s"(pc));
    const int i = (blockIdx.x >> 3) * 16 + (threadIdx.x >> 4);
    touch_in(mkd((const void*)(pc & ~127ull), CODE_BYTES), (threadIdx.x & 15) == 0 ? i * 128 : OOB, 0, L_JUNK);
  }
  const int w_ = __builtin_amdgcn_readfirstlane(threadIdx.x >> 6);
  const int b = blockIdx.x, xcd = b & 7, jb = b >> 3;
  const int pa = xcd * 4 + (jb & 3), ca = jb >> 2, pb = (xcd >> 1) * 8 + (jb & 7), cb = (xcd & 1) * 4 + (jb >> 3);
  const int po_ = MAPO ? pb : pa, co_ = MAPO ? cb : ca, pz_ = MAPZ ? pb : pa, cz_ = MAPZ ? cb : ca;
  const int P_ = MAPQ ? (xcd >> 1) * 4 + (jb & 3) : xcd * 2 + (jb & 1), h_ = MAPQ ? (xcd & 1) * 8 + (jb >> 2) : jb >> 1;
  const rsrc_t rws = mk(ws, WS_USED);
  const v4i dws = mkd(ws, WS_USED);
  STAMP(0); CSTAMP(62);
  unsigned tag_lo, tag_hi;
  v4u W[4][16];
#pragma unroll
  for (int r = 0; r < 4; r++)
#pragma unroll
    for (int s = 0; s < 16; s++) asm volatile("" : "=a"(W[r][s]));
  {
    LANE();
    {
      const v4i dq = mkd(wqkv, 640 * 3840);
      const int vq = g * 3840 + c16 * 16 + (c16 < 5 ? 1280 : c16 < 10 ? -80 : 2400), s0 = (P * 40 + w * 12) * 3840 + 80 * h, kill = w == 3 ? OOB : 0;
      glds_in(dq, vq, s0, w * 3072);
      glds_in(dq, vq, (s0 + 4 * 3840) | kill, w * 3072 + 1024);
      glds_in(dq, vq, (s0 + 8 * 3840) | kill, w * 3072 + 2048);
    }
    const int xrt = 8 * P + (h >> 1), xc = 4 * (h & 1) + w;
    {
      const v4i dx = mkd(x0, 2048 * 1280);
#pragma unroll
      for (int i = 0; i < 4; i++) {
        const int k = (c16 - g - 4 * i) & 15;
        glds_in(dx, k < 10 ? g * 1280 + k * 16 : OOB, (xrt * 16 + 4 * i) * 1280 + xc * 160, 12288 + w * 4096 + i * 1024);
      }
    }
    float ep0 = ld4h<0>(dws, 0, 0), ep1 = ld4h<4>(dws, 0, 0);
    const v4i dw = mkd(wsp, 2048 * 4096);
    const int wst = opq_s(57344 + w * 25344), wb0 = opq_s(64 * pz * 4096);
    int wv[2];
#pragma unroll
    for (int i = 0; i < 2; i++) wv[i] = opq_v((l >> 5) * 4096 + pan(4 * i + ((l >> 3) & 3), pz, KQ) * 128 + ((l & 7) ^ (l >> 5)) * 16);
    auto ldW = [&](auto ni) AI { constexpr int n = ni.v; glds_w(dw, wv[(n >> 3) & 1], wb0 + ((w + (n >> 4)) & 3) * 65536 + (n & 7) * 8192, wst + (n % 24) * 1056); };
    sfor<0, WPRE>(ldW);
    const int wrd = opq_v(wst + (c16 >> 1) * 1056 + (c16 & 1) * 512 + (g ^ (c16 & 1)) * 16);
    auto rdW = [&](auto ni) AI { constexpr int n = ni.v; ldsa16<(n % 24 >> 3) * 8448 + (n & 7) * 64>(W[n >> 4][n & 15], wrd); };
    const int ct = 2 * w + (g & 1), pq = l & 3;
    const int cb_ = ct == 2 ? 32 + (pq & 1) * 8 + (pq >> 1) * 80 : ct == 7 ? 192 + (pq & 1) * 8 : (ct < 2 ? ct * 48 : ((ct - 1) >> 1) * 80 + ((ct - 1) & 1) * 48) + pq * 8;
    const int trq = opq_v(((g >> 1) * 8 + (c16 >> 2)) * 256 + cb_);
    int qv[3], xa[3];
#pragma unroll
    for (int i = 0; i < 3; i++) {
      const int k = 2 * i + (g >> 1), kap = 5 * P + k;
      const int kr_ = (kap >> 2) + (LOCF && h >= 8 ? (kap >= 48 ? -12 : kap < 32 ? 12 : 0) : 0);
      qv[i] = opq_v((k < 5 && (ct < 7 || c16 < 8)) ? kr_ * 7680 + ct * 1024 + ((kap & 3) * (ct == 7 ? 8 : 16) + c16) * 16 : OOB);
      xa[i] = opq_v(12288 + w * 4096 + c16 * 256 + ((i * 4 + g + c16) & 15) * 16);
    }
    WAITV(WPRE);
    pin(ep0); pin(ep1);
    STAMP(1);
    BAR0();
    {
      v4u xf[3]; v2u lo[3], hi[3];
#pragma unroll
      for (int i = 0; i < 3; i++) { lo[i] = trd(trq + i * 4096); hi[i] = trd(trq + i * 4096 + 1024); xf[i] = lds16(xa[i]); }
      SB();
      const int tile = ACT_OFF + A_XIN + xrt * XT;
#pragma unroll
      for (int i = 0; i < 3; i++) {
        st16(v4u{lo[i].x, lo[i].y, hi[i].x, hi[i].y}, rws, qv[i], WQ_OFF + h * WQ_T);
        st16(xf[i], rws, (i == 2 ? seloob(l16, M_LO) : l16) + i * 1024, tile + xc * 2560);
      }
      float s = 0.f, q = 0.f, cs = 0.f;
#pragma unroll
      for (int i = 0; i < 3; i++) {
#pragma unroll
        for (int e = 0; e < 4; e++) { s = dot2(xf[i][e], ONE2, s); q = dot2(xf[i][e], xf[i][e], q); }
        cs = dot2(lo[i].x, ONE2, cs); cs = dot2(lo[i].y, ONE2, cs); cs = dot2(hi[i].x, ONE2, cs); cs = dot2(hi[i].y, ONE2, cs);
      }
      __builtin_amdgcn_raw_buffer_store_b32(asu(xsum2(s, q)), rws, seloob(c16 * 8 + (g >> 1) * 4, M_GEVEN), tile + 20480 + xc * 128, SC1);
      const v2u t = sw32(asu(cs), asu(cs));
      __builtin_amdgcn_raw_buffer_store_b32(asu(asf(t.x) + asf(t.y)), rws, seloob(l * 4, M_LO), CSQ_OFF + (h * 16 + P) * 512 + w * 128, SC1);
    }
    FENCE();
    sfor<WPRE, 16>(ldW);
    const int tau = jb, sl = xcd, stg = 28672 + w * 7168;
    const int ty = tau < 8 ? 0 : tau < 24 ? 1 : 2, ot = tau - ty * 8 - (ty >> 1) * 8, cc = ot & 7;
    const int o_kg = opq_u((ty < 2 ? 10 * sl : sl < 4 ? 14 * sl : 4 + 13 * sl) + w2), o_n = opq_u((ty < 2 ? 10 : sl < 4 ? 14 : 13) - w2);
    {
      const int ldb = ty == 1 ? 3456 : 1280, lim = ty != 1 ? 9 : cc < 6 ? 13 : 11, chi = kh * 8 + (l & 7);
      const v4i dsrc = mkd(ty == 0 ? wo : ty == 1 ? win : wout, (ty == 2 ? 864 : 640) * ldb);
      const int ov = opq_v((l >> 3) * ldb + (chi < lim ? chi : lim) * 16);
      const int o0 = opq_u(o_kg * 8 * ldb + (ty != 1 ? ot * 160 : (ot < 8 ? 1728 : 0) + (cc < 6 ? 7 * cc : 6 + 6 * cc) * 32));
      const int od = opq_u(16 * ldb), k5 = opq_u(o_n > 10 ? 0 : OOB), k6 = opq_u(o_n > 12 ? 0 : OOB);
      const u64 epoch = ((u64)(unsigned)__builtin_amdgcn_readfirstlane(asu(ep1)) << 32) | (unsigned)__builtin_amdgcn_readfirstlane(asu(ep0));
      const u64 tag = epoch * 0x9E3779B97F4A7C15ull + 0xD1B54A32D192ED03ull;
      tag_lo = opq_s((unsigned)tag); tag_hi = opq_s((unsigned)(tag >> 32));
#pragma unroll
      for (int k = 0; k < 7; k++) glds_in(dsrc, ov, (o0 + k * od) | (k == 5 ? k5 : k == 6 ? k6 : 0), stg + k * 1024);
    }
    WAITV(23 - WPRE);
    BAR0();
    flag(F0R + (P * 16 + h) * FS);
    STAMP(2);
    FENCE();
    sfor<16, 24>(ldW);
    {
      int o_s[7];
      const int o_v = opq_v(kh * 4 + g < (ty == 1 ? 7 : 5) ? (kh * 4 + g) * 1024 + c16 * 16 : OOB);
      const int o_dst = ty == 0 ? WO_OFF + ot * WO_T : ty == 1 ? WI_OFF + ot * WI_T : WU_OFF + ot * WU_T, kb = ty < 2 ? 10 : 14, kstr = ty == 1 ? 7168 : 5120;
      const int kap = o_kg + 2 * l, ks = kap >> 2, hi = ks >= kb, kq = ks - (hi ? kb : 0), hs = kb >> 1, kw = kq >= hs, far = hi != (LOCF && cc >= 4);
      const int pos = 2 * (kq - (kw ? hs : 0) + (far ? hs - (ty == 2 && kw && !hi) : 0)) + kw;
      const int v = 2 * l < o_n ? o_dst + pos * kstr + (kap & 3) * 256 : OOB;
#pragma unroll
      for (int i = 0; i < 7; i++) o_s[i] = opq_s(__builtin_amdgcn_readlane(v, i));
      const int o_c = opq_u(ty == 1 ? CSI_OFF + (ot * 16 + sl * 2 + w2) * 512 + kh * 256 : OOB);
      WAITV(9);
      sfor<0, 16>(rdW);
      const int tro = stg + (c16 >> 2) * 128 + g * 32 + (l & 3) * 8;
      float ocs = 0.f;
#pragma unroll
      for (int i = 0; i < 7; i++) {
        const v2u lo = trd(tro + i * 1024), hi2 = trd(tro + i * 1024 + 512);
        if (i < 5) { ocs = dot2(lo.x, ONE2, ocs); ocs = dot2(lo.y, ONE2, ocs); ocs = dot2(hi2.x, ONE2, ocs); ocs = dot2(hi2.y, ONE2, ocs); }
        st16(v4u{lo.x, lo.y, hi2.x, hi2.y}, rws, o_v, o_s[i]);
      }
      __builtin_amdgcn_raw_buffer_store_b32(asu(ocs), rws, l * 4, o_c, SC1);
      st16(v4u{0, 0, 0, 0}, rws, seloob(g * ST_LAY + c16 * 16, M_LO), ST_OFF + po * 8192 + w * 2048 + co * 256);
    }
    FENCE();
    asm volatile("s_waitcnt lgkmcnt(0)" ::: "memory");
    sfor<24, 40>(ldW);
    sfor<0, 3>([&](auto bi) AI {
      constexpr int n = 40 + 8 * bi.v;
      WAITV(bi.v == 0 ? 25 : 16);
      sfor<n - 24, n - 16>(rdW);
      asm volatile("s_waitcnt lgkmcnt(0)" ::: "memory");
      sfor<n, n + 8>(ldW);
    });
    if constexpr (FTCH) touch(dws, seloob(F0B + l * FARR + b * FS, 0x1FFFull), w == 1 ? 0 : OOB);
    STAMP(3);
    {
      const bool on = l < 32 || (l < 34 && P > 0);
      poll(rws, on ? F0R + (l < 16 ? P * 16 + l : l < 32 ? (l - 16) * 16 + h : P * 16 + l - 34) * FS : OOB, !on, tag_lo, tag_hi);
    }
    sfor<40, 64>(rdW);
    BAR();
    flag(F0B + b * FS);
  }

#pragma unroll 1
  for (int Lr = 0;; Lr = 1) {
    const int L = opq_s(Lr), kill1 = L << 30;
    const int act = ACT_OFF + L * ACT_LAY, fl = FLAY + L * FLAY_SZ;
    const int sb = L * 24 + 5;
    f32x4 z1[7];
    unsigned braw;
    v2u rr[5];
    constexpr int RZ = 14;
    v4u Z[RZ];
    { LANE();
      const int qo = qslot_off(w), wq = WQ_OFF + h * WQ_T + qo;
      int vb[4], vh[4];
#pragma unroll
      for (int i = 0; i < 4; i++) { const int k = (i + w) & 3; vb[i] = opq_v(l16 + k * 7680); vh[i] = opq_v((g * 8 + (l & 7)) * 16 + k * 7680); }
      const int h2 = w < 2 ? w + 4 : w;
      const int c4 = opq_v(c16 * 4), g16 = opq_v(g * 16), tv = opq_v(seloob(l * 128, (1ull << 35) - 1));
      const int wos = opq_s(WO_OFF + co * WO_T), wzs = opq_s(WI_OFF + cz * WI_T);
      f32x4 acc[2][8];
      float mt[3], rt_[3], csn[3], hcn; f32x4 mn[3], rn[3], cst[5], ha[2][4], hcs;
      auto qkv = [&](auto l0) AI {
        constexpr bool L0 = l0.v;
        v4u A[2][20], Hh[5], B[8];
        const int gsh = opq_s(LOCF ? (h >> 3) * 12288 : 0);
        int va[4], spo, xoff, hoff, fbo, cqo, rv;
        auto ldB = [&](auto fi) AI { constexpr int f = fi.v, t = f >> 3, ct = f & 7; B[f & 7] = lds16((ct < 7 ? vb[t & 3] : vh[t & 3]) + ((t >> 2) & 1) * 30720 + ct * 1024); };
        auto ldA = [&](auto ni) AI {
          constexpr int n = ni.v, t = q_step(n), i = (n % 9) & 1;
          const int go = (t >> 2) * 4096 + (t < 8 ? gsh : t < 12 ? 0 : -gsh);
          if constexpr (n % 9 == 0) Hh[t >> 2] = ld16h<0>(dws, va[0], hoff + go);
          else A[i][t] = ld16h<0>(dws, va[t & 3], xoff + i * XT + go);
        };
        constexpr const QTab& QT = L0 ? QT0 : QT1;
        rv = opq_v((g >> 1) * 256 + c16 * 16 + (g & 1) * 8);
#pragma unroll
        for (int i = 0; i < 4; i++) va[i] = opq_v(l16 + ((i + w) & 3) * 1024);
        spo = opq_v(g < 2 ? (2 * w + 1 + g) * XT + c16 * 8 : (g == 2 && P > 0) ? c16 * 8 : OOB);
        xoff = opq_s(act + A_XIN + (8 * P + 2 * w) * XT); hoff = opq_s(P > 0 ? act + A_XIN + (8 * P - 1) * XT : OOB);
        if constexpr (L0) {
          STAMP(sb - 1);
          cp_run<0, 8>(dws, l16, wq, qo);
          sfor<0, QPRE>(ldA);
          cp_run<0, 8>(dws, l16, wq + 30720, qo + 30720);
          cqo = opq_v((l >> 5) * 4096 + (l & 31) * 4);
          fbo = opq_v(l < 32 ? F0B + (((l >> 3) * 8 + (((l >> 3) + 1) & 2 ? cz : co)) * 8 + (l & 7)) * FS : l < 48 ? F0R + ((po >> 1) * 16 + l - 32) * FS : OOB);
          WAITV(8 + QPRE);
          BAR0();
          sfor<0, 6>([&](auto fi) AI { ldB(IC<(fi.v ^ 1)>{}); SB(); });
        } else {
          sfor<0, 6>([&](auto fi) AI { ldB(IC<(fi.v ^ 1)>{}); SB(); });
          const bool on = l < 8 || (l < 16 && P > 0);
          poll(rws, on ? FLAY + F6 + (P * 16 + h) * 2 * FS + (l < 8 ? w2 * 64 : FS - 64) + l * 8 : OOB, !on, tag_lo, tag_hi);
          STAMP(sb - 1);
          sfor<0, QPRE>(ldA);
          SB();
        }
        f32x4 hac[6];
        v2u sp[8], fb; float ss[4], sq[4], cq[8]; int xa, xb;
        sfor<0, 160>([&](auto fi) AI {
          constexpr int f = fi.v, t = f >> 3, ct = f & 7, hc = ct < 3 ? ct : ct - 2;
          constexpr bool halo = (t & 3) == 0 && (ct < 3 || ct >= 5);
          constexpr int fr = QT.fr[f], qn = QT.ld[f];
          constexpr int si = q_si(f); (void)si;
          const v4u bb = B[f & 7];
          auto mm = [&](f32x4& d, const v4u& x) AI {
            if constexpr (ct < 5) mma<t == 0, false>(d, bb, x); else mma<t == 0, false>(d, x, bb);
            SB();
          };
          if constexpr (ct == 0) WAITV(QT.wt[t]);
          mm(acc[0][ct], A[0][t]);
          if constexpr (L0 && f % 32 == 26) WAITV(QT.cum[f] - QT.cum[f - 26]);
          if constexpr (f == 95) WAITV(QT.cum[95] - QT.cum[72]);
          if constexpr (f == 26 || f == 90 || f == 100 || f == 130 || (L0 && (f == 58 || f == 122))) BAR0();
          if constexpr (((f + 6) ^ 1) < 160) ldB(IC<((f + 6) ^ 1)>{});
          if constexpr ((t & 7) >= 4 && t < 16 && ct == 3) vb[t & 3] = gaddi<61440>(vb[t & 3]);
          if constexpr ((t & 7) >= 4 && t < 16 && ct == 5) vh[t & 3] = gaddi<61440>(vh[t & 3]);
          if constexpr (L0 && f % 16 == 2 && f < 96) cp_arm(qo + (2 + f / 32) * 30720 + (f & 16) * 256);
          if constexpr (!L0 && f == 10) cp_arm(qo + 3 * 30720 + 4096);
          if constexpr (!L0 && (fr == 0 || fr == 4)) cp_arm(qo + 4 * 30720 + fr * 1024);
          if constexpr (f >= 107 && f < 155 && (f - 107) % 12 == 0) cp_arm(L_WO + w * 8192 + (f - 107) / 24 * 32768 + ((f - 107) / 12 & 1) * 4096);
          {
            if constexpr (f >= 96 && f < 104 && !(f & 1)) ss[(f - 96) / 2] = gadd(asf(sp[(f - 96) / 2].x), asf(sp[(f - 96) / 2 + 4].x));
            if constexpr (f == 104 || f == 106) ss[(f - 104) / 2] = gadd(ss[(f - 104) / 2], ss[(f - 104) / 2 + 2]);
            if constexpr (f == 108) ss[0] = gadd(ss[0], ss[1]);
            if constexpr (f == 110) { ss[0] = ss[0] * (1.f / 640.f); pin(ss[0]); }
            if constexpr (f == 111) ss[1] = gmul(ss[0], ss[0]);
            if constexpr (f == 113) { sq[0] = sq[0] * (1.f / 640.f) - ss[1]; pin(sq[0]); }
            if constexpr (f == 114) { sq[0] = fmaxf(sq[0], 0.f); pin(sq[0]); }
            if constexpr (f == 116) { sq[0] += 1e-5f; pin(sq[0]); }
            if constexpr (f == 118) { sq[0] = __builtin_amdgcn_rsqf(sq[0]); pin(sq[0]); }
            if constexpr (f == 119) xa = opq_v(L_XST1 + w * 512 + l * 4);
            if constexpr (f == 120) *lp<float>(xa) = ss[0];
            if constexpr (f == 121) *lp<float>(xa + 256) = sq[0];
          }
          if constexpr (f == 126) xb = opq_v(L_XST1 + w * 512 + g16);
          if constexpr (f >= 127 && f < 130) mn[f - 127] = ldsf4(xb + (f - 127) * 64);
          if constexpr (f == 130 || f == 131 || f == 133) rn[f == 133 ? 2 : f - 130] = ldsf4(xb + 256 + (f == 133 ? 2 : f - 130) * 64);
          if constexpr (f == 134) xa = opq_v(L_HALO + w * 6144 + l16);
          if constexpr (f == 142) xb = opq_v(L_CSQ + g16);
          if constexpr (f >= 143 && f < 148) cst[f - 143] = ldsf4(xb + (f - 143) * 64);
          if constexpr (f == 148) xb = opq_v(xb + w * 64);
          if constexpr (f == 149) hcs = ldsf4(xb);
          if constexpr (f == 150) xa = opq_v(L_HALO + w * 1024 + l16);
          if constexpr (f == 151) xb = opq_v(L_HALO + h2 * 1024 + l16);
          if constexpr (f >= 152 && f < 156) ha[0][f - 152] = ldsf4(xa + (f - 152) * 6144);
          SB();
          mm(acc[1][ct], A[1][t]);
          if constexpr (L0) {
            if constexpr (f < 96 && f % 4 == 3) cp_one<f / 4>(dws, l16, wq + (2 + f / 32) * 30720 + (f & 16) * 256);
            if constexpr (fr >= 0 && fr < 8) cq[fr] = ld4h<fr * 512>(dws, cqo, CSQ_OFF + h * 8192 + w * 128);
            if constexpr (f == 49) fb = ld8h<0>(dws, fbo, 0);
            if constexpr (f == 75) { WAITV(QT.cum[76] - QT.cum[50]); pin(fb); if (!__all(l >= 48 || (fb.x == tag_lo && fb.y == tag_hi))) poll(rws, fbo, l >= 48, tag_lo, tag_hi); }
            if constexpr (f == 94) {
#pragma unroll
              for (int i = 0; i < 8; i++) pin(cq[i]);
              const float cs = ((cq[0] + cq[1]) + (cq[2] + cq[3])) + ((cq[4] + cq[5]) + (cq[6] + cq[7]));
              const v2u t2 = sw32(asu(cs), asu(cs));
              *lp<float>(L_CSQ + w * 128 + (l & 31) * 4) = asf(t2.x) + asf(t2.y);
            }
          }
          {
            if constexpr (qn >= 0) ldA(IC<qn>{});
            if constexpr (!L0 && f >= 8 && f < 24 && f % 4 == 3) cp_one<(f - 8) / 4>(dws, l16, wq + 3 * 30720 + 4096);
            if constexpr (!L0 && fr >= 0 && fr < 8) cp_one<fr & 7>(dws, l16, wq + 4 * 30720 + (fr & 4) * 1024);
            if constexpr (si >= 0) sp[si] = ld8h<si * 128>(dws, spo, act + A_XIN + (8 * P - 1) * XT + 20480);
            if constexpr (f >= 76 && f < 96 && f % 4 == 1) rr[(f - 76) / 4] = ld8h<(f - 76) / 4 * 512>(dws, rv, act + A_XIN + (4 * po + w) * XT + co * 2560);
            if constexpr (f >= 96 && f < 104 && (f & 1)) sq[(f - 96) / 2] = gadd(asf(sp[(f - 96) / 2].y), asf(sp[(f - 96) / 2 + 4].y));
            if constexpr (f == 105 || f == 107) sq[(f - 105) / 2] = gadd(sq[(f - 105) / 2], sq[(f - 105) / 2 + 2]);
            if constexpr (f == 109) sq[0] = gadd(sq[0], sq[1]);
          }
          if constexpr (f == 123) xa = opq_v(L_XST1 + w * 512 + c4);
          if constexpr (f >= 108 && f < 156 && f % 3 == 0) cp_one<(f - 108) / 3>(dws, l16, wos + w * 8192 + (f - 108) / 24 * 32768 + ((f - 108) / 12 & 1) * 4096);
          if constexpr (f == 125 || f == 127 || f == 131) { constexpr int i = f == 131 ? 2 : (f - 125) / 2; mt[i] = ldsf(xa + i * 64); rt_[i] = ldsf(xa + 256 + i * 64); }
          if constexpr (f >= 136 && f < 142) *lp<f32x4>(xa + (f - 136) * 1024) = hac[f - 136];
          if constexpr (f == 143) xa = opq_v(L_CSQ + c4);
          if constexpr (f == 144) { csn[0] = ldsf(xa + 320); csn[1] = ldsf(xa + 384); }
          if constexpr (f == 146) { csn[2] = ldsf(xa + 448); xa = opq_v(xa + (h2 + 2) * 64); }
          if constexpr (f == 148) hcn = ldsf(xa);
          if constexpr (f == 150) BAR0();
          if constexpr (f >= 152 && f < 156) ha[1][f - 152] = ldsf4(xb + (f - 152) * 6144);
          if constexpr (halo) { SB(); mm(hac[hc], Hh[t >> 2]); }
          SB();
        });
        mma_settle();
#pragma unroll
        for (int j = 0; j < 8; j++) { pin(acc[0][j]); pin(acc[1][j]); }
      };
      if (L == 0) qkv(IC<1>{}); else qkv(IC<0>{});
      STAMP(sb + 0);
      const int nbw = opq_v(L_NB + l * 8), w3s = wos + 65536 + w * 9216, w3d = L_WO + 65536 + w * 9216;
      if constexpr (TCH) touch(dws, tv, wzs + ((pz & 7) * 4 + w) * 4480);
      {
        const f32x4 a0 = (ha[0][0] + ha[0][1]) + (ha[0][2] + ha[0][3]), a1 = (ha[1][0] + ha[1][1]) + (ha[1][2] + ha[1][3]);
        *lp<v2u>(nbw + (w < 3 ? w * 512 : 15360)) = pk4((a0 - mt[2] * hcs) * rt_[2]);
        *lp<v2u>(nbw + (w != 2 ? h2 * 512 : 15360)) = pk4((a1 - mn[2] * hcn) * rn[2]);
      }
      v2u tk[2][8];
      auto fix = [&](int i, int ct) AI { tk[i][ct] = ct < 5 ? pk4((acc[i][ct] - mt[i] * cst[ct]) * rt_[i]) : pk4((acc[i][ct] - mn[i] * csn[ct - 5]) * rn[i]); };
      fix(1, 0); fix(1, 1); fix(1, 2); fix(1, 5); fix(1, 6); fix(1, 7);
      const int nbr = opq_v(nbw + w * 3072);
#pragma unroll
      for (int i = 0; i < 6; i++) *lp<v2u>(nbr + 3072 + i * 512) = tk[1][i < 3 ? i : i + 2];
      SB();
      fix(0, 0); fix(0, 1); fix(0, 2); fix(1, 3); fix(1, 4);
      BAR();
      v2u nb[6];
#pragma unroll
      for (int i = 0; i < 6; i++) { nb[i] = lds8(nbr + i * 512); asm volatile("" ::: "memory"); }
      cp_run<0, 3>(dws, l16, w3s, w3d);
      STAMP(sb + 1);
      SB();
      fix(0, 3); fix(0, 4);
      v6u K6[3]; v4u Q[2][2];
      {
        const v2u qx = sw32(tk[0][2].x, tk[1][2].x), qy = sw32(tk[0][2].y, tk[1][2].y);
        const unsigned q1x = sw32(qx.y, qx.y).y, q1y = sw32(qy.y, qy.y).y;
        Q[0][0] = v4u{tk[0][3].x, tk[0][3].y, sel0(qx.y, M_G0), sel0(qy.y, M_G0)};
        Q[0][1] = v4u{sel0(qx.y, M_G1), sel0(qy.y, M_G1), tk[0][4].x, tk[0][4].y};
        Q[1][0] = v4u{tk[1][3].x, tk[1][3].y, sel0(q1x, M_G0), sel0(q1y, M_G0)};
        Q[1][1] = v4u{sel0(q1x, M_G1), sel0(q1y, M_G1), tk[1][4].x, tk[1][4].y};
      }
#pragma unroll
      for (int i = 0; i < 2; i++) {
        K6[i + 1] = v6u{tk[i][0].x, tk[i][0].y, tk[i][2].x, tk[i][2].y, tk[i][1].x, tk[i][1].y};
        pin(Q[i][0]); pin(Q[i][1]); pin(K6[i + 1]);
      }
      K6[0] = v6u{nb[0].x, nb[0].y, nb[2].x, nb[2].y, nb[1].x, nb[1].y};
      pin(K6[0]);
      NOP2();
      f32x4 S[4][2];
#pragma unroll
      for (int u = 3; u >= 0; u--)
#pragma unroll
        for (int k = 1; k >= 0; k--) {
          const v6u kk = K6[(u >> 1) + k];
          mma<true, false>(S[u][k], (u & 1) ? __builtin_shufflevector(kk, kk, 2, 3, 4, 5) : __builtin_shufflevector(kk, kk, 0, 1, 2, 3), Q[u >> 1][u & 1]);
        }
      SB();
      cp_run<3, 6>(dws, l16, w3s, w3d);
      unsigned sel[4];
      {
        const unsigned zz = 0x0c0c0c0cu, a = ::sel(0x0c0c0504u, 0x05040c0cu, M_ODD), d = ::sel(0x0c0c0706u, 0x07060c0cu, M_ODD);
        sel[2] = ::sel(zz, a, le_mask(0)); sel[3] = ::sel(zz, d, le_mask(2));
        sel[0] = ::sel(a, zz, le_mask(0)); sel[1] = ::sel(d, zz, le_mask(2));
      }
      const int ovo = (g & 1) * 768 + (g >> 1) * 256 + c16 * 16, ovt = seloob(g * OT + 512 + c16 * 16, M_LO);
      fix(0, 5); fix(0, 6); fix(0, 7);
      v6u V6[3];
#pragma unroll
      for (int j = 0; j < 3; j++) { V6[j] = v6u{nb[3 + j].x, nb[3 + j].y, tk[0][5 + j].x, tk[0][5 + j].y, tk[1][5 + j].x, tk[1][5 + j].y}; pin(V6[j]); }
      SB();
#pragma unroll
      for (int u = 0; u < 8; u++) pin(S[u >> 1][u & 1]);
      if (P + w == 0) { S[0][0] = f32x4{-1e30f, -1e30f, -1e30f, -1e30f}; pin(S[0][0]); S[1][0] = S[0][0]; }
      float s0[4], s1[4], mx[4];
#pragma unroll
      for (int u = 0; u < 4; u++) {
        const f32x4 a = S[u][0], bq = S[u][1];
        s0[u] = self(self(a[0], a[1], M_ODD), self(bq[0], bq[1], M_ODD), le_mask(0));
        s1[u] = self(self(a[2], a[3], M_ODD), self(bq[2], bq[3], M_ODD), le_mask(2));
        mx[u] = vmax(s0[u], s1[u]);
      }
      auto spread = [&](float t, float (&o)[4]) AI {
        const v2u a = sw16(asu(t), asu(t)), x = sw32(a.x, a.x), y = sw32(a.y, a.y);
        o[0] = asf(x.x); o[1] = asf(x.y); o[2] = asf(y.x); o[3] = asf(y.y);
      };
      const float kk = 0.22360679774997896f * 1.4426950408889634f;
      {
        const v2u a = sw32(asu(mx[0]), asu(mx[1])), bq = sw32(asu(mx[2]), asu(mx[3]));
        const v2u d = sw16(asu(vmax(asf(a.x), asf(a.y))), asu(vmax(asf(bq.x), asf(bq.y))));
        spread(vmax(asf(d.x), asf(d.y)), mx);
      }
      SB();
      cp_run<6, 9>(dws, l16, w3s, w3d);
      float sm4[4];
#pragma unroll
      for (int u = 0; u < 4; u++) {
        const f32x2 e = (f32x2{s0[u], s1[u]} - mx[u]) * kk;
        s0[u] = __builtin_amdgcn_exp2f(e.x); s1[u] = __builtin_amdgcn_exp2f(e.y);
        sm4[u] = s0[u] + s1[u];
      }
      {
        const v2u a = sw32(asu(sm4[0]), asu(sm4[1])), bq = sw32(asu(sm4[2]), asu(sm4[3]));
        const v2u d = sw16(asu(asf(a.x) + asf(a.y)), asu(asf(bq.x) + asf(bq.y)));
        spread(__builtin_amdgcn_rcpf(asf(d.x) + asf(d.y)), sm4);
      }
      v4u pf[4];
#pragma unroll
      for (int u = 0; u < 4; u++) {
        const f32x2 pn = f32x2{s0[u], s1[u]} * sm4[u];
        const unsigned pp = pk(pn.x, pn.y);
        pf[u] = v4u{__builtin_amdgcn_perm(pp, 0u, sel[0]), __builtin_amdgcn_perm(pp, 0u, sel[1]), __builtin_amdgcn_perm(pp, 0u, sel[2]), __builtin_amdgcn_perm(pp, 0u, sel[3])};
        pin(pf[u]);
      }
      NOP2();
      f32x4 Om[4], Ot[4];
#pragma unroll
      for (int u = 0; u < 4; u++) { const v6u v = V6[u & 1]; mma<true, false>(Om[u], (u >> 1) ? __builtin_shufflevector(v, v, 2, 3, 4, 5) : __builtin_shufflevector(v, v, 0, 1, 2, 3), pf[u]); }
#pragma unroll
      for (int u = 0; u < 4; u++) { const v6u v = V6[2]; mma<true, false>(Ot[u], (u >> 1) ? __builtin_shufflevector(v, v, 2, 3, 4, 5) : __builtin_shufflevector(v, v, 0, 1, 2, 3), pf[u]); }
      asm volatile("s_nop 3");
#pragma unroll
      for (int u = 0; u < 4; u++) pin(Om[u]);
      const int so = act + A_O + (8 * P + 2 * w) * OT + 5 * h * 256;
      st16(join16(pk4(Om[0]), pk4(Om[1])), rws, ovo, so);
      st16(join16(pk4(Om[2]), pk4(Om[3])), rws, ovo, so + OT);
      SB();
#pragma unroll
      for (int u = 0; u < 4; u++) pin(Ot[u]);
      {
        const v2u x0_ = pk4(Ot[0]), y0_ = pk4(Ot[1]), x1_ = pk4(Ot[2]), y1_ = pk4(Ot[3]);
        st16(v4u{sw16(x0_.x, x1_.x).x, sw16(x0_.y, x1_.y).x, sw16(y0_.x, y1_.x).y, sw16(y0_.y, y1_.y).y}, rws, ovt, so);
      }
      STAMP(sb + 2);
#pragma unroll
      for (int j = 0; j < 5; j++) pin(rr[j]);
      cp_run<0, 6>(dws, l16, wzs + w * 6144, w * 6144);
      cp_run<0, 4>(dws, l16, wzs + 40960 + w * 4096, 40960 + w * 4096);
      const int fv = opq_v(seloob(l * FS, M_G0)), fo = opq_s(fl + F1 + P * 16 * FS + h * 8);
      WAITV(10);
      BAR();
      push(fv, fo);
      STAMP(sb + 3);
      touch(dws, (g < 2 && c16 < 10) ? g * OT + c16 * 128 : OOB, so);
      cp_run<0, 4>(dws, l16, wzs + 24576 + w * 4096, 24576 + w * 4096);
    }
    { LANE();
      f32x4 acc[2][5];
      const int tile = opq_s(act + A_X1 + (4 * po + w) * XT), wzs = opq_s(WI_OFF + cz * WI_T);
      const int vo = opq_v((g & 1) * 512 + (g >> 1) * 256 + c16 * 16), vo4 = opq_v(seloob(vo, M_GEVEN)), vs = opq_v(seloob(c16 * 8 + (g >> 1) * 4, M_GEVEN));
      const int xw = opq_v(L_X2 + (w ^ 1) * 5120 + l16), xr = opq_v(L_X2 + w * 5120 + l16);
      const int lo_ = LOCF ? (co >> 2) * 10240 : 0, dl = opq_s(10240 - 2 * lo_);
      const int a0 = opq_s(act + A_O + (4 * po + w) * OT + kh * 5120 + lo_), a1 = opq_s(act + A_O + (4 * po + (w ^ 1)) * OT + kh * 5120 + lo_);
      const int b0 = opq_v(L_WO + l16 + kh * 5120), b1 = opq_v(b0 + 51200);
      const int pv = opq_v(seloob(fl + F1 + (po * 8 + co) * FS + (kh * 4 + (l >> 2) * 4 + l) * 8, 0xFFull));
      v4u A[2][10], ci[4];
      const int civ = opq_v(g * 512 + c16 * 16), cis = opq_s((CSI_OFF + (w2 * 8 + cz) * 8192 + kh * 256) | kill1);
      gemm_m2k2<10, 5, 3, true, WINA, true, nv_o>([&](auto ti) AI { return ti.v < 5 ? b0 + ti.v * 10240 : b1 + (ti.v - 5) * 10240; }, xw, A, acc,
          [&]() AI {
#pragma unroll
        for (int j = 0; j < 5; j++) { acc[0][j] = unpk4(rr[j]); pin(acc[0][j]); }
        poll<PDLY>(rws, pv, l >= 8, tag_lo, tag_hi);
        STAMP(sb + 4);
      },
          [&](auto ii, auto ti) AI { return ld16h<(ti.v % 5 & 3) * 1024>(dws, l16, (ii.v ? a1 : a0) + (ti.v >= 5 ? dl : 0) + (ti.v % 5 >> 2) * 4096); },
          [&](auto fi) AI {
        constexpr int f = fi.v;
        if constexpr (f == 13 || f == 31) BAR0();
        if constexpr (f == 32 || f == 36) cp_arm(L_Z2B + 71680 + w * 7168 + (f - 32) * 1024);
        if constexpr (f == 35) cp_one<3>(dws, l16, wzs + 71680 + w * 7168);
        if constexpr (f == 39 || f == 43 || f == 47) cp_one<(f - 39) / 4>(dws, l16, wzs + 71680 + w * 7168 + 4096);
      },
          [&](auto fi) AI {
        constexpr int f = fi.v;
        if constexpr (f >= 17 && f < 25 && (f & 1)) ci[(f - 17) / 2] = ld16h<((f - 17) / 2 & 1) * 2048>(dws, civ, cis + (f - 17) / 4 * 4096);
        if constexpr (f >= 32) cp_one<f - 32>(dws, l16, wzs + 71680 + w * 7168);
      });
      STAMP(sb + 5);
      BAR();
      xchg_read<5>(xr, acc);
      store_x(acc[0], rws, tile + co * 2560, tile + 20480 + co * 128, vo, vo4, vs);
#pragma unroll
      for (int j = 0; j < 4; j++) pin(ci[j]);
      if (L == 0) {
        f32x4 t = (__builtin_bit_cast(f32x4, ci[0]) + __builtin_bit_cast(f32x4, ci[1])) + (__builtin_bit_cast(f32x4, ci[2]) + __builtin_bit_cast(f32x4, ci[3]));
#pragma unroll
        for (int e = 0; e < 4; e++) { const v2u a = sw32(asu(t[e]), asu(t[e])); const float u = asf(a.x) + asf(a.y); const v2u d = sw16(asu(u), asu(u)); t[e] = asf(d.x) + asf(d.y); }
        *lp<f32x4>(L_CS2 + w * 256 + c16 * 16) = t;
      }
      const int zc = 100352 + ((w * 10752) & ~1023);
      cp_run<0, 11>(dws, l16, wzs + zc, L_Z2B + zc);
      const int fv = opq_v(seloob(l * FS, 0xFFull)), fo = opq_s(fl + F2 + po * 8 * FS + co * 8);
      WAITV(11);
      BAR();
      push(fv, fo);
      STAMP(sb + 6);
      touch(dws, l < 20 ? co * 2560 + l * 128 : l == 20 ? 20480 + co * 128 : OOB, tile);
      cp_run<0, 4>(dws, l16, wzs + 57344 + ((w * 3584) & ~1023), L_Z2B + 57344 + ((w * 3584) & ~1023));
    }
    { LANE();
      const int wz1 = opq_s(WI_OFF + (8 + cz) * WI_T);
      const int lo_ = LOCF ? (cz >> 2) * 10240 : 0, dl = opq_s(10240 - 2 * lo_);
      const int a0 = opq_s(act + A_X1 + (4 * pz + w) * XT + kh * 5120 + lo_), a1 = opq_s(act + A_X1 + (4 * pz + (w ^ 1)) * XT + kh * 5120 + lo_);
      int xw = opq_v(L_X3 + (w ^ 1) * 7168 + l16), xr = opq_v(L_X3 + w * 7168 + l16);
      const int gso = opq_v(seloob(l * 4, 0x1111111111111111ull)), gro = opq_v(((l >> 2) & 3) * 256 + (4 * g + (l & 3)) * 16);
      const int zvo = opq_v((g & 1) * 1024 + (g >> 1) * 256 + c16 * 16), spo = opq_v(g * 256 + c16 * 8);
      const int sta = opq_v(L_XST3 + w * 128 + (g & 1) * 64 + c16 * 4), stb = opq_v(L_XST3 + w * 128 + g * 16), tv = opq_v(l * 128);
      const int b0 = opq_v(l16 + kh * 7168), b1 = opq_v(b0 + L_Z1B + 3 * 14336), b2 = opq_v(b1 + 4 * 14336);
      static_assert(L_Z1B == L_Z2B, "");
      const int sps = opq_s(act + A_X1 + (4 * pz + w) * XT + 20480), za0 = opq_s((w * 10752) & ~1023);
      float cs2[7];
#pragma unroll
      for (int j = 0; j < 7; j++) { cs2[j] = ldsf(L_CS2 + c16 * 4 + j * 64); pin(cs2[j]); }
      braw = __builtin_amdgcn_raw_buffer_load_b16(mk(bsp, 4096), c16 * 2, (64 * pz + 16 * w) * 2, 0);
      v4u A[2][10]; f32x4 acc[2][7];
      v2u sp0, sp1, u_;
      float m1, r1, st_; f32x4 m4, r4;
      gemm_m2k2<10, 7, 1, false, WINA, false, nv_z>([&](auto ti) AI { return ti.v < 4 ? b0 + ti.v * 14336 : ti.v < 7 ? b1 + (ti.v - 3) * 14336 : b2 + (ti.v - 7) * 14336; }, xw, A, acc,
          [&]() AI { poll<PDLY>(rws, seloob(fl + F2 + (pz * 8 + cz) * FS + l * 8, 0xFFull), l >= 8, tag_lo, tag_hi); pin(braw); STAMP(sb + 7); },
          [&](auto ii, auto ti) AI { return ld16h<(ti.v % 5 & 3) * 1024>(dws, l16, (ii.v ? a1 : a0) + (ti.v >= 5 ? dl : 0) + (ti.v % 5 >> 2) * 4096); },
          [&](auto fi) AI {
        constexpr int f = fi.v;
        if constexpr (f == 15 || f == 31) BAR0();
        if constexpr (f == 32 || f == 44 || f == 54) cp_arm(za0 + (f - 30) / 12 * 4096);
        if constexpr (f == 47) { u_ = sw32(asu(m1), asu(r1)); pin(u_); }
        if constexpr (f == 48) { st_ = gadd(asf(u_.x), asf(u_.y)); }
        if constexpr (f == 49) { u_ = sw16(asu(st_), asu(st_)); pin(u_); }
        if constexpr (f == 50) { st_ = gadd(asf(u_.x), asf(u_.y)); }
        if constexpr (f == 51) { u_ = sw32(asu(st_), asu(st_)); pin(u_); }
        if constexpr (f == 52) { m1 = asf(u_.x) * (1.f / 640.f); pin(m1); }
        if constexpr (f == 53) { st_ = gmul(m1, m1); }
        if constexpr (f == 55) { r1 = asf(u_.y) * (1.f / 640.f) - st_; pin(r1); }
        if constexpr (f == 56) { r1 = fmaxf(r1, 0.f); pin(r1); }
        if constexpr (f == 57) { r1 += 1e-5f; pin(r1); }
        if constexpr (f == 58) { r1 = __builtin_amdgcn_rsqf(r1); pin(r1); }
        if constexpr (f == 60) *lp<float>(sta) = self(r1, m1, M_GEVEN);
        if constexpr (f == 63) { m4 = ldsf4(stb); r4 = ldsf4(stb + 64); }
      },
          [&](auto fi) AI {
        constexpr int f = fi.v;
        if constexpr (f == 1) sp0 = ld8h<0>(dws, spo, sps);
        if constexpr (f == 3) sp1 = ld8h<128>(dws, spo, sps);
        if constexpr (f == 43) { m1 = gadd(asf(sp0.x), asf(sp1.x)); }
        if constexpr (f == 45) { r1 = gadd(asf(sp0.y), asf(sp1.y)); }
        if constexpr (f >= 32 && f % 3 == 2) cp_one<(f - 32) / 3>(dws, l16, wz1 + za0 + (f - 32) / 12 * 4096);
      });
      STAMP(sb + 8);
      BAR();
      xchg_read<7>(xr, acc);
      BAR();
      f32x4 z2[7], zs, zq;
      const int gz_ = 43008 + ((w * 17920) & ~1023), gs = wz1 + gz_, gd = L_Z1B + gz_;
      auto gel = [&](auto l0) AI {
        sfor<0, 7>([&](auto ji) AI {
          constexpr int j = ji.v;
          if constexpr (j < 6) cp_run<3 * j, 3 * j + 3>(dws, l16, gs, gd);
          SB();
          f32x4 v = gelu4((acc[0][j] - m4 * cs2[j]) * r4);
          if constexpr (j == 6) v *= cz < 6 ? 1.f : 0.f;
          z2[j] = v;
          if (j == 0) { zs = v; zq = v * v; } else { zs += v; zq += v * v; }
          pin(zs); pin(zq);
          SB();
        });
      };
      gel(IC<0>{});
      const int sto = ST_OFF + L * ST_LAY + pz * 8192 + w * 2048;
      {
        const f32x2 t = rowsum8(zs, zq);
        st16(v4u{asu(t.x), GTAG, asu(t.y), GTAG}, rws, gso, sto + cz * 256);
      }
      STAMP(sb + 9);
      const int zo = opq_s(act + A_Z + (cz * 64 + 2 * pz) * 7168), z8 = opq_s(114688 + w * 7168), us = opq_s(WU_OFF + co * WU_T);
      const int fv = opq_v(seloob(0, 1ull)), fo = opq_s(fl + F4 + (cz * 32 + pz) * FS);
      f32x4 cs1[7];
      int tmp;
      xw += L_X3B - L_X3; xr += L_X3B - L_X3;
      v4u ga, gb;
      WAITV(19);
      BAR0();
      gemm_m2k2<10, 7, 2, true, 0, false, nv_none>([&](auto ti) AI { return ti.v < 3 ? b0 + ti.v * 14336 : ti.v < 7 ? b1 + (ti.v - 3) * 14336 : b2 + (ti.v - 7) * 14336; }, xw, A, acc,
          [&]() AI {}, [&](auto, auto) AI { return v4u{}; },
          [&](auto fi) AI {
        constexpr int f = fi.v;
        if constexpr (f == 0 || f == 8) cp_arm(L_Z1B + z8 + f * 512);
        if constexpr (f == 13) { WAITV(8); BAR0(); }
        if constexpr (f == 21 || f == 45) BAR0();
        if constexpr (f == 41 || f == 49) cp_arm(w * 7168 + (f - 41) * 512);
        if constexpr (f == PUBF) { WAITV(7); BAR0(); push(fv, fo); }
        if constexpr (f == 47) tmp = opq_v(L_CS1 + g * 16);
        if constexpr (f >= 56 && !(f & 1)) cs1[(f - 56) / 2] = ldsf4(tmp + (f - 56) * 32);
      },
          [&](auto fi) AI {
        constexpr int f = fi.v;
        if constexpr (f < 14 && !(f & 1)) cp_one<f / 2>(dws, l16, wz1 + z8 + f / 8 * 4096);
        if constexpr (f >= 42 && !(f & 1)) cp_one<(f - 42) / 2>(dws, l16, us + w * 7168 + (f - 42) / 8 * 4096);
        if constexpr (f == GLD) ga = ld16h<0>(dws, gro, sto);
        if constexpr (f == GLD + 2) gb = ld16h<1024>(dws, gro, sto);
        if constexpr (f == GLD + 14) {
          DRAIN(); pin(ga); pin(gb);
          for (unsigned spins = 0; spins < (1u << 18); ++spins) {
            if (__all(ga.y == GTAG && ga.w == GTAG && gb.y == GTAG && gb.w == GTAG)) break;
            asm volatile("" ::: "memory");
            ga = ld16<SC1>(rws, gro, sto); gb = ld16<SC1>(rws, gro + 1024, sto);
          }
          STAMP(sb + 10);
          float ts = asf(ga.x) + asf(gb.x), tq = asf(ga.z) + asf(gb.z);
          ts += dpp<0x128>(ts); tq += dpp<0x128>(tq); ts += dpp<0x124>(ts); tq += dpp<0x124>(tq);
          f32x2 zz = finish_stat(ts, tq, 1.f / 864.f);
          zz.x = -zz.x * zz.y;
          const f32x4 mm = {dpp<0x00>(zz.x), dpp<0x55>(zz.x), dpp<0xAA>(zz.x), dpp<0xFF>(zz.x)};
          const f32x4 rs = {dpp<0x00>(zz.y), dpp<0x55>(zz.y), dpp<0xAA>(zz.y), dpp<0xFF>(zz.y)};
          const int so = zo + w2 * 7168 + kh * 512;
          v2u d[7];
#pragma unroll
          for (int j = 0; j < 7; j++) d[j] = pk4(z2[j] * rs + mm);
          st16(join16(d[0], d[1]), rws, zvo, so);
          st16(join16(d[2], d[3]), rws, zvo + 2048, so);
          st16(join16(d[4], d[5]), rws, zvo, so + 4096);
          st16(join16(d[6], d[6]), rws, seloob(zvo, M_GEVEN) + 2048, so + 4096);
          STAMP(sb + 11);
        }
      });
      BAR();
      STAMP(sb + 12);
      touch(dws, seloob(tv, (1ull << 28) - 1), zo + w * 3584);
      xchg_read<7>(xr, acc);
      const int z0 = opq_s(act + A_Z + (cz * 32 + pan(0, pz, KQ)) * 14336);
      const int fno = opq_v(seloob(fl + F4 + (cz * 32 + (pz & 24) + 2 * KQ + l) * FS, 0x3ull));
      auto ldZ0 = [&](auto fi) AI { Z[fi.v] = ld16h<(fi.v & 3) * 1024>(dws, l16, z0 + (fi.v >> 2) * 4096); };
      v2u fn_;
      sfor<0, 7>([&](auto ji) AI {
        constexpr int j = ji.v;
        if constexpr (j < 3) cp_run<(5 * j + 1) / 2, j == 2 ? 7 : (5 * j + 6) / 2>(dws, l16, us + 28672 + w * 7168, 28672 + w * 7168);
        if constexpr (j == NCHK - 2) fn_ = ld8<SC1>(rws, fno, 0);
        if constexpr (j == NCHK) { if (!__all(l >= 2 || (fn_.x == tag_lo && fn_.y == tag_hi))) poll(rws, fno, l >= 2, tag_lo, tag_hi); FENCE(); }
        if constexpr (j >= NCHK) sfor<4 * (j - NCHK), 4 * (j - NCHK) + 4>(ldZ0);
        SB();
        z1[j] = gelu4((acc[0][j] - m1 * cs1[j]) * r1);
        pin(z1[j]);
        SB();
      });
      STAMP(sb + 13);
      sfor<4 * (7 - NCHK), RZ>(ldZ0);
    }
    { LANE();
      const int ct0 = cz < 6 ? 7 * cz : 6 + 6 * cz, kq = opq_s(KQ);
      STAMP(sb + 14);
      f32x4 acc[4][7];
      const int so = act + A_G + (4 * pz + w) * GT + ct0 * 512;
      f32x4 bias = blo(braw);
      pin(bias);
      int zp[8], za, rw[3], rd, vo, rv; unsigned d[4];
      constexpr int RN = RZ + 1;
      v4u Y[RN];
      sfor<0, RZ>([&](auto fi) AI { Y[fi.v] = Z[fi.v]; });
      const int ut = opq_s(WU_OFF + co * WU_T + 57344 + w * 10240);
      const int zb = opq_s(act + A_Z + cz * 32 * 14336);
      zp[0] = opq_s(zb + pan(0, pz, kq) * 14336);
      const int fzo = opq_v(seloob(fl + F4 + (cz * 32 + (l >> 1) * 8 + 2 * kq + (l & 1)) * FS, 0xFFull));
      v2u fz;
      NOP2();
      SB();
      sfor<0, 112>([&](auto fi) AI {
        constexpr int f = fi.v, s = sp_s(f), j = sp_j(f), fn = f + RZ;
        constexpr int q = f < 52 ? -1 : (f - 52) / 24, k = f < 52 ? 0 : (f - 52) % 24, buf = (q & 1) ? L_RED1 - L_RED0 : 0;
        auto fin = [&](auto ui) AI {
          constexpr int u = ui.v;
          if constexpr (q >= 0 && u >= 0 && u < 36) {
            constexpr int jj = 2 * q + (u < 32 ? (u >> 2) & 1 : (u - 32) >> 1), e = u & 3, st = u >> 3;
            if constexpr (st == 0) acc[0][jj][e] = gadd(acc[0][jj][e], acc[1][jj][e]);
            if constexpr (st == 1) acc[2][jj][e] = gadd(acc[2][jj][e], acc[3][jj][e]);
            if constexpr (st == 2) acc[0][jj][e] = gadd(acc[0][jj][e], acc[2][jj][e]);
            if constexpr (st == 3) acc[0][jj][e] = gmul(acc[0][jj][e], z1[jj][e]);
            if constexpr (st == 4) d[u - 32] = gcvt(acc[0][jj][(u & 1) * 2], acc[0][jj][(u & 1) * 2 + 1]);
          }
        };
        constexpr int per = q == 2 ? 7 : 4, u0 = (k - (q == 2 ? 9 : 10)) * per;
        const v4u z = Y[f % RN];
        WAITV(s_wait(f));
        mma<s == 0, true>(acc[1][j], z, W[1][s]);
        SB();
        if constexpr (fn < 28 && !(fn & 1)) za = opq_s(zp[0] + fn * 1024);
        if constexpr (fn >= 28 && fn < 112 && (fn >= 100 || !(fn & 1))) za = opq_s(zp[sp_s(fn) / 2] + ((sp_s(fn) & 1) * 7 + (sp_j(fn) & 6)) * 1024);
        if constexpr (q >= 0 && k >= 1 && k < 4) *lp<f32x4>(rw[k - 1] + buf) = acc[k][2 * q];
        if constexpr (q >= 0 && k >= 6 && k < 9) acc[k - 5][2 * q] = ldsf4(rd + buf + (k - 6) * 2048);
        fin(IC<u0>{});
        if constexpr (q == 2) fin(IC<u0 + 1>{});
        SB();
        mma<s == 0, true>(acc[2][j], z, W[2][s]);
        SB();
        if constexpr (fn < 112) Y[fn % RN] = ld16h<(fn < 100 ? fn & 1 : 0) * 1024>(dws, l16, za);
        if constexpr (f >= 98 && f < 108) cp_one<f - 98>(dws, l16, ut + (f - 98) / 4 * 4096);
        if constexpr (q == 2) { fin(IC<u0 + 2>{}); fin(IC<u0 + 3>{}); }
        SB();
        mma<s == 0, true>(acc[3][j], z, W[3][s]);
        SB();
        if constexpr (q >= 0 && k >= 2 && k < 5) *lp<f32x4>(rw[k - 2] + buf + 1024) = acc[k - 1][2 * q + 1];
        if constexpr (q >= 0 && k >= 6 && k < 9) acc[k - 5][2 * q + 1] = ldsf4(rd + buf + (k - 6) * 2048 + 1024);
        fin(IC<u0 + per - 3>{}); fin(IC<u0 + per - 2>{});
        if constexpr (f == 4) fz = ld8h<0>(dws, fzo, 0);
        if constexpr (f == 13) { WAITV(9); pin(fz); if (!__all(l >= 8 || (fz.x == tag_lo && fz.y == tag_hi))) poll(rws, fzo, l >= 8, tag_lo, tag_hi); }
        if constexpr (f >= 40 && f < 50 && !(f & 1)) rr[(f - 40) / 2] = ld8h<(f - 40) * 256>(dws, rv, act + A_X1 + (4 * po + w) * XT + co * 2560);
        if constexpr (f == 97 || f == 101 || f == 105) cp_arm(57344 + w * 10240 + (f - 97) * 1024);
        if constexpr (f % 4 == 2 && f / 4 + 2 < 8) zp[f / 4 + 2] = opq_s(pan(f / 4 + 2, pz, kq));
        if constexpr (f % 4 == 3 && f / 4 + 2 < 8) zp[f / 4 + 2] = opq_s(zb + zp[f / 4 + 2] * 14336);
        if constexpr (f == 31 || f == 33 || f == 35) rw[(f - 31) / 2] = opq_v(L_RED0 + ((((w + (f - 29) / 2) & 3) * 3 + (f - 31) / 2) * 2) * 1024 + l16);
        if constexpr (f == 37) rd = opq_v(L_RED0 + w * 6144 + l16);
        if constexpr (f == 24) vo = opq_v((g & 1) * 512 + c16 * 16);
        if constexpr (f == 26) vo = opq_v(vo + (g >> 1) * 256);
        if constexpr (f == 28) rv = opq_v(vo - (g & 1) * 504);
        if constexpr (q >= 0 && q < 2 && k == 20) { const v2u a = sw16(d[0], d[2]), e = sw16(d[1], d[3]); d[0] = a.x; d[2] = a.y; d[1] = e.x; d[3] = e.y; pin(d[0]); pin(d[1]); }
        if constexpr (q >= 0 && q < 2 && k == 21) st16(v4u{d[0], d[1], d[2], d[3]}, rws, vo + q * 1024, so);
        if constexpr (f == 39) BAR0();
        if constexpr (q >= 0 && k == 5) BAR();
        SB();
        if constexpr (s == 0) mma_c(acc[0][j], z, W[0][s], bias); else mma<false, true>(acc[0][j], z, W[0][s]);
        SB();
        fin(IC<u0 + per - 1>{});
        SB();
      });
#pragma unroll
      for (int e = 1; e < 4; e++) acc[0][5][e] = gadd(acc[0][5][e], acc[2][5][e]);
      const int rw2 = opq_v(L_RED2 + l16);
      asm volatile("s_nop 0" ::: "memory");
#pragma unroll
      for (int i = 1; i < 4; i++) pin(acc[i][6]);
      STAMP(sb + 15);
#pragma unroll
      for (int r = 1; r < 4; r++) *lp<f32x4>(rw2 + (((w + r) & 3) * 3 + r - 1) * 1024) = acc[r][6];
      st16(join16(pk4(acc[0][4] * z1[4]), pk4(acc[0][5] * z1[5])), rws, vo + 2048, so);
      BAR();
#pragma unroll
      for (int r = 1; r < 4; r++) acc[r][6] = ldsf4(rw2 + (w * 3 + r - 1) * 1024);
      pin(acc[0][6]);
      {
        const v2u dd = pk4(((acc[0][6] + acc[1][6]) + (acc[2][6] + acc[3][6])) * z1[6]);
        st16(join16(dd, dd), rws, seloob(vo, M_GEVEN) + 3072, cz < 6 ? so : OOB);
      }
      const int uc = 98304 + ((w * 9984) & ~1023);
      cp_run<0, 10>(dws, l16, WU_OFF + co * WU_T + uc, uc);
      const int fv = opq_v(seloob(l * FS, 0xFFull)), fo = opq_s(fl + F5 + pz * 8 * FS + cz * 8);
      WAITV(10);
      BAR();
      push(fv, fo);
      STAMP(sb + 16);
      touch(dws, l < (cz < 6 ? 28 : 24) ? l * 128 : OOB, so);
    }
    { LANE();
      f32x4 acc[2][5];
      const int vo = opq_v(L == 0 ? (g & 1) * 512 + (g >> 1) * 256 + c16 * 16 : c16 * 1280 + (g & 1) * 32 + (g >> 1) * 16);
      const int vo4 = opq_v(seloob(vo, M_GEVEN)), vs = opq_v(seloob(c16 * 8 + (g >> 1) * 4, M_GEVEN));
      const int xw = opq_v(L_X5 + (w ^ 1) * 5120 + l16), xr = opq_v(L_X5 + w * 5120 + l16);
      const int fl_ = LOCF ? co >> 2 : 0, sh6 = fl_ & kh;
      const int a0 = opq_s(act + A_G + (4 * po + w) * GT + kh * 7168), a1 = opq_s(act + A_G + (4 * po + (w ^ 1)) * GT + kh * 7168);
      const int dn = opq_s(fl_ * 14336), df = opq_s(14336 - fl_ * 14336 + sh6 * 1024), d6 = opq_s(sh6 ? 0 : fl_ * 14336 + 6144), d13 = opq_s((df + 6144) | (kh << 30));
      const int b0 = opq_v(l16 + kh * 5120), b1 = opq_v(b0 + 61440), b2 = opq_v(b0 + 122880), b3 = opq_v(l16 + 133120);
      const int qo = opq_s(qslot_off(w)), wq = opq_s((WQ_OFF + h * WQ_T + qo) | kill1);
      const int tile = opq_s(ACT_OFF + ACT_LAY + A_XIN + (4 * po + w) * XT);
      const int pv = opq_v(seloob(fl + F5 + (po * 8 + co) * FS + (kh * 2 + (l >> 1) * 2 + l) * 8, 0xFull));
      v4u A[2][14];
      gemm_m2k2<14, 5, 3, true, WINA, true, nv_u>([&](auto ti) AI { return ti.v < 6 ? b0 + ti.v * 10240 : ti.v < 12 ? b1 + (ti.v - 6) * 10240 : ti.v == 12 ? b2 : b3; }, xw, A, acc,
          [&]() AI {
#pragma unroll
        for (int j = 0; j < 5; j++) { acc[0][j] = unpk4(rr[j]); pin(acc[0][j]); }
        poll<PDLY>(rws, pv, l >= 4, tag_lo, tag_hi);
        STAMP(sb + 17);
      },
          [&](auto ii, auto ti) AI { constexpr int t = ti.v, u = t % 7; return ld16h<(u == 6 ? 0 : u & 3) * 1024>(dws, l16, (ii.v ? a1 : a0) + (t == 6 ? d6 : t == 13 ? d13 : (t < 7 ? dn : df) + (u >> 2) * 4096)); },
          [&](auto fi) AI {
        constexpr int f = fi.v;
        if constexpr (f == 21) BAR0();
        if constexpr (f == 43 || f == 51) cp_arm(qo + (f - 43) * 512);
        if constexpr (f == 55 || f == 59) cp_one<2 + (f - 55) / 4>(dws, l16, wq + 4096);
      },
          [&](auto fi) AI {
        constexpr int f = fi.v;
        if constexpr (f >= 44 && !(f & 1)) cp_one<(f - 44) / 2>(dws, l16, wq + (f - 44) / 8 * 4096);
      });
      STAMP(sb + 18);
      BAR();
      xchg_read<5>(xr, acc);
      if (L == 1) {
        const rsrc_t ro_ = mk(out, 2048 * 1280);
        v2u d[5];
#pragma unroll
        for (int j = 0; j < 5; j++) d[j] = pk4(acc[0][j]);
        const int so = (64 * po + 16 * w) * 1280 + co * 160;
        __builtin_amdgcn_raw_buffer_store_b128(join16(d[0], d[1]), ro_, vo, so, 0);
        __builtin_amdgcn_raw_buffer_store_b128(join16(d[2], d[3]), ro_, vo + 64, so, 0);
        __builtin_amdgcn_raw_buffer_store_b128(join16(d[4], d[4]), ro_, vo4 + 128, so, 0);
        flag(b == 0 ? 0 : OOB);
        __builtin_amdgcn_endpgm();
      }
      store_x(acc[0], rws, tile + co * 2560, tile + 20480 + co * 128, vo, vo4, vs);
      cp_run<0, 8>(dws, l16, wq + 30720, qo + 30720);
      cp_run<0, 4>(dws, l16, wq + 2 * 30720, qo + 2 * 30720);
      const int fv = opq_v(l < 16 ? l * 2 * FS + (po & 1) * 64 : (l < 32 && (po & 1) && po < 31) ? (l * 2 + 1) * FS : OOB), fo = opq_s(fl + F6 + (po >> 1) * 32 * FS + co * 8);
      WAITV(12);
      BAR();
      push(fv, fo);
      STAMP(sb + 19);
      touch(dws, l < 20 ? co * 2560 + l * 128 : l == 20 ? 20480 + co * 128 : OOB, tile);
      cp_run<4, 8>(dws, l16, wq + 2 * 30720, qo + 2 * 30720);
      cp_run<0, 4>(dws, l16, wq + 3 * 30720, qo + 3 * 30720);
    }
  }
}
}

extern "C" void kernel_launch(void* const* d_in, const int* in_sizes, int n_in, void* d_out, int out_size, void* d_ws, size_t ws_size, hipStream_t stream) {
  (void)hipFuncSetAttribute((const void*)fused_kernel, hipFuncAttributeMaxDynamicSharedMemorySize, LDS_BYTES);
  fused_kernel<<<256, 256, LDS_BYTES, stream>>>((const char*)d_in[0], (const char*)d_in[1], (const char*)d_in[2], (const char*)d_in[4],
                                                (const char*)d_in[5], (const char*)d_in[6], (const char*)d_in[7], (char*)d_out, (char*)d_ws);
}
```
